```python
import torch

def _softmax(x, axis=-1):
    return torch.softmax(x, dim=axis)
def _gelu(x):
    return torch.nn.functional.gelu(x, approximate="tanh")
def _silu(x):
    return torch.nn.functional.silu(x)
def _sigmoid(x):
    return torch.sigmoid(x)
def _layernorm(x, axis=-1, eps=1e-5):
    m = x.mean(dim=axis, keepdim=True)
    v = x.var(dim=axis, keepdim=True, unbiased=False)
    return (x - m) / torch.sqrt(v + eps)
def _scatter_add(tgt, idx, src, axis=0):
    return tgt.clone().index_add_(axis, idx, src)
def _segment_sum(src, seg, n):
    out = torch.zeros((n,) + src.shape[1:], dtype=src.dtype)
    return out.index_add_(0, seg, src)
def _pad(x, pw):
    flat = tuple(v for pair in reversed(pw) for v in pair)
    return torch.nn.functional.pad(x, flat)

kernel_name = 'standalone__mixer_channel_mlp__prompt'

def setup_inputs():

    _w = (lambda a: (lambda t: t.to(torch.bfloat16) if t.is_floating_point() else t)(torch.as_tensor(a)))
    B, S, D, F = 1, 2048, 576, 4608
    return {
        "x": _w((lambda *s: torch.randn(*s, dtype=torch.bfloat16) * 0.05)(B, S, D)),
        "w1": _w((lambda *s: torch.randn(*s, dtype=torch.bfloat16) * 0.8)(D, F) * (D ** -0.5)),
        "b1": _w((lambda *s: torch.randn(*s, dtype=torch.bfloat16) * 0.4)(F)),
        "w2": _w((lambda *s: torch.randn(*s, dtype=torch.bfloat16) * 0.05)(F, D) * (F ** -0.5)),
        "b2": _w((lambda *s: torch.randn(*s, dtype=torch.bfloat16) * 0.05)(D)),
    }

def reference(x, w1, b1, w2, b2):
    y = _layernorm(x, axis=-1)
    h = _gelu(torch.einsum("bsd,df->bsf", y, w1) + b1)
    return torch.einsum("bsf,fd->bsd", h, w2) + b2
```

```cpp
#include <hip/hip_runtime.h>
#include <hip/hip_bf16.h>
typedef __attribute__((ext_vector_type(8))) __bf16 bf16x8;
typedef __attribute__((ext_vector_type(4))) float f32x4;
constexpr int S_=2048, D_=576, F_=4608;
__device__ __forceinline__ float bf2f(unsigned short u){ return __uint_as_float(((unsigned)u)<<16); }
__device__ __forceinline__ unsigned short f2bf(float f){ __bf16 b=(__bf16)f; return __builtin_bit_cast(unsigned short,b); }

__global__ void ln_kernel(const unsigned short* x, unsigned short* y){
  int row=blockIdx.x; int lane=threadIdx.x;
  float v[9]; float s=0;
  for(int i=0;i<9;i++){ v[i]=bf2f(x[row*D_+i*64+lane]); s+=v[i]; }
  for(int o=32;o;o>>=1) s+=__shfl_xor(s,o);
  float m=s/D_; float q=0;
  for(int i=0;i<9;i++){ float d=v[i]-m; q+=d*d; }
  for(int o=32;o;o>>=1) q+=__shfl_xor(q,o);
  float r=rsqrtf(q/D_+1e-5f);
  for(int i=0;i<9;i++) y[row*D_+i*64+lane]=f2bf((v[i]-m)*r);
}
template<int K,int N,bool GELU>
__global__ __launch_bounds__(256) void gemm_kernel(const unsigned short* A,const unsigned short* B,const unsigned short* bias,unsigned short* C){
  __shared__ unsigned short Bt[64][40];
  int w=threadIdx.x>>6, lane=threadIdx.x&63;
  int m0=blockIdx.y*64+w*16, n0=blockIdx.x*64;
  f32x4 acc[4]={};
  for(int k0=0;k0<K;k0+=32){
    __syncthreads();
    for(int i=threadIdx.x;i<32*64;i+=256){ int k=i>>6,n=i&63; Bt[n][k]=B[(size_t)(k0+k)*N+n0+n]; }
    __syncthreads();
    bf16x8 a=*(const bf16x8*)(A+(size_t)(m0+(lane&15))*K+k0+8*(lane>>4));
    for(int j=0;j<4;j++){
      bf16x8 b=*(const bf16x8*)(&Bt[j*16+(lane&15)][8*(lane>>4)]);
      acc[j]=__builtin_amdgcn_mfma_f32_16x16x32_bf16(a,b,acc[j],0,0,0);
    }
  }
  for(int j=0;j<4;j++) for(int r=0;r<4;r++){
    int row=m0+(lane>>4)*4+r, col=n0+j*16+(lane&15);
    float v=acc[j][r]+bf2f(bias[col]);
    if(GELU){ float u=0.7978845608028654f*(v+0.044715f*v*v*v); v=v/(1.f+__expf(-2.f*u)); }
    C[(size_t)row*N+col]=f2bf(v);
  }
}
extern "C" void kernel_launch(void* const* d_in, const int* in_sizes, int n_in, void* d_out, int out_size, void* d_ws, size_t ws_size, hipStream_t stream){
  const unsigned short* x=(const unsigned short*)d_in[0];
  const unsigned short* w1=(const unsigned short*)d_in[1];
  const unsigned short* b1=(const unsigned short*)d_in[2];
  const unsigned short* w2=(const unsigned short*)d_in[3];
  const unsigned short* b2=(const unsigned short*)d_in[4];
  unsigned short* y=(unsigned short*)d_ws; unsigned short* h=y+S_*D_;
  ln_kernel<<<S_,64,0,stream>>>(x,y);
  gemm_kernel<D_,F_,true><<<dim3(F_/64,S_/64),256,0,stream>>>(y,w1,b1,h);
  gemm_kernel<F_,D_,false><<<dim3(D_/64,S_/64),256,0,stream>>>(h,w2,b2,(unsigned short*)d_out);
}
```

```cpp
#include <hip/hip_runtime.h>
#include <stdint.h>
#include <utility>

typedef __bf16 bf16x8 __attribute__((ext_vector_type(8)));
typedef __bf16 bf16x2 __attribute__((ext_vector_type(2)));
typedef short s16x4 __attribute__((ext_vector_type(4)));
typedef float f32x16 __attribute__((ext_vector_type(16)));
typedef float f32x4 __attribute__((ext_vector_type(4)));
typedef float f32x2 __attribute__((ext_vector_type(2)));
typedef unsigned u32x4 __attribute__((ext_vector_type(4)));
typedef unsigned u32x2 __attribute__((ext_vector_type(2)));
typedef __amdgpu_buffer_rsrc_t srd_t;
#define DI __device__ __forceinline__
#define LAS __attribute__((address_space(3)))
#define LDSP(T, a) ((LAS T*)(size_t)(a))

constexpr unsigned S_ = 2048, D_ = 576, F_ = 4608;
constexpr unsigned XROW = D_ * 2, W1ROW = F_ * 2, W2ROW = D_ * 2;
constexpr unsigned SLOT = 36864, RING = 4 * SLOT, SLICE = 28672;
constexpr unsigned L_B1 = RING, L_CSP = L_B1 + 1152, L_CS8 = L_CSP + 2048, L_CVEC = L_CS8 + 512, L_STAT = L_CVEC + 1024;
constexpr unsigned L_PAT = L_STAT + 1024, L_EX = L_PAT + 2304, LDS_BYTES = L_EX + 8192;
static_assert(LDS_BYTES <= 163840, "LDS");
constexpr unsigned WS_FLAG = 0, WS_EPOCH = 32768, WS_H = 65536, H_BYTES = S_ * F_ * 2;
constexpr unsigned WS_P = WS_H + H_BYTES, P_BYTES = 16 * 4 * 4 * 4 * 9216;
constexpr unsigned WS_USED = WS_P + P_BYTES;
constexpr float KAP = 0.4686686930532063f, GAM = 4.91222953926424f, RKAP = 2.1337034344781243f;
#define KD 9
#define STAMP(i) do {} while (0)

DI unsigned rfl(unsigned x) { return __builtin_amdgcn_readfirstlane(x); }
DI s16x4 trr(unsigned a) { return __builtin_amdgcn_ds_read_tr16_b64_v4i16(LDSP(s16x4, a)); }
DI bf16x8 cat(s16x4 lo, s16x4 hi) { return __builtin_bit_cast(bf16x8, __builtin_shufflevector(lo, hi, 0, 1, 2, 3, 4, 5, 6, 7)); }
DI f32x4 ldsf4(unsigned a) { return *LDSP(f32x4, a); }
DI unsigned cvtpk(float lo, float hi) { f32x2 v = {lo, hi}; return __builtin_bit_cast(unsigned, __builtin_convertvector(v, bf16x2)); }
template <int CTRL> DI float dpp_add(float v) {
  return v + __builtin_bit_cast(float, __builtin_amdgcn_update_dpp(0, __builtin_bit_cast(int, v), CTRL, 0xf, 0xf, false));
}
#define MFMA32(a, b, c) __builtin_amdgcn_mfma_f32_32x32x16_bf16((a), (b), (c), 0, 0, 0)
#define MFMA16(a, b, c) __builtin_amdgcn_mfma_f32_16x16x32_bf16((a), (b), (c), 0, 0, 0)
#define BARRIER_LGKM() asm volatile("s_waitcnt lgkmcnt(0)\n\ts_barrier" ::: "memory")
#define GAP() __builtin_amdgcn_sched_barrier(0)
#define PIN(x) asm volatile("" : "+v"(x))

struct Ctx {
  srd_t sx, s1, s2, sw;
  unsigned w, t, hf, r, j, q;
  unsigned freg2, f82;
  unsigned tag, sdma, sw1, ldst;
  u32x4 vpat;
  unsigned vpat4;
  unsigned lw, lcs;
  unsigned wb, cb;
  unsigned vhst, vhld, tb, tc;
  float rs, nm;
  bf16x8 sel, csf;
  f32x4 csa;
  float cp[8];
  f32x4 gb, gs;
  float gv[2], gz[2], gm;
  unsigned hv[4];
  bf16x8 X[36];
  bf16x8 F[4];
  f32x16 acc[2];
  float a8[8];
  f32x4 acc2[2][9];
  u32x4 Hb[4][2];
  u32x2 pollv, polld;
};
DI unsigned lane_id() {
  unsigned l;
  asm volatile("v_mbcnt_lo_u32_b32 %0, -1, 0\n\tv_mbcnt_hi_u32_b32 %0, -1, %0" : "=v"(l));
  return l;
}
DI unsigned opq(unsigned v) { PIN(v); return v; }

constexpr unsigned unit_of(int hf, int n) {
  if (n < 2) return 0u | (unsigned)(2 * hf + n) << 2;
  if (n == 13) return hf ? (1u | 6u << 2) : (0u | 8u << 2);
  if (n == 5 || n == 9) return 0u | (unsigned)(n / 2 + 2 + hf) << 2;
  const int i = n < 13 ? 6 * ((n - 2) / 4) + 2 * ((n - 2) % 4) + hf : 18 + 1 + 2 * (n - 14) + hf;
  return (unsigned)(i % 3 + 1) | (unsigned)(i / 3) << 2;
}
constexpr unsigned slot_pack(int h) {
  return unit_of(0, 2 * (h - 6)) | unit_of(0, 2 * (h - 6) + 1) << 8 | unit_of(1, 2 * (h - 6)) << 16 | unit_of(1, 2 * (h - 6) + 1) << 24;
}
constexpr bool is_pub(int h) { return h == 2 || h == 4 || h == 6 || h == 8 || h == 10; }
constexpr int kmid(int h) { return is_pub(h) ? KD : 9; }
constexpr int hload_at(int h, int k) {
  if (h == 5 && k == -1) return 0;
  if (h < 5 || h > 13) return -1;
  int n = -1;
  if (k == (is_pub(h) ? KD : 0)) n = 2 * (h - 6) + 3;
  if (k == (is_pub(h) ? KD + 1 : 9)) n = 2 * (h - 6) + 4;
  return n <= 17 ? n : -1;
}
constexpr int dma_first(int h) { return is_pub(h) ? KD + 1 : 1; }
constexpr int dma_lo(int h, int k) {
  const int f = dma_first(h), room = 16 - f;
  if (k < f) return 0;
  if (room >= 9) return (k - f + 1) / 2 > 5 ? 5 : (k - f + 1) / 2;
  const int v = ((k - f) * 5 + room - 1) / room;
  return v > 5 ? 5 : v;
}
constexpr int dma_cnt(int h, int k) { return h + 3 > 14 || h == 0 ? 0 : dma_lo(h, k + 1) - dma_lo(h, k); }
constexpr bool poll_at(int h, int k) { return (k == 1 && (h == 4 || h == 6 || h == 8 || h == 10)) || (k == -2 && (h == 11 || h == 12)); }
constexpr bool hst_at(int h, int k) { return (h < 8 && (h & 1) && (k == 0 || k == 15)) || (h == 9 && k == 15); }
constexpr int nops(int h, int k, int n) {
  int s = (poll_at(h, k) ? 1 : 0) + (hload_at(h, k) >= 0 ? 2 : 0) + (hst_at(h, k) ? 1 : 0);
  if (h == 0 && k == -2) s += 2 * n;
  const int lo = dma_lo(h, k), cnt = dma_cnt(h, k);
  for (int i = lo; i < lo + cnt; ++i) s += (i < n) ? 1 : 0;
  return s;
}
constexpr int between(int h0, int k0, int h1, int k1, int n) {
  int s = 0;
  for (int h = h0; h <= h1; ++h)
    for (int k = -2; k < 16; ++k)
      if ((h > h0 || k > k0) && (h < h1 || k < k1)) s += nops(h, k, n);
  return s;
}
constexpr int cmin(int a, int b) { return a < b ? a : b; }

DI unsigned fstart(unsigned qq, unsigned ch) {
  const unsigned odd = (qq & 1u) * 32u;
  return 288u * qq + (ch < 8u ? odd + 32u * ch : 256u - 8u * odd);
}
DI unsigned hunit(const Ctx& c, unsigned qq, unsigned ch) { return WS_H + ((((c.r * 4u + c.j) * 4u + qq) * 9u + ch) * 4u + c.t) * 2048u; }

DI void dma16(unsigned m0v, unsigned voff, srd_t srd, unsigned soff) {
  asm volatile("s_mov_b32 m0, %0\n\ts_nop 0\n\tbuffer_load_dwordx4 %1, %2, %3 offen lds" ::"s"(m0v), "v"(voff), "s"(srd), "s"(soff));
}
template <int HS, int I> DI void issue_piece(Ctx& c) {
  const srd_t sd = HS < 6 ? c.s1 : c.s2;
  if constexpr (I == 0) {
    if constexpr (HS < 6) c.sdma = c.sw1 + ((HS & 1) * 288u * W1ROW + ((HS >> 1) + 1) * 128u);
    else {
      const unsigned u = (slot_pack(HS) >> (8u * c.t)) & 0xffu;
      c.sdma = (1152u * c.j + fstart((c.q + (u & 3u)) & 3u, u >> 2)) * W2ROW + 288u * c.q;
    }
    dma16((HS & 3) * SLOT + c.ldst, c.vpat[0], sd, c.sdma);
  } else if constexpr (I < 4) {
    asm volatile("buffer_load_dwordx4 %0, %1, %2 offen offset:%3 lds" ::"v"(c.vpat[I]), "s"(sd), "s"(c.sdma), "i"(1024 * I));
  } else {
    asm volatile("s_cmp_lg_u32 %4, 0\n\ts_cbranch_scc1 .Lskip%=\n\ts_mov_b32 m0, %0\n\ts_nop 0\n\tbuffer_load_dwordx4 %1, %2, %3 offen lds\n.Lskip%=:"
                 ::"s"((HS & 3) * SLOT + 4096u + c.ldst), "v"(c.vpat4), "s"(sd), "s"(c.sdma), "s"(c.hf) : "scc");
  }
}
template <int HS> DI void issue_slot(Ctx& c) {
  issue_piece<HS, 0>(c); issue_piece<HS, 1>(c); issue_piece<HS, 2>(c); issue_piece<HS, 3>(c); issue_piece<HS, 4>(c);
}
template <int N> DI void issue_hload(Ctx& c) {
  const unsigned u = c.hf ? unit_of(1, N) : unit_of(0, N);
  const unsigned so = hunit(c, (c.q + (u & 3u)) & 3u, u >> 2);
  asm volatile("buffer_load_dwordx4 %0, %1, %2, %3 offen sc1" : "=v"(c.Hb[N & 3][0]) : "v"(c.vhld), "s"(c.sw), "s"(so));
  asm volatile("buffer_load_dwordx4 %0, %1, %2, %3 offen offset:1024 sc1" : "=v"(c.Hb[N & 3][1]) : "v"(c.vhld), "s"(c.sw), "s"(so));
}
DI unsigned poll_off(const Ctx& c, bool dest) {
  const unsigned l = lane_id(), dp = (l & 3u) ? (l & 3u) : 1u;
  return WS_FLAG + ((dest ? (c.r * 4u + ((c.j + dp) & 3u)) * 4u + c.q : (c.r * 4u + c.j) * 4u + ((c.q + dp) & 3u)) << 7);
}
DI void store_flag(Ctx& c, unsigned count) {
  u32x2 v = {count, c.tag};
  const unsigned off = WS_FLAG + (((c.r * 4u + c.j) * 4u + c.q) << 7);
  unsigned long long sv;
  asm volatile("s_cmp_lg_u32 %4, 0\n\ts_cbranch_scc1 .Lskip%=\n\ts_mov_b64 %0, exec\n\ts_mov_b64 exec, 1\n\t"
               "buffer_store_dwordx2 %1, off, %2, %3 sc1\n\ts_mov_b64 exec, %0\n.Lskip%=:"
               : "=&s"(sv) : "v"(v), "s"(c.sw), "s"(off), "s"(c.w) : "scc");
}
DI void check_poll(Ctx& c, u32x2& pv, bool dest, unsigned need) {
  PIN(pv);
  const bool ok = pv[1] == c.tag && pv[0] - need <= 5u - need;
  if (__builtin_expect(!__all(ok), 0)) {
    const unsigned voff = poll_off(c, dest);
    for (unsigned spin = 0; spin < (1u << 16); ++spin) {
      u32x2 v;
      asm volatile("s_sleep 1\n\tbuffer_load_dwordx2 %0, %1, %2, 0 offen sc1\n\ts_waitcnt vmcnt(0)" : "=&v"(v) : "v"(voff), "s"(c.sw) : "memory");
      if (__all(v[1] == c.tag && v[0] - need <= 5u - need)) break;
    }
  }
}
DI void store_h(Ctx& c, unsigned ch, unsigned piece) {
  const u32x4 v = {c.hv[0], c.hv[1], c.hv[2], c.hv[3]};
  const unsigned so = hunit(c, c.q, ch) + piece * 512u;
  asm volatile("buffer_store_dwordx4 %0, %1, %2, %3 offen sc1\n\ts_nop 1" ::"v"(v), "v"(c.vhst), "s"(c.sw), "s"(so));
}
template <int N0, int N1> DI void wait_vm(const Ctx& c) {
  static_assert(N1 <= N0 && N1 >= 0 && N0 < 64, "vmcnt");
  if constexpr (N0 == N1) asm volatile("s_waitcnt vmcnt(%0)" ::"i"(N0) : "memory");
  else asm volatile("s_waitcnt vmcnt(%0)\n\ts_cmp_eq_u32 %2, 0\n\ts_cbranch_scc1 .Lskip%=\n\ts_waitcnt vmcnt(%1)\n.Lskip%=:" ::"i"(N0), "i"(N1), "s"(c.hf) : "memory", "scc");
}

DI void cs_mm(Ctx& c, bool first) { f32x4 z = {}; c.csa = MFMA16(c.sel, c.csf, first ? z : c.csa); }
DI void cs_publish(Ctx& c) {
  f32x4 v;
#pragma unroll
  for (int i = 0; i < 4; ++i) v[i] = dpp_add<0x128>(c.csa[i]);
  const unsigned l = lane_id();
  *LDSP(f32x4, L_CSP + c.w * 256u + (l & 7u) * 32u + (l & 16u)) = v;
}

template <int H> DI void set_bases(Ctx& c) {
  if constexpr (H == 6) {
    const unsigned l = lane_id(), g = l >> 4;
    c.lw = (16u * (g >> 1) + 4u * (g & 1u) + ((l >> 2) & 3u)) * 288u + 8u * (l & 3u) + c.hf * 18432u;
  }
  c.wb = opq((H & 3) * SLOT + c.lw);
  if constexpr (H < 6) c.cb = opq((H & 3) * SLOT + c.lcs);
}
template <int H, int S> DI void rd(Ctx& c) {
  constexpr unsigned o = H < 6 ? 2048u * S : 9216u * (S / 9) + 32u * (S % 9), e = H < 6 ? 512u : 2304u;
  c.F[(H * 18 + S) & 3] = cat(trr(c.wb + o), trr(c.wb + o + e));
}
template <int H, int S> DI void mm(Ctx& c) {
  const bf16x8 a = c.F[(H * 18 + S) & 3];
  if constexpr (H < 6) {
    constexpr int B = ((H >> 1) + 1) & 1;
    f32x16 z = {};
    c.acc[B] = MFMA32(a, c.X[18 * (H & 1) + S], ((H & 1) == 0 && S == 0) ? z : c.acc[B]);
  } else {
    constexpr int N = 2 * (H - 6) + S / 9, dt = S % 9;
    f32x4 z = {};
    c.acc2[0][dt] = MFMA16(a, __builtin_bit_cast(bf16x8, c.Hb[N & 3][0]), N == 0 ? z : c.acc2[0][dt]);
    c.acc2[1][dt] = MFMA16(a, __builtin_bit_cast(bf16x8, c.Hb[N & 3][1]), N == 0 ? z : c.acc2[1][dt]);
  }
}
template <int E, int ST> DI void gelu_stage(Ctx& c, float a) {
  constexpr int p = E & 1;
  if constexpr (ST == 0) {
    c.gv[p] = __builtin_fmaf(c.rs, a, __builtin_fmaf(c.nm, c.gs[E & 3], c.gb[E & 3]));
    c.gz[p] = __builtin_fmaf(c.gv[p], c.gv[p], GAM);
    PIN(c.gv[p]); PIN(c.gz[p]);
  }
  if constexpr (ST == 1) { c.gz[p] = __builtin_amdgcn_exp2f(-(c.gv[p] * c.gz[p])); PIN(c.gz[p]); }
  if constexpr (ST == 2) { c.gz[p] = __builtin_amdgcn_rcpf(1.0f + c.gz[p]); PIN(c.gz[p]); }
  if constexpr (ST == 3) {
    if constexpr (p == 0) { c.gm = c.gv[0] * c.gz[0]; PIN(c.gm); }
    else { float m = c.gv[1] * c.gz[1], m0 = c.gm; PIN(m); PIN(m0); c.hv[(E >> 1) & 3] = cvtpk(m0, m); PIN(c.hv[(E >> 1) & 3]); }
  }
}
template <int B, int G> DI void gelu_gap(Ctx& c) {
  if constexpr (G >= 2 && G - 2 < 32) gelu_stage<((G - 2) >> 1), 2 + (G & 1)>(c, 0.f);
  if constexpr (G >= 0 && G < 32) gelu_stage<(G >> 1), (G & 1)>(c, c.acc[B & 1][G >> 1]);
  if constexpr (G == -1 || G == 6 || G == 14 || G == 22) {
    constexpr int qd = (G + 2) / 8;
    c.gb = ldsf4(c.tb + B * 256u + (qd >> 1) * 64u + (qd & 1) * 16u);
    c.gs = ldsf4(c.tc + (qd >> 1) * 64u + (qd & 1) * 16u);
  }
}
template <int G> DI void gelu8_gap(Ctx& c) {
  if constexpr (G == -1 || G == 15) {
    c.gb = ldsf4(c.tb + (G == 15 ? 16u : 0u));
    c.gs = ldsf4(c.tc + (G == 15 ? 16u : 0u));
  }
  if constexpr (G >= 0 && G < 32) gelu_stage<(G >> 2), (G & 3)>(c, c.a8[G >> 2]);
}

template <int H, int K> DI void side(Ctx& c) {
  if constexpr (is_pub(H) && K == KD) {
    asm volatile("s_waitcnt vmcnt(0)" ::: "memory");
    if constexpr (H >= 4) check_poll(c, c.pollv, false, (H - 2) / 2);
  }
  if constexpr (H == 12 && K == 9) {
    wait_vm<between(12, -2, 12, 9, 5), between(12, -2, 12, 9, 4)>(c);
    check_poll(c, c.pollv, false, 5);
  }
  if constexpr (poll_at(H, K)) {
    if constexpr (H == 11) asm volatile("buffer_load_dwordx2 %0, %1, %2, 0 offen sc1" : "=v"(c.polld) : "v"(poll_off(c, true)), "s"(c.sw));
    else asm volatile("buffer_load_dwordx2 %0, %1, %2, 0 offen sc1" : "=v"(c.pollv) : "v"(poll_off(c, false)), "s"(c.sw));
  }
  if constexpr (K == -2 && (H & 1) && H >= 3 && H <= 11) store_flag(c, (H - 1) / 2);
  if constexpr (H == 5 && K == -1) c.vhld = lane_id() * 16u;
  if constexpr (hload_at(H, K) >= 0) issue_hload<hload_at(H, K)>(c);
  if constexpr (H == 0 && K == -2) { issue_slot<2>(c); issue_slot<3>(c); }
  if constexpr (H == 3 && K == 0) {
    const unsigned l = lane_id();
#pragma unroll
    for (int i = 0; i < 4; ++i) c.vpat[i] = *LDSP(unsigned, L_PAT + (c.hf * 5u + i) * 256u + l * 4u);
    c.vpat4 = *LDSP(unsigned, L_PAT + 1024u + l * 4u);
  }
  if constexpr (dma_cnt(H, K) >= 1) issue_piece<H + 3, dma_lo(H, K)>(c);
  if constexpr (dma_cnt(H, K) >= 2) issue_piece<H + 3, dma_lo(H, K) + 1>(c);
  static_assert(dma_cnt(H, K) <= 2, "dma");
  if constexpr ((H < 8 && (H & 1) == 0) || H == 8) {
    if constexpr (K == -2) {
      const unsigned l = lane_id();
      if constexpr (H < 8) {
        const unsigned a = L_CSP + c.hf * 128u + (l & 31u) * 4u;
#pragma unroll
        for (int i = 0; i < 8; ++i) c.cp[i] = *LDSP(float, a + i * 256u);
      } else {
        const unsigned a = L_CS8 + (l & 31u) * 4u;
#pragma unroll
        for (int i = 0; i < 4; ++i) c.cp[i] = *LDSP(float, a + i * 128u);
        c.tb = c.tb + 1024u - c.hf * 64u;
        if (c.hf) {
          const f32x4 e0 = ldsf4(L_EX + c.t * 2048u + l * 16u), e1 = ldsf4(L_EX + c.t * 2048u + 1024u + l * 16u);
#pragma unroll
          for (int i = 0; i < 4; ++i) { c.a8[i] = e0[i]; c.a8[4 + i] = e1[i]; }
        }
      }
    }
    if constexpr (K == -1) {
      float s0 = c.cp[0] + c.cp[1], s1 = c.cp[2] + c.cp[3];
      PIN(s0);
      if constexpr (H < 8) { float s2 = c.cp[4] + c.cp[5]; PIN(s2); float s3 = c.cp[6] + c.cp[7]; s0 += s2; PIN(s0); s1 += s3; }
      *LDSP(float, L_CVEC + c.w * 128u + (lane_id() & 31u) * 4u) = s0 + s1;
      if constexpr (H == 8) c.tc += c.hf * 64u;
    }
  }
  if constexpr (H < 8) gelu_gap<(H >> 1), K + 18 * (H & 1)>(c);
  if constexpr (H == 8 || H == 9) gelu8_gap<K + 18 * (H & 1)>(c);
  if constexpr (hst_at(H, K)) {
    if constexpr (H < 8) store_h(c, 2u * (H >> 1) + c.hf, K == 0 ? 0u : 1u);
    else store_h(c, 8u, c.hf);
  }
  if constexpr (H < 6) {
    if constexpr (K == 2 || K == 5 || K == 8 || K == 11) cs_mm(c, K == 2 && (H & 1) == 0);
    if constexpr (K == 13) { if (c.hf == 0) cs_mm(c, false); }
    if constexpr (K == 0 || K == 3 || K == 6 || K == 9) c.csf = *LDSP(bf16x8, c.cb + 1024u * (K / 3));
    if constexpr (K == 11) c.csf = *LDSP(bf16x8, c.cb + 4096u);
    if constexpr (K == 15 && (H & 1)) cs_publish(c);
  }
}
template <int H> DI void end_hs(Ctx& c) {
  if constexpr (!is_pub(H) && H < 14) {
    constexpr int hp = H - 1, kp = H >= 6 ? (is_pub(hp) ? KD + 1 : 9) : 0;
    if constexpr (H == 0) wait_vm<between(0, -3, 0, 16, 5), between(0, -3, 0, 16, 4)>(c);
    else if constexpr (H == 1) wait_vm<5 + between(0, -2, 1, 16, 5), 4 + between(0, -2, 1, 16, 4)>(c);
    else if constexpr (H == 5) wait_vm<between(5, -1, 5, 16, 5), between(5, -1, 5, 16, 4)>(c);
    else if constexpr (H >= 7) wait_vm<cmin(between(hp, kp, H, 16, 5), between(H - 2, 15, H, 16, 5)), cmin(between(hp, kp, H, 16, 4), between(H - 2, 15, H, 16, 4))>(c);
  }
  if constexpr (H < 14) BARRIER_LGKM();
  if constexpr (H >= 5 && H < 14) { PIN(c.Hb[(2 * (H - 5)) & 3][0]); PIN(c.Hb[(2 * (H - 5)) & 3][1]); }
  GAP();
}
template <int H> DI void mid_wait(Ctx& c) {
  if constexpr (H >= 6) {
    constexpr int hp = H - 1, kp = is_pub(hp) ? KD : 0;
    if constexpr (!(is_pub(H) && KD < 9)) wait_vm<between(hp, kp, H, 9, 5), between(hp, kp, H, 9, 4)>(c);
    PIN(c.Hb[(2 * (H - 6) + 1) & 3][0]); PIN(c.Hb[(2 * (H - 6) + 1) & 3][1]);
  }
}
template <int H, int K> DI void step(Ctx& c) {
  if constexpr (K == 9) mid_wait<H>(c);
  mm<H, K>(c);
  if constexpr (K + 4 < 18) rd<H, K + 4>(c);
  side<H, K>(c);
  GAP();
}
template <int H, int... Ks> DI void steps(Ctx& c, std::integer_sequence<int, Ks...>) { (step<H, Ks>(c), ...); }
template <int H> DI void half_stage(Ctx& c) {
  set_bases<H>(c);
  rd<H, 0>(c); rd<H, 1>(c);
  GAP();
  if constexpr (H > 0) mm<H - 1, 16>(c);
  rd<H, 2>(c);
  side<H, -2>(c);
  GAP();
  if constexpr (H > 0) mm<H - 1, 17>(c);
  rd<H, 3>(c);
  side<H, -1>(c);
  GAP();
  steps<H>(c, std::make_integer_sequence<int, 16>{});
  if constexpr (H == 14) { mm<14, 16>(c); mm<14, 17>(c); }
  end_hs<H>(c);
}

struct B0 {
  unsigned vx, vw1, vc8, xa[4], l8, lc8;
  bf16x8 G[4], selx, c8f;
  f32x16 accB;
  f32x4 accM;
};
template <int HF, int S> DI void issue_slice(Ctx& c, B0& b) {
  const unsigned dst = (S % 5) * SLICE, kk = S * 64u;
  dma16(dst + c.w * 1024u, b.vx, c.sx, c.r * 128u * XROW + kk * 2u);
  dma16(dst + c.w * 1024u + 8192u, b.vx, c.sx, (c.r * 128u + 64u) * XROW + kk * 2u);
  dma16(dst + c.w * 1024u + 16384u, b.vw1, c.s1, (kk + 8u * c.w) * W1ROW + c.freg2);
  if constexpr (HF == 0) dma16(dst + c.t * 1024u + 24576u, b.vc8, c.s1, (kk + 16u * c.t) * W1ROW + c.f82);
}
template <int HF, int S, int K> DI void rd0(Ctx& c, B0& b) {
  constexpr unsigned so = (S & 1) ? 0u : 0u;
  c.X[S * 4 + K] = *LDSP(bf16x8, b.xa[K] + so);
  c.F[K] = cat(trr(c.wb + 2048u * K), trr(c.wb + 2048u * K + 512u));
  if constexpr (HF == 0) b.G[K] = cat(trr(b.l8 + 1024u * K), trr(b.l8 + 1024u * K + 256u));
}
template <int HF, int S, int K> DI void mm0(Ctx& c, B0& b) {
  constexpr bool first = S == 0 && K == 0;
  const bf16x8 xx = c.X[S * 4 + K];
  f32x16 z = {}; f32x4 z4 = {};
  c.acc[0] = MFMA32(c.F[K], xx, first ? z : c.acc[0]);
  b.accB = MFMA32(HF == 0 ? b.G[K] : xx, xx, first ? z : b.accB);
  if constexpr (HF == 1) b.accM = MFMA16(b.selx, xx, first ? z4 : b.accM);
}
template <int HF, int S> DI void slice(Ctx& c, B0& b, const unsigned lx, const unsigned l8, const unsigned lc8) {
  constexpr int n0 = HF ? 3 : 4, n = HF ? 4 : 5, M = 5;
  constexpr int N = S == 0 ? 4 * n0 + M : S <= 4 ? 3 * n0 + M : S <= 7 ? (8 - S) * n0 : n;
  if constexpr (S == 0) STAMP(1);
  asm volatile("s_waitcnt vmcnt(%0) lgkmcnt(0)\n\ts_barrier" ::"i"(N) : "memory");
  STAMP(2 + S);
  GAP();
  constexpr unsigned base = (S % 5) * SLICE;
#pragma unroll
  for (int k = 0; k < 4; ++k) b.xa[k] = opq(base + (lx ^ (unsigned)(k << 5)));
  c.wb = opq(base + 16384u + c.lw);
  if constexpr (HF == 0) b.l8 = opq(base + 24576u + l8);
  rd0<HF, S, 0>(c, b); rd0<HF, S, 1>(c, b);
  GAP();
  if constexpr (S > 0) mm0<HF, S - 1, 2>(c, b);
  if constexpr (S >= 1 && S <= 4) issue_slice<HF, S + 4>(c, b);
  if constexpr (S == 7) issue_slot<0>(c);
  if constexpr (S == 8) issue_slot<1>(c);
  GAP();
  if constexpr (S > 0) mm0<HF, S - 1, 3>(c, b);
  rd0<HF, S, 2>(c, b); rd0<HF, S, 3>(c, b);
  c.csf = *LDSP(bf16x8, base + 16384u + c.w * 1024u + (c.lcs & 1023u));
  if constexpr (HF == 0) b.c8f = *LDSP(bf16x8, base + 24576u + lc8);
  GAP();
  mm0<HF, S, 0>(c, b);
  GAP();
  mm0<HF, S, 1>(c, b);
  cs_mm(c, S == 0);
  if constexpr (HF == 0) { f32x4 z = {}; b.accM = MFMA16(c.sel, b.c8f, S == 0 ? z : b.accM); }
  GAP();
}
template <int HF> DI void block0(Ctx& c, const srd_t sb1) {
  B0 b;
  const unsigned l = threadIdx.x & 63u, g = l >> 4, qq = (l >> 2) & 3u, pp = l & 3u, h = l >> 5;
  b.vw1 = (l >> 3) * W1ROW + (((l & 7u) ^ ((g & 1u) << 2)) << 4);
  b.vx = ((l >> 3) + 8u * c.w) * XROW + (((l & 7u) ^ ((g + 4u * c.w) & 7u)) << 4);
  b.vc8 = (l >> 2) * W1ROW + (pp << 4);
  issue_slice<HF, 0>(c, b); issue_slice<HF, 1>(c, b); issue_slice<HF, 2>(c, b); issue_slice<HF, 3>(c, b); issue_slice<HF, 4>(c, b);
  unsigned b1raw, epoch;
  {
    const unsigned tid = threadIdx.x < 288u ? threadIdx.x : 287u;
    const unsigned off = c.freg2 + tid * 2u + (tid >> 8) * (c.f82 - c.freg2 - 512u);
    asm volatile("buffer_load_ushort %0, %1, %2, 0 offen" : "=v"(b1raw) : "v"(off), "s"(sb1));
    asm volatile("buffer_load_dword %0, off, %1, %2 sc1" : "=v"(epoch) : "s"(c.sw), "s"(WS_EPOCH + c.r * 128u));
    const u32x4 ones = {~0u, ~0u, ~0u, ~0u};
    c.vhld = l * 16u;
#pragma unroll
    for (int k = 0; k < 3; ++k) {
      const unsigned so = WS_P + (((c.r * 4u + c.q) * 4u + c.j) * 4u + ((c.j + 1u + k) & 3u)) * 9216u;
      asm volatile("buffer_store_dwordx4 %0, %1, %2, %3 offen sc1" ::"v"(ones), "v"(c.vhld), "s"(c.sw), "s"(so + c.w * 1024u));
      if (c.w == (unsigned)k) asm volatile("buffer_store_dwordx4 %0, %1, %2, %3 offen sc1" ::"v"(ones), "v"(c.vhld), "s"(c.sw), "s"(so + 8192u));
    }
    asm volatile("s_nop 1");
  }
#pragma unroll
  for (int i = 0; i < 4; ++i) c.vpat[i] = b.vw1 + i * (8u * W1ROW - 1024u);
  c.vpat4 = b.vw1 + 32u * W1ROW;
  c.lw = (8u * h + qq) * 128u + (((4u * HF + 2u * (g & 1u) + (pp & 1u)) ^ (((qq >> 1) & 1u) << 2)) << 4) + 8u * (pp >> 1);
  c.lcs = c.t * 9216u + HF * 5120u + (l >> 3) * 128u + (((l & 7u) ^ ((g & 1u) << 2)) << 4);
  const unsigned xrow = 32u * c.t + (l & 31u);
  const unsigned lx = xrow * 128u + ((h ^ ((xrow >> 1) & 7u)) << 4);
  const unsigned l8 = (8u * h + qq) * 64u + 32u * (g & 1u) + 16u * (pp & 1u) + 8u * (pp >> 1), lc8 = c.t * 1024u + l * 16u;
  {
    u32x4 s;
#pragma unroll
    for (int i = 0; i < 4; ++i) s[i] = ((l & 7u) >> 1) == (unsigned)i ? (0x3f80u << (16u * (l & 1u))) : 0u;
    c.sel = __builtin_bit_cast(bf16x8, s);
    const unsigned o = (l & 15u) == (g & 1u) ? 0x3f803f80u : 0u;
    b.selx = __builtin_bit_cast(bf16x8, (u32x4){o, o, o, o});
  }
#pragma unroll
  for (int k = 0; k < 2; ++k) {
    const unsigned i = k ? 8u : c.w, n = 64u * i + l, rho = n / 18u, chn = n - rho * 18u;
    const unsigned f = (rho & 19u) | ((rho & 4u) << 1) | ((rho & 8u) >> 1);
    const unsigned rel = i < 5u ? i : i - 5u;
    *LDSP(unsigned, L_PAT + i * 256u + l * 4u) = f * W2ROW + chn * 16u - (rel < 4u ? rel * 1024u : 0u);
  }
  slice<HF, 0>(c, b, lx, l8, lc8); slice<HF, 1>(c, b, lx, l8, lc8); slice<HF, 2>(c, b, lx, l8, lc8);
  slice<HF, 3>(c, b, lx, l8, lc8); slice<HF, 4>(c, b, lx, l8, lc8); slice<HF, 5>(c, b, lx, l8, lc8);
  PIN(b1raw); PIN(epoch);
  if (threadIdx.x < 288u) *LDSP(float, L_B1 + threadIdx.x * 4u) = __uint_as_float(b1raw << 16) * KAP;
  c.tag = rfl(epoch) + 1u;
  slice<HF, 6>(c, b, lx, l8, lc8); slice<HF, 7>(c, b, lx, l8, lc8); slice<HF, 8>(c, b, lx, l8, lc8);
  mm0<HF, 8, 2>(c, b); mm0<HF, 8, 3>(c, b);
  cs_publish(c);
  const unsigned ll = lane_id();
  if constexpr (HF == 0) {
    f32x4 v, e0, e1;
#pragma unroll
    for (int i = 0; i < 4; ++i) { v[i] = dpp_add<0x124>(dpp_add<0x128>(b.accM[i])); e0[i] = b.accB[8 + i]; e1[i] = b.accB[12 + i]; }
    *LDSP(f32x4, L_CS8 + c.t * 128u + (ll & 3u) * 32u + (ll & 16u)) = v;
    *LDSP(f32x4, L_EX + c.t * 2048u + ll * 16u) = e0;
    *LDSP(f32x4, L_EX + c.t * 2048u + 1024u + ll * 16u) = e1;
#pragma unroll
    for (int i = 0; i < 8; ++i) c.a8[i] = b.accB[i];
  } else {
    const unsigned cc = ll & 31u, sl = (cc & 3u) + 4u * (cc >> 3);
    float d = b.accB[0];
#pragma unroll
    for (int i = 1; i < 16; ++i) d = sl == (unsigned)i ? b.accB[i] : d;
    if ((ll >> 5) == ((cc >> 2) & 1u)) *LDSP(float, L_STAT + c.t * 256u + 128u + cc * 4u) = d;
    if (ll < 16u) {
      *LDSP(float, L_STAT + c.t * 256u + ll * 4u) = b.accM[0];
      *LDSP(float, L_STAT + c.t * 256u + 64u + ll * 4u) = b.accM[1];
    }
#pragma unroll
    for (int i = 0; i < 8; ++i) c.a8[i] = 0.f;
  }
  asm volatile("s_waitcnt vmcnt(%0) lgkmcnt(0)\n\ts_barrier" ::"i"(HF ? 4 : 5) : "memory");
}

__global__ __launch_bounds__(512, 2) void mixer_mlp_kernel(const void* __restrict__ x, const void* __restrict__ w1, const void* __restrict__ b1,
                                                           const void* __restrict__ w2, const void* __restrict__ b2, void* __restrict__ out,
                                                           void* __restrict__ ws) {
  Ctx c;
  const unsigned blk = blockIdx.x, xg = blk & 7u, idx = blk >> 3;
  c.r = 8u * (xg >> 2) + (idx >> 2); c.j = xg & 3u; c.q = idx & 3u;
  c.w = rfl(threadIdx.x >> 6); c.t = c.w & 3u; c.hf = c.w >> 2;
  STAMP(0);
  c.sx = __builtin_amdgcn_make_buffer_rsrc((void*)x, 0, S_ * D_ * 2, 0x00020000);
  c.s1 = __builtin_amdgcn_make_buffer_rsrc((void*)w1, 0, D_ * F_ * 2, 0x00020000);
  c.s2 = __builtin_amdgcn_make_buffer_rsrc((void*)w2, 0, D_ * F_ * 2, 0x00020000);
  c.sw = __builtin_amdgcn_make_buffer_rsrc(ws, 0, WS_USED, 0x00020000);
  const srd_t sb1 = __builtin_amdgcn_make_buffer_rsrc((void*)b1, 0, F_ * 2, 0x00020000);
  c.freg2 = (1152u * c.j + fstart(c.q, 0)) * 2u;
  c.f82 = (1152u * c.j + fstart(c.q, 8)) * 2u;
  c.sw1 = (72u * c.t + 40u * c.hf) * W1ROW + c.freg2;
  c.ldst = c.t * 9216u + c.hf * 5120u;
  if (c.hf == 0) block0<0>(c, sb1); else block0<1>(c, sb1);
  asm volatile("" : "+s"(c.sw1), "+s"(c.ldst));
  STAMP(11);
  {
    const unsigned l = lane_id(), tok = l & 31u, h = l >> 5;
    c.vhst = (l & 16u) * 64u + ((l & 15u) + 16u * h) * 16u;
    c.tb = L_B1 + c.hf * 128u + h * 32u;
    c.tc = L_CVEC + c.w * 128u + h * 32u;
    const float s = *LDSP(float, L_STAT + c.t * 256u + tok * 4u), ss = *LDSP(float, L_STAT + c.t * 256u + 128u + tok * 4u);
    const float mean = s * (1.0f / 576.0f);
    const float var = fmaxf(__builtin_fmaf(-mean, mean, ss * (1.0f / 576.0f)), 0.f);
    const float rstd = __builtin_amdgcn_rsqf(var + 1e-5f);
    c.rs = rstd * KAP;
    c.nm = -mean * rstd;
    c.nm *= KAP;
  }
  half_stage<0>(c); STAMP(12);
  half_stage<1>(c); STAMP(13);
  half_stage<2>(c); STAMP(14);
  half_stage<3>(c); STAMP(15);
  half_stage<4>(c); STAMP(16);
  half_stage<5>(c); STAMP(17);
  half_stage<6>(c); STAMP(18);
  half_stage<7>(c); STAMP(19);
  half_stage<8>(c); STAMP(20);
  half_stage<9>(c); STAMP(21);
  half_stage<10>(c); STAMP(22);
  half_stage<11>(c); STAMP(23);
  half_stage<12>(c); STAMP(24);
  half_stage<13>(c); STAMP(25);
  half_stage<14>(c); STAMP(26);

  const unsigned l = lane_id();
  {
    const unsigned wa = c.w * 9216u + l * 16u, ra = (c.w ^ 4u) * 9216u + l * 16u;
    if (c.hf) {
#pragma unroll
      for (int dt = 0; dt < 9; ++dt) *LDSP(f32x4, wa + dt * 1024u) = c.acc2[0][dt];
    } else {
#pragma unroll
      for (int dt = 0; dt < 9; ++dt) { *LDSP(f32x4, wa + dt * 1024u) = c.acc2[1][dt]; c.acc2[1][dt] = c.acc2[0][dt]; }
    }
    check_poll(c, c.polld, true, 1);
    BARRIER_LGKM();
#pragma unroll
    for (int dt = 0; dt < 9; ++dt) c.acc2[1][dt] += ldsf4(ra + dt * 1024u);
  }
  const f32x4* R = c.acc2[1];
  if (c.t != c.j) {
    const unsigned so = WS_P + (((c.r * 4u + c.q) * 4u + c.t) * 4u + c.j) * 9216u + c.hf * 4608u;
#pragma unroll
    for (int m = 0; m < 4; ++m) {
      u32x4 v = {cvtpk(R[2 * m][0], R[2 * m][1]), cvtpk(R[2 * m][2], R[2 * m][3]), cvtpk(R[2 * m + 1][0], R[2 * m + 1][1]), cvtpk(R[2 * m + 1][2], R[2 * m + 1][3])};
#pragma unroll
      for (int i = 0; i < 4; ++i) v[i] = v[i] < 0xfffffffeu ? v[i] : 0xfffffffeu;
      asm volatile("buffer_store_dwordx4 %0, %1, %2, %3 offen sc1\n\ts_nop 1" ::"v"(v), "v"(c.vhld), "s"(c.sw), "s"(so + m * 1024u));
    }
    u32x2 v = {cvtpk(R[8][0], R[8][1]), cvtpk(R[8][2], R[8][3])};
#pragma unroll
    for (int i = 0; i < 2; ++i) v[i] = v[i] < 0xfffffffeu ? v[i] : 0xfffffffeu;
    asm volatile("buffer_store_dwordx2 %0, %1, %2, %3 offen sc1" ::"v"(v), "v"(l * 8u), "s"(c.sw), "s"(so + 4096u));
  } else {
#pragma unroll
    for (int dt = 0; dt < 9; ++dt) *LDSP(f32x4, 3u * SLOT + c.hf * 9216u + dt * 1024u + l * 16u) = R[dt];
  }
  const unsigned short* b2p = (const unsigned short*)b2 + 144u * c.q + 4u * (l >> 4);
  const u32x2 bA = *(const u32x2*)(b2p + 32u * c.t), bB = *(const u32x2*)(b2p + 32u * c.t + 16u), bC = *(const u32x2*)(b2p + 128u);
  asm volatile("s_waitcnt vmcnt(0) lgkmcnt(0)\n\ts_barrier" ::: "memory");
  STAMP(27);
  {
    const unsigned sp = WS_P + ((c.r * 4u + c.q) * 4u + c.j) * 4u * 9216u + c.hf * 4608u;
    const unsigned s0 = sp + ((c.j + 1u) & 3u) * 9216u, s1 = sp + ((c.j + 2u) & 3u) * 9216u, s2 = sp + ((c.j + 3u) & 3u) * 9216u;
    const unsigned va = c.vhld + c.t * 1024u, vb = 4096u + l * 8u;
    u32x4 pa[3]; u32x2 pb[3];
    f32x4 o0 = ldsf4(3u * SLOT + c.hf * 9216u + c.t * 2048u + l * 16u), o1 = ldsf4(3u * SLOT + c.hf * 9216u + c.t * 2048u + 1024u + l * 16u);
    f32x4 o2 = ldsf4(3u * SLOT + c.hf * 9216u + 8192u + l * 16u);
    for (unsigned spin = 0; spin < (1u << 16); ++spin) {
      asm volatile("buffer_load_dwordx4 %0, %6, %8, %9 offen sc1\n\tbuffer_load_dwordx4 %1, %6, %8, %10 offen sc1\n\t"
                   "buffer_load_dwordx4 %2, %6, %8, %11 offen sc1\n\t"
                   "s_cmp_lg_u32 %12, 0\n\ts_cbranch_scc1 .Lskip%=\n\t"
                   "buffer_load_dwordx2 %3, %7, %8, %9 offen sc1\n\tbuffer_load_dwordx2 %4, %7, %8, %10 offen sc1\n\t"
                   "buffer_load_dwordx2 %5, %7, %8, %11 offen sc1\n.Lskip%=:\n\ts_waitcnt vmcnt(0)"
                   : "=&v"(pa[0]), "=&v"(pa[1]), "=&v"(pa[2]), "=&v"(pb[0]), "=&v"(pb[1]), "=&v"(pb[2])
                   : "v"(va), "v"(vb), "s"(c.sw), "s"(s0), "s"(s1), "s"(s2), "s"(c.t) : "memory", "scc");
      unsigned mx = 0;
#pragma unroll
      for (int k = 0; k < 3; ++k) {
#pragma unroll
        for (int i = 0; i < 4; ++i) mx = mx > pa[k][i] ? mx : pa[k][i];
        if (c.t == 0) { mx = mx > pb[k][0] ? mx : pb[k][0]; mx = mx > pb[k][1] ? mx : pb[k][1]; }
      }
      if (!__any(mx == ~0u)) break;
    }
    STAMP(28);
#pragma unroll
    for (int k = 0; k < 3; ++k)
#pragma unroll
      for (int i = 0; i < 2; ++i) {
        o0[2 * i] += __uint_as_float(pa[k][i] << 16); o0[2 * i + 1] += __uint_as_float(pa[k][i] & 0xffff0000u);
        o1[2 * i] += __uint_as_float(pa[k][2 + i] << 16); o1[2 * i + 1] += __uint_as_float(pa[k][2 + i] & 0xffff0000u);
      }
    unsigned short* op = (unsigned short*)out + (size_t)(128u * c.r + 32u * c.j + 16u * c.hf + (l & 15u)) * D_ + 144u * c.q;
    const unsigned e0 = cvtpk(__builtin_fmaf(o0[0], RKAP, __uint_as_float(bA[0] << 16)), __builtin_fmaf(o0[1], RKAP, __uint_as_float(bA[0] & 0xffff0000u)));
    const unsigned e1 = cvtpk(__builtin_fmaf(o0[2], RKAP, __uint_as_float(bA[1] << 16)), __builtin_fmaf(o0[3], RKAP, __uint_as_float(bA[1] & 0xffff0000u)));
    const unsigned f0 = cvtpk(__builtin_fmaf(o1[0], RKAP, __uint_as_float(bB[0] << 16)), __builtin_fmaf(o1[1], RKAP, __uint_as_float(bB[0] & 0xffff0000u)));
    const unsigned f1 = cvtpk(__builtin_fmaf(o1[2], RKAP, __uint_as_float(bB[1] << 16)), __builtin_fmaf(o1[3], RKAP, __uint_as_float(bB[1] & 0xffff0000u)));
    auto r0 = __builtin_amdgcn_permlane16_swap(e0, f0, false, false);
    auto r1 = __builtin_amdgcn_permlane16_swap(e1, f1, false, false);
    *(u32x4*)(op + 32u * c.t + 16u * ((l >> 4) & 1u) + 8u * (l >> 5)) = (u32x4){r0[0], r1[0], r0[1], r1[1]};
    if (c.t == 0) {
#pragma unroll
      for (int k = 0; k < 3; ++k)
#pragma unroll
        for (int i = 0; i < 2; ++i) { o2[2 * i] += __uint_as_float(pb[k][i] << 16); o2[2 * i + 1] += __uint_as_float(pb[k][i] & 0xffff0000u); }
      *(u32x2*)(op + 128u + 4u * (l >> 4)) =
          (u32x2){cvtpk(__builtin_fmaf(o2[0], RKAP, __uint_as_float(bC[0] << 16)), __builtin_fmaf(o2[1], RKAP, __uint_as_float(bC[0] & 0xffff0000u))),
                  cvtpk(__builtin_fmaf(o2[2], RKAP, __uint_as_float(bC[1] << 16)), __builtin_fmaf(o2[3], RKAP, __uint_as_float(bC[1] & 0xffff0000u)))};
    }
    if (c.w == 0 && c.j == 0 && c.q == 0 && l == 0)
      asm volatile("buffer_store_dword %0, off, %1, %2 sc1" ::"v"(c.tag), "s"(c.sw), "s"(WS_EPOCH + c.r * 128u));
  }
}

extern "C" void kernel_launch(void* const* d_in, const int* in_sizes, int n_in, void* d_out, int out_size, void* d_ws, size_t ws_size,
                              hipStream_t stream) {
  (void)in_sizes; (void)n_in; (void)out_size; (void)ws_size;
  (void)hipFuncSetAttribute((const void*)mixer_mlp_kernel, hipFuncAttributeMaxDynamicSharedMemorySize, LDS_BYTES);
  mixer_mlp_kernel<<<256, 512, LDS_BYTES, stream>>>(d_in[0], d_in[1], d_in[2], d_in[3], d_in[4], d_out, d_ws);
}
```

```cpp
#include <hip/hip_runtime.h>
#include <stdint.h>
#include <utility>

typedef __bf16 bf16x8 __attribute__((ext_vector_type(8)));
typedef __bf16 bf16x2 __attribute__((ext_vector_type(2)));
typedef short s16x4 __attribute__((ext_vector_type(4)));
typedef float f32x16 __attribute__((ext_vector_type(16)));
typedef float f32x4 __attribute__((ext_vector_type(4)));
typedef float f32x2 __attribute__((ext_vector_type(2)));
typedef unsigned u32x4 __attribute__((ext_vector_type(4)));
typedef unsigned u32x2 __attribute__((ext_vector_type(2)));
typedef __amdgpu_buffer_rsrc_t srd_t;
#define DI __device__ __forceinline__
#define LAS __attribute__((address_space(3)))
#define LDSP(T, a) ((LAS T*)(size_t)(a))

constexpr unsigned S_ = 2048, D_ = 576, F_ = 4608;
constexpr unsigned XROW = D_ * 2, W1ROW = F_ * 2, W2ROW = D_ * 2;
constexpr unsigned SLOT = 36864, RING = 4 * SLOT, SLICE = 28672;
constexpr unsigned L_B1 = RING, L_PAT = L_B1 + 1152, L_EX = L_PAT + 2304, LDS_BYTES = L_EX + 8192;
static_assert(LDS_BYTES <= 163840, "LDS");
constexpr unsigned WS_FLAG = 0, WS_EPOCH = 32768, WS_ROWF = 65536, WS_Y = 98304, WS_H = WS_Y + S_ * D_ * 2, H_BYTES = S_ * F_ * 2;
constexpr unsigned WS_P = WS_H + H_BYTES, P_BYTES = 16 * 4 * 4 * 4 * 9216;
constexpr unsigned WS_USED = WS_P + P_BYTES;
constexpr float KAP = 0.4686686930532063f, GAM = 4.91222953926424f, RKAP = 2.1337034344781243f;
#define KSW 10
#define PSLEEP 2
#define PREF 1
#define STAMP(i) do {} while (0)

DI unsigned rfl(unsigned x) { return __builtin_amdgcn_readfirstlane(x); }
DI s16x4 trr(unsigned a) { return __builtin_amdgcn_ds_read_tr16_b64_v4i16(LDSP(s16x4, a)); }
DI bf16x8 cat(s16x4 lo, s16x4 hi) { return __builtin_bit_cast(bf16x8, __builtin_shufflevector(lo, hi, 0, 1, 2, 3, 4, 5, 6, 7)); }
DI f32x4 ldsf4(unsigned a) { return *LDSP(f32x4, a); }
DI unsigned cvtpk(float lo, float hi) { f32x2 v = {lo, hi}; return __builtin_bit_cast(unsigned, __builtin_convertvector(v, bf16x2)); }
template <int CTRL> DI float dpp_add(float v) {
  return v + __builtin_bit_cast(float, __builtin_amdgcn_update_dpp(0, __builtin_bit_cast(int, v), CTRL, 0xf, 0xf, false));
}
#define MFMA32(a, b, c) __builtin_amdgcn_mfma_f32_32x32x16_bf16((a), (b), (c), 0, 0, 0)
#define MFMA16(a, b, c) __builtin_amdgcn_mfma_f32_16x16x32_bf16((a), (b), (c), 0, 0, 0)
#define BARRIER_LGKM() asm volatile("s_waitcnt lgkmcnt(0)\n\ts_barrier" ::: "memory")
#define GAP() __builtin_amdgcn_sched_barrier(0)
#define PIN(x) asm volatile("" : "+v"(x))

struct Ctx {
  srd_t s1, s2, sw;
  unsigned w, t, hf, r, j, q;
  unsigned freg2, f82;
  unsigned tag, sdma, sw1, ldst, hbase;
  u32x4 vpat;
  unsigned vpat4;
  unsigned wb;
  unsigned vhld, vhst, tb, pref;
  float gz[2], gm, gz8, gm8;
  unsigned hv[4], hv8[4];
  bf16x8 X[36];
  bf16x8 F[4];
  f32x16 acc[2];
  float a8[8];
  f32x4 acc2[2][9];
  u32x4 Hb[4][2];
  u32x2 pollv, polld;
};
DI unsigned lane_id() {
  unsigned l;
  asm volatile("v_mbcnt_lo_u32_b32 %0, -1, 0\n\tv_mbcnt_hi_u32_b32 %0, -1, %0" : "=v"(l));
  return l;
}
DI unsigned opq(unsigned v) { PIN(v); return v; }

constexpr unsigned unit_of(int hf, int n) {
  if (n < 2) return 0u | (unsigned)(2 * n + hf) << 2;
  if (n == 2 && hf == 0) return 0u | 8u << 2;
  if (n == 7) return 0u | (unsigned)(4 + hf) << 2;
  if (n == 11) return 0u | (unsigned)(6 + hf) << 2;
  if (n < 7) { const int i = 2 * (n - 2) + hf - 1; return (unsigned)(i % 3 + 1) | (unsigned)(i < 6 ? i / 3 : 8) << 2; }
  const int i = 6 + 2 * (n < 11 ? n - 8 : n - 9) + hf;
  return (unsigned)(i % 3 + 1) | (unsigned)(i / 3) << 2;
}
constexpr unsigned slot_pack(int h) {
  return unit_of(0, 2 * (h - 6)) | unit_of(0, 2 * (h - 6) + 1) << 8 | unit_of(1, 2 * (h - 6)) << 16 | unit_of(1, 2 * (h - 6) + 1) << 24;
}
constexpr int hload_at(int h, int k) {
  if (h == 4 && (k == 10 || k == 12)) return 0;
  if (h < 5 || h > 13) return -1;
  int n = -1;
  if (k == 0 || k == 2) n = 2 * (h - 6) + 3;
  if (k == 10 || k == 12) n = 2 * (h - 6) + 4;
  return n <= 17 ? n : -1;
}
constexpr int dma_at(int h, int k) {
  if (h == 0) return (k < 16 && (k & 1) == 0) ? (k < 8 ? 2 * 8 + (k + 2) / 2 : 3 * 8 + (k - 8) / 2) : (k == 15 ? 3 * 8 + 4 : -1);
  if (h + 3 > 14) return -1;
  const int i = h < 4 ? (k == -2 ? 0 : k == 2 ? 1 : k == 6 ? 2 : k == 10 ? 3 : k == 13 ? 4 : -1)
                      : (k == -2 ? 0 : k == 4 ? 1 : k == 6 ? 2 : k == 8 ? 3 : k == 14 ? 4 : -1);
  return i < 0 ? -1 : (h + 3) * 8 + i;
}
constexpr bool poll_at(int h, int k) { return k == -1 && (h == 4 || h == 7 || h == 9 || h == 10 || h == 11); }
constexpr int flag_at(int h) { return (h == 3 || h == 5 || h == 7 || h == 9) ? (h + 1) / 2 : 0; }
constexpr bool hst_at(int h, int k) { return (h < 8 && (h & 1) && (k == 0 || k == 15)) || (h == 1 && k == 12); }
constexpr bool pref_at(int h, int k) { return PREF && h == 1 && k == 8; }
constexpr int nops(int h, int k, int n) {
  return (poll_at(h, k) ? 1 : 0) + (hload_at(h, k) >= 0 ? 1 : 0) + (hst_at(h, k) ? 1 : 0) + (pref_at(h, k) ? 1 : 0) +
         (dma_at(h, k) >= 0 && (dma_at(h, k) & 7) < n ? 1 : 0);
}
constexpr int between(int h0, int k0, int h1, int k1, int n) {
  int s = 0;
  for (int h = h0; h <= h1; ++h)
    for (int k = -2; k < 16; ++k)
      if ((h > h0 || k > k0) && (h < h1 || k < k1)) s += nops(h, k, n);
  return s;
}
constexpr int cmin(int a, int b) { return a < b ? a : b; }

DI unsigned fstart(unsigned qq, unsigned ch) { return 32u * (9u * qq + ch + (qq & 1u) * (1u - 9u * (ch >> 3))); }
DI unsigned hunit(const Ctx& c, unsigned qq, unsigned ch) { return c.hbase + (qq * 9u + ch) * 8192u; }

DI void dma16(unsigned m0v, unsigned voff, srd_t srd, unsigned soff) {
  asm volatile("s_mov_b32 m0, %0\n\ts_nop 0\n\tbuffer_load_dwordx4 %1, %2, %3 offen lds" ::"s"(m0v), "v"(voff), "s"(srd), "s"(soff));
}
template <int HS, int I> DI void issue_piece(Ctx& c) {
  const srd_t sd = HS < 6 ? c.s1 : c.s2;
  if constexpr (I == 0) {
    if constexpr (HS < 6) c.sdma = c.sw1 + ((HS & 1) * 288u * W1ROW + ((HS >> 1) + 1) * 128u);
    else {
      const unsigned u = (slot_pack(HS) >> (8u * c.t)) & 0xffu;
      c.sdma = (1152u * c.j + fstart((c.q + (u & 3u)) & 3u, u >> 2)) * W2ROW + 288u * c.q;
    }
    dma16((HS & 3) * SLOT + c.ldst, c.vpat[0], sd, c.sdma);
  } else if constexpr (I < 4) {
    asm volatile("buffer_load_dwordx4 %0, %1, %2 offen offset:%3 lds" ::"v"(c.vpat[I]), "s"(sd), "s"(c.sdma), "i"(1024 * I));
  } else {
    asm volatile("s_cmp_lg_u32 %4, 0\n\ts_cbranch_scc1 .Lskip%=\n\ts_mov_b32 m0, %0\n\ts_nop 0\n\tbuffer_load_dwordx4 %1, %2, %3 offen lds\n.Lskip%=:"
                 ::"s"((HS & 3) * SLOT + 4096u + c.ldst), "v"(c.vpat4), "s"(sd), "s"(c.sdma), "s"(c.hf) : "scc");
  }
}
template <int HS> DI void issue_slot(Ctx& c) {
  issue_piece<HS, 0>(c); issue_piece<HS, 1>(c); issue_piece<HS, 2>(c); issue_piece<HS, 3>(c); issue_piece<HS, 4>(c);
}
template <int N, int U> DI void issue_hload(Ctx& c) {
  const unsigned u = ((unit_of(0, N) | unit_of(1, N) << 8) >> (8u * c.hf)) & 0xffu;
  const unsigned so = hunit(c, (c.q + (u & 3u)) & 3u, u >> 2);
  asm volatile("buffer_load_dwordx4 %0, %1, %2, %3 offen offset:%4 sc1" : "=v"(c.Hb[N & 3][U]) : "v"(c.vhld), "s"(c.sw), "s"(so), "i"(1024 * U));
}
DI unsigned poll_off(const Ctx& c, bool dest) {
  const unsigned l = lane_id(), dp = (l & 3u) ? (l & 3u) : 1u;
  return WS_FLAG + ((dest ? (c.r * 4u + ((c.j + dp) & 3u)) * 4u + c.q : (c.r * 4u + c.j) * 4u + ((c.q + dp) & 3u)) << 7);
}
DI void store_flag(Ctx& c, unsigned count) {
  u32x2 v = {count, c.tag};
  const unsigned off = WS_FLAG + (((c.r * 4u + c.j) * 4u + c.q) << 7);
  unsigned long long sv;
  asm volatile("s_cmp_lg_u32 %4, 0\n\ts_cbranch_scc1 .Lskip%=\n\ts_mov_b64 %0, exec\n\ts_mov_b64 exec, 1\n\t"
               "buffer_store_dwordx2 %1, off, %2, %3 sc1\n\ts_mov_b64 exec, %0\n.Lskip%=:"
               : "=&s"(sv) : "v"(v), "s"(c.sw), "s"(off), "s"(c.w) : "scc");
}
DI void check_poll(Ctx& c, u32x2& pv, bool dest, unsigned need) {
  PIN(pv);
  const bool ok = pv[1] == c.tag && pv[0] - need <= 5u - need;
  if (__builtin_expect(!__all(ok), 0)) {
    const unsigned voff = poll_off(c, dest);
    for (unsigned spin = 0; spin < (1u << 16); ++spin) {
      u32x2 v;
      asm volatile("s_sleep 1\n\tbuffer_load_dwordx2 %0, %1, %2, 0 offen sc1\n\ts_waitcnt vmcnt(0)" : "=&v"(v) : "v"(voff), "s"(c.sw) : "memory");
      if (__all(v[1] == c.tag && v[0] - need <= 5u - need)) break;
    }
  }
}
DI void store_h(Ctx& c, const unsigned (&hv)[4], unsigned ch, unsigned piece) {
  const u32x4 v = {hv[0], hv[1], hv[2], hv[3]};
  const unsigned so = hunit(c, c.q, ch) + piece * 512u;
  asm volatile("buffer_store_dwordx4 %0, %1, %2, %3 offen sc1\n\ts_nop 1" ::"v"(v), "v"(c.vhst), "s"(c.sw), "s"(so));
}
template <int N0, int N1> DI void wait_vm(const Ctx& c) {
  static_assert(N1 <= N0 && N1 >= 0 && N0 < 64, "vmcnt");
  if constexpr (N0 == N1) asm volatile("s_waitcnt vmcnt(%0)" ::"i"(N0) : "memory");
  else asm volatile("s_waitcnt vmcnt(%0)\n\ts_cmp_eq_u32 %2, 0\n\ts_cbranch_scc1 .Lskip%=\n\ts_waitcnt vmcnt(%1)\n.Lskip%=:" ::"i"(N0), "i"(N1), "s"(c.hf) : "memory", "scc");
}

template <int H> DI void set_bases(Ctx& c) {
  constexpr unsigned d = (H & 3) ? SLOT : 0u - 3u * SLOT;
  if constexpr (H == 6) {
    const unsigned l = lane_id(), g = l >> 4;
    c.wb = opq(2u * SLOT + (16u * (g >> 1) + 4u * (g & 1u) + ((l >> 2) & 3u)) * 288u + 8u * (l & 3u) + c.hf * 18432u);
  } else if constexpr (H > 0) c.wb = opq(c.wb + d);
}
template <int H, int S> DI void rd(Ctx& c) {
  constexpr unsigned o = H < 6 ? 2048u * S : 9216u * (S / 9) + 32u * (S % 9), e = H < 6 ? 512u : 2304u;
  c.F[(H * 18 + S) & 3] = cat(trr(c.wb + o), trr(c.wb + o + e));
}
template <int H, int S> DI void mm(Ctx& c) {
  const bf16x8 a = c.F[(H * 18 + S) & 3];
  if constexpr (H < 6) {
    constexpr int B = ((H >> 1) + 1) & 1;
    c.acc[B] = MFMA32(a, c.X[18 * (H & 1) + S], c.acc[B]);
  } else {
    constexpr int N = 2 * (H - 6) + S / 9, dt = S % 9;
    f32x4 z = {};
    c.acc2[0][dt] = MFMA16(a, __builtin_bit_cast(bf16x8, c.Hb[N & 3][0]), N == 0 ? z : c.acc2[0][dt]);
    c.acc2[1][dt] = MFMA16(a, __builtin_bit_cast(bf16x8, c.Hb[N & 3][1]), N == 0 ? z : c.acc2[1][dt]);
  }
}
template <int Q> DI void bias_quad(f32x16& acc, unsigned a) {
  const f32x4 v = ldsf4(a + (Q >> 1) * 64u + (Q & 1) * 16u);
#pragma unroll
  for (int i = 0; i < 4; ++i) acc[4 * Q + i] = v[i];
}
template <int E, int ST> DI void gelu_stage(float s, float& z, float& m0, unsigned (&hv)[4]) {
  if constexpr (ST == 0) { z = s * __builtin_fmaf(s, s, GAM); PIN(z); }
  if constexpr (ST == 1) { z = __builtin_amdgcn_exp2f(-z); PIN(z); }
  if constexpr (ST == 2) { z = __builtin_amdgcn_rcpf(1.0f + z); PIN(z); }
  if constexpr (ST == 3) {
    if constexpr ((E & 1) == 0) { m0 = s * z; PIN(m0); }
    else { float m = s * z, mm0 = m0; PIN(m); PIN(mm0); hv[(E >> 1) & 3] = cvtpk(mm0, m); PIN(hv[(E >> 1) & 3]); }
  }
}
constexpr int gstart(int e) { return 1 + (e * 29) / 15; }
constexpr int gelem(int g, int st) {
  for (int e = 0; e < 16; ++e) if (gstart(e) + st == g) return e;
  return -1;
}
template <int B, int G, int ST> DI void gelu_part(Ctx& c) {
  constexpr int e = gelem(G, ST);
  if constexpr (e >= 0) gelu_stage<e, ST>(c.acc[B & 1][e], c.gz[e & 1], c.gm, c.hv);
}
template <int B, int G> DI void gelu_gap(Ctx& c) {
  gelu_part<B, G, 3>(c); gelu_part<B, G, 2>(c); gelu_part<B, G, 1>(c); gelu_part<B, G, 0>(c);
  if constexpr (B < 2) {
    if constexpr (G == 27) { bias_quad<0>(c.acc[B & 1], c.tb + (B + 2) * 256u); bias_quad<1>(c.acc[B & 1], c.tb + (B + 2) * 256u); }
    if constexpr (G == 31) bias_quad<2>(c.acc[B & 1], c.tb + (B + 2) * 256u);
    if constexpr (G == 33) bias_quad<3>(c.acc[B & 1], c.tb + (B + 2) * 256u);
  }
}

template <int H, int K> DI void side(Ctx& c) {
  if constexpr (poll_at(H, K)) {
    if constexpr (H == 10) asm volatile("buffer_load_dwordx2 %0, %1, %2, 0 offen sc1" : "=v"(c.polld) : "v"(poll_off(c, true)), "s"(c.sw));
    else asm volatile("buffer_load_dwordx2 %0, %1, %2, 0 offen sc1" : "=v"(c.pollv) : "v"(c.pref), "s"(c.sw));
  }
  if constexpr (K == -1 && flag_at(H)) store_flag(c, flag_at(H));
  if constexpr (H == 4 && K == 9) c.vhld = lane_id() * 16u;
  if constexpr (H == 3 && K == 11) { c.pref = poll_off(c, false); PIN(c.pref); }
  if constexpr (hload_at(H, K) >= 0) issue_hload<hload_at(H, K), (K >> 1) & 1>(c);
  if constexpr (pref_at(H, K)) {
    const unsigned i = c.w * 64u + lane_id();
    asm volatile("buffer_load_dword %0, %1, %2, %3 offen" : "=v"(c.pref) : "v"((i < 324u ? i : 323u) * 128u), "s"(c.s2), "s"(c.j * 1152u * W2ROW + ((c.r & 7u) * 4u + c.q) * 324u * 128u));
  }
  if constexpr (dma_at(H, K) >= 0) issue_piece<(dma_at(H, K) >> 3), (dma_at(H, K) & 7)>(c);
  if constexpr (H == 2 && K == 14) {
    const unsigned l = lane_id();
#pragma unroll
    for (int i = 0; i < 4; ++i) c.vpat[i] = *LDSP(unsigned, L_PAT + (c.hf * 5u + i) * 256u + l * 4u);
    c.vpat4 = *LDSP(unsigned, L_PAT + 1024u + l * 4u);
  }
  if constexpr (H == 0 && K == -2) {
    if (c.hf) {
      const unsigned l = lane_id();
      const f32x4 e0 = ldsf4(L_EX + c.t * 2048u + l * 16u), e1 = ldsf4(L_EX + c.t * 2048u + 1024u + l * 16u);
#pragma unroll
      for (int i = 0; i < 4; ++i) { c.a8[i] = e0[i]; c.a8[4 + i] = e1[i]; }
    }
  }
  if constexpr (H < 8) gelu_gap<(H >> 1), K + 18 * (H & 1)>(c);
  if constexpr (H < 2) {
    constexpr int G = K + 18 * H - 1;
    if constexpr (G >= 0 && G < 28) gelu_stage<G / 4, (G & 3)>(c.a8[G / 4], c.gz8, c.gm8, c.hv8);
    if constexpr (G == 28) { gelu_stage<7, 0>(c.a8[7], c.gz8, c.gm8, c.hv8); gelu_stage<7, 1>(c.a8[7], c.gz8, c.gm8, c.hv8); }
    if constexpr (G == 29) { gelu_stage<7, 2>(c.a8[7], c.gz8, c.gm8, c.hv8); gelu_stage<7, 3>(c.a8[7], c.gz8, c.gm8, c.hv8); }
  }
  if constexpr (hst_at(H, K)) {
    if constexpr (K == 12) store_h(c, c.hv8, 8u, c.hf);
    else store_h(c, c.hv, 2u * (H >> 1) + c.hf, K == 0 ? 0u : 1u);
  }
}
template <int H0, int K0, int H1, int K1> DI void wait_after(const Ctx& c) {
  wait_vm<between(H0, K0, H1, K1, 5), between(H0, K0, H1, K1, 4)>(c);
}
template <int H> DI void end_hs(Ctx& c) {
  if constexpr (H == 0) wait_after<0, -3, 0, 16>(c);
  else if constexpr (H == 1) wait_after<0, 6, 1, 16>(c);
  else if constexpr (H == 3) wait_after<1, 15, 3, 16>(c);
  else if constexpr (poll_at(H, -1) && H != 10) { wait_after<H, -1, H, 16>(c); check_poll(c, c.pollv, false, H == 4 ? 2 : (H - 1) / 2); }
  else if constexpr (flag_at(H + 1)) wait_after<H - 1, 15, H, 16>(c);
  else if constexpr (H < 14) wait_after<H - 1, 12, H, 16>(c);
  if constexpr (H == 2 && PREF) asm volatile("" ::"v"(c.pref));
  if constexpr (H < 14) BARRIER_LGKM();
  if constexpr (H >= 5 && H < 14) { PIN(c.Hb[(2 * (H - 5)) & 3][0]); PIN(c.Hb[(2 * (H - 5)) & 3][1]); }
  GAP();
}
template <int H> DI void mid_wait(Ctx& c) {
  if constexpr (H >= 6) wait_after<H - 1, 2, H, 9>(c);
  if constexpr (H >= 6) { PIN(c.Hb[(2 * (H - 6) + 1) & 3][0]); PIN(c.Hb[(2 * (H - 6) + 1) & 3][1]); }
}
#define GST(H, i) do {} while (0)
template <int H, int K> DI void step(Ctx& c) {
  GST(H, K + 2);
  if constexpr (K == 9) mid_wait<H>(c);
  if constexpr (K == KSW) asm volatile("s_setprio 0");
  mm<H, K>(c);
  if constexpr (K + 4 < 18) rd<H, K + 4>(c);
  side<H, K>(c);
  GAP();
}
template <int H, int... Ks> DI void steps(Ctx& c, std::integer_sequence<int, Ks...>) { (step<H, Ks>(c), ...); }
DI void prio_young(const Ctx& c) {
  asm volatile("s_cmp_eq_u32 %0, 0\n\ts_cbranch_scc1 .Lskip%=\n\ts_setprio 1\n.Lskip%=:" ::"s"(c.hf) : "scc");
}
template <int H> DI void half_stage(Ctx& c) {
  prio_young(c);
  set_bases<H>(c);
  rd<H, 0>(c); rd<H, 1>(c);
  GAP();
  GST(H, 0);
  if constexpr (H > 0) mm<H - 1, 16>(c); else c.acc[0] = MFMA32(c.F[2], c.X[34], c.acc[0]);
  rd<H, 2>(c);
  side<H, -2>(c);
  GAP();
  GST(H, 1);
  if constexpr (H > 0) mm<H - 1, 17>(c); else c.acc[0] = MFMA32(c.F[3], c.X[35], c.acc[0]);
  rd<H, 3>(c);
  side<H, -1>(c);
  GAP();
  steps<H>(c, std::make_integer_sequence<int, 16>{});
  if constexpr (H == 14) { mm<14, 16>(c); mm<14, 17>(c); }
  GST(H, 18);
  end_hs<H>(c);
  GST(H, 19);
}

struct B0 {
  unsigned vy, vw1, vc8, xa[4], l8, l80, lx, lw;
  bf16x8 G[4];
  f32x16 accB;
};
template <int S> DI void issue_y(Ctx& c, B0& b) {
  const unsigned dst = (S % 5) * SLICE + c.w * 1024u, so = WS_Y + c.r * 128u * XROW + S * 128u;
  asm volatile("s_mov_b32 m0, %0\n\ts_nop 0\n\tbuffer_load_dwordx4 %1, %2, %3 offen sc1 lds" ::"s"(dst), "v"(b.vy), "s"(c.sw), "s"(so));
  asm volatile("s_mov_b32 m0, %0\n\ts_nop 0\n\tbuffer_load_dwordx4 %1, %2, %3 offen sc1 lds" ::"s"(dst + 8192u), "v"(b.vy), "s"(c.sw), "s"(so + 64u * XROW));
}
template <int HF, int S> DI void issue_w(Ctx& c, B0& b) {
  const unsigned dst = (S % 5) * SLICE, kk = S * 64u;
  dma16(dst + c.w * 1024u + 16384u, b.vw1, c.s1, (kk + 8u * c.w) * W1ROW + c.freg2);
  if constexpr (HF == 0) dma16(dst + c.t * 1024u + 24576u, b.vc8, c.s1, (kk + 16u * c.t) * W1ROW + c.f82);
}
template <int HF, int S, int K> DI void rd0(Ctx& c, B0& b) {
  c.X[S * 4 + K] = *LDSP(bf16x8, b.xa[K]);
  c.F[K] = cat(trr(c.wb + 2048u * K), trr(c.wb + 2048u * K + 512u));
  if constexpr (HF == 0) b.G[K] = cat(trr(b.l8 + 1024u * K), trr(b.l8 + 1024u * K + 256u));
}
template <int S, int K> DI void mmA(Ctx& c) { c.acc[0] = MFMA32(c.F[K], c.X[S * 4 + K], c.acc[0]); }
template <int HF, int S, int K> DI void mmB(Ctx& c, B0& b) {
  if constexpr (HF == 0) b.accB = MFMA32(b.G[K], c.X[S * 4 + K], b.accB);
}
template <int HF, int S> DI void bases0(Ctx& c, B0& b) {
  constexpr unsigned base = (S % 5) * SLICE;
#pragma unroll
  for (int k = 0; k < 4; ++k) b.xa[k] = opq(base + (b.lx ^ (unsigned)(k << 5)));
  c.wb = opq(base + 16384u + b.lw);
  if constexpr (HF == 0) b.l8 = opq(base + 24576u + b.l80);
}
template <int HF, int S> DI void slice(Ctx& c, B0& b) {
  constexpr int n0 = HF ? 3 : 4, n = HF ? 4 : 5, M = 3;
  constexpr int N = S == 0 ? 2 : S == 1 ? 4 : S == 2 ? 4 + n0 : S == 3 ? 2 + 2 * n0 : S <= 5 ? 3 * n0 : S == 6 ? 2 * n0 + M : S == 7 ? n0 + M : M + n;
  asm volatile("s_waitcnt vmcnt(%0) lgkmcnt(0)\n\ts_barrier" ::"i"(N) : "memory");
  STAMP(3 + S);
  GAP();
  if constexpr (S > 0) mmA<S - 1, 2>(c);
  rd0<HF, S, 0>(c, b);
  GAP();
  if constexpr (S > 0) mmB<HF, S - 1, 2>(c, b);
  rd0<HF, S, 1>(c, b);
  GAP();
  if constexpr (S > 0) mmA<S - 1, 3>(c);
  if constexpr (S <= 1) issue_y<2 * S + 2>(c, b);
  if constexpr (S >= 2 && S <= 4) issue_y<S + 4>(c, b);
  if constexpr (S == 7) issue_slot<0>(c);
  if constexpr (S == 8) issue_slot<1>(c);
  GAP();
  if constexpr (S > 0) mmB<HF, S - 1, 3>(c, b);
  rd0<HF, S, 2>(c, b);
  GAP();
  mmA<S, 0>(c);
  rd0<HF, S, 3>(c, b);
  GAP();
  mmB<HF, S, 0>(c, b);
  if constexpr (S <= 1) issue_y<2 * S + 3>(c, b);
  if constexpr (S >= 1 && S <= 4) issue_w<HF, S + 4>(c, b);
  if constexpr (S == 5) {
    const unsigned l = lane_id();
    const u32x4 ones = {~0u, ~0u, ~0u, ~0u};
#pragma unroll
    for (int k = 0; k < 3; ++k) {
      const unsigned so = WS_P + (((c.r * 4u + c.q) * 4u + c.j) * 4u + ((c.j + 1u + k) & 3u)) * 9216u;
      asm volatile("buffer_store_dwordx4 %0, %1, %2, %3 offen sc1" ::"v"(ones), "v"(l * 16u), "s"(c.sw), "s"(so + c.w * 1024u));
      if (c.w == (unsigned)k) asm volatile("buffer_store_dwordx4 %0, %1, %2, %3 offen sc1" ::"v"(ones), "v"(l * 16u), "s"(c.sw), "s"(so + 8192u));
    }
    asm volatile("s_nop 1");
  }
  if constexpr (S == 6) {
#pragma unroll
    for (int i = 0; i < 4; ++i) c.vpat[i] = b.vw1 + i * (8u * W1ROW - 1024u);
    c.vpat4 = b.vw1 + 32u * W1ROW;
    bias_quad<0>(c.acc[1], c.tb + 256u); bias_quad<1>(c.acc[1], c.tb + 256u); bias_quad<2>(c.acc[1], c.tb + 256u); bias_quad<3>(c.acc[1], c.tb + 256u);
  }
  if constexpr (S < 8) bases0<HF, S + 1>(c, b);
  GAP();
  mmA<S, 1>(c);
  GAP();
  mmB<HF, S, 1>(c, b);
  GAP();
}
DI float wave_sum(float v) {
  v = dpp_add<0x121>(v); v = dpp_add<0x122>(v); v = dpp_add<0x124>(v); v = dpp_add<0x128>(v);
  auto a = __builtin_amdgcn_permlane16_swap(__float_as_uint(v), __float_as_uint(v), false, false);
  v = __uint_as_float(a[0]) + __uint_as_float(a[1]);
  auto b = __builtin_amdgcn_permlane32_swap(__float_as_uint(v), __float_as_uint(v), false, false);
  return __uint_as_float(b[0]) + __uint_as_float(b[1]);
}
template <int HF> DI void block0(Ctx& c, const srd_t sx, const srd_t sb1) {
  B0 b;
  const unsigned l = threadIdx.x & 63u, g = l >> 4, qq = (l >> 2) & 3u, pp = l & 3u, h = l >> 5;
  const unsigned row = (c.r * 128u + (c.j * 4u + c.q) * 8u + c.w) * XROW, vb = 1024u + (l & 31u) * 4u;
  u32x4 xa; unsigned xb, b1raw, epoch;
  asm volatile("buffer_load_dwordx4 %0, %1, %2, %3 offen" : "=v"(xa) : "v"(l * 16u), "s"(sx), "s"(row));
  asm volatile("buffer_load_dword %0, %1, %2, %3 offen" : "=v"(xb) : "v"(vb), "s"(sx), "s"(row));
  asm volatile("buffer_load_dword %0, off, %1, %2 sc1" : "=v"(epoch) : "s"(c.sw), "s"(WS_EPOCH + c.r * 128u));
  b.vw1 = (l >> 3) * W1ROW + (((l & 7u) ^ ((g & 1u) << 2)) << 4);
  b.vc8 = (l >> 2) * W1ROW + (pp << 4);
  issue_w<HF, 0>(c, b);
  GAP();
  asm volatile("s_waitcnt vmcnt(%2)" : "+v"(xa), "+v"(xb) : "i"(HF ? 2 : 3) : "memory");
  {
    float v[10];
#pragma unroll
    for (int i = 0; i < 4; ++i) { v[2 * i] = __uint_as_float(xa[i] << 16); v[2 * i + 1] = __uint_as_float(xa[i] & 0xffff0000u); }
    v[8] = __uint_as_float(xb << 16); v[9] = __uint_as_float(xb & 0xffff0000u);
    const float wt = h ? 0.f : 1.f;
    const float mean = wave_sum(((v[0] + v[1]) + (v[2] + v[3])) + ((v[4] + v[5]) + (v[6] + v[7])) + wt * (v[8] + v[9])) * (1.0f / 576.0f);
#pragma unroll
    for (int i = 0; i < 10; ++i) v[i] -= mean;
    float ss = wt * (v[8] * v[8] + v[9] * v[9]);
#pragma unroll
    for (int i = 0; i < 8; ++i) ss = __builtin_fmaf(v[i], v[i], ss);
    const float rs = KAP * __builtin_amdgcn_rsqf(wave_sum(ss) * (1.0f / 576.0f) + 1e-5f);
    const u32x4 ya = {cvtpk(v[0] * rs, v[1] * rs), cvtpk(v[2] * rs, v[3] * rs), cvtpk(v[4] * rs, v[5] * rs), cvtpk(v[6] * rs, v[7] * rs)};
    const unsigned yb = cvtpk(v[8] * rs, v[9] * rs);
    asm volatile("buffer_store_dwordx4 %0, %1, %2, %3 offen sc1\n\ts_nop 1" ::"v"(ya), "v"(l * 16u), "s"(c.sw), "s"(WS_Y + row));
    asm volatile("buffer_store_dword %0, %1, %2, %3 offen sc1" ::"v"(yb), "v"(vb), "s"(c.sw), "s"(WS_Y + row));
  }
  GAP();
  STAMP(1);
  b.vy = ((l >> 3) + 8u * c.w) * XROW + (((l & 7u) ^ ((g + 4u * c.w) & 7u)) << 4);
  b.lw = (8u * h + qq) * 128u + (((4u * HF + 2u * (g & 1u) + (pp & 1u)) ^ (((qq >> 1) & 1u) << 2)) << 4) + 8u * (pp >> 1);
  const unsigned xrow = 32u * c.t + (l & 31u);
  b.lx = xrow * 128u + ((h ^ ((xrow >> 1) & 7u)) << 4);
  b.l80 = (8u * h + qq) * 64u + 32u * (g & 1u) + 16u * (pp & 1u) + 8u * (pp >> 1);
  c.tb = L_B1 + HF * 128u + h * 32u;
  c.vhst = (l & 16u) * 64u + (l & 15u) * 16u + (l & 32u) * 8u;
  bases0<HF, 0>(c, b);
  GAP();
  asm volatile("s_waitcnt vmcnt(0)\n\ts_barrier" : "+v"(epoch)::"memory");
  c.tag = rfl(epoch) + 1u;
  STAMP(31);
  store_flag(c, 1u);
  {
    const unsigned tid = threadIdx.x < 288u ? threadIdx.x : 287u;
    const unsigned off = c.freg2 + tid * 2u + (tid >> 8) * (c.f82 - c.freg2 - 512u);
    asm volatile("buffer_load_ushort %0, %1, %2, 0 offen" : "=v"(b1raw) : "v"(off), "s"(sb1));
  }
  issue_w<HF, 1>(c, b); issue_w<HF, 2>(c, b); issue_w<HF, 3>(c, b); issue_w<HF, 4>(c, b);
#pragma unroll
  for (int k = 0; k < 2; ++k) {
    const unsigned i = k ? 8u : c.w, nn = 64u * i + l, rho = nn / 18u, chn = nn - rho * 18u;
    const unsigned f = (rho & 19u) | ((rho & 4u) << 1) | ((rho & 8u) >> 1);
    const unsigned rel = i < 5u ? i : i - 5u;
    *LDSP(unsigned, L_PAT + i * 256u + l * 4u) = f * W2ROW + chn * 16u - (rel < 4u ? rel * 1024u : 0u);
  }
  asm volatile("s_waitcnt vmcnt(%1)" : "+v"(b1raw) : "i"(HF ? 4 : 8) : "memory");
  if (threadIdx.x < 288u) *LDSP(float, L_B1 + threadIdx.x * 4u) = __uint_as_float(b1raw << 16) * KAP;
  if (HF == 1 && c.w == 7) {
    const unsigned voff = WS_FLAG + ((c.r * 16u + (l & 15u)) << 7);
    for (unsigned spin = 0; spin < (1u << 16); ++spin) {
      u32x2 v;
      asm volatile("s_sleep %3\n\tbuffer_load_dwordx2 %0, %1, %2, 0 offen sc1\n\ts_waitcnt vmcnt(0)" : "=&v"(v) : "v"(voff), "s"(c.sw), "i"(PSLEEP) : "memory");
      if (__all(v[1] == c.tag && v[0] - 1u <= 4u)) break;
    }
  }
  BARRIER_LGKM();
  STAMP(2);
  issue_y<0>(c, b); issue_y<1>(c, b);
  bias_quad<0>(c.acc[0], c.tb); bias_quad<1>(c.acc[0], c.tb); bias_quad<2>(c.acc[0], c.tb); bias_quad<3>(c.acc[0], c.tb);
  if constexpr (HF == 0) { bias_quad<0>(b.accB, c.tb + 1024u); bias_quad<1>(b.accB, c.tb + 1024u); bias_quad<2>(b.accB, c.tb + 1024u); bias_quad<3>(b.accB, c.tb + 1024u); }
  slice<HF, 0>(c, b); slice<HF, 1>(c, b); slice<HF, 2>(c, b); slice<HF, 3>(c, b); slice<HF, 4>(c, b);
  slice<HF, 5>(c, b); slice<HF, 6>(c, b); slice<HF, 7>(c, b); slice<HF, 8>(c, b);
  mmB<HF, 8, 2>(c, b); mmB<HF, 8, 3>(c, b);
  c.wb = opq(b.lw);
  if constexpr (HF == 0) {
    const unsigned ll = lane_id();
    f32x4 e0, e1;
#pragma unroll
    for (int i = 0; i < 4; ++i) { e0[i] = b.accB[8 + i]; e1[i] = b.accB[12 + i]; }
    *LDSP(f32x4, L_EX + c.t * 2048u + ll * 16u) = e0;
    *LDSP(f32x4, L_EX + c.t * 2048u + 1024u + ll * 16u) = e1;
#pragma unroll
    for (int i = 0; i < 8; ++i) c.a8[i] = b.accB[i];
  } else {
#pragma unroll
    for (int i = 0; i < 8; ++i) c.a8[i] = 0.f;
  }
  asm volatile("s_waitcnt vmcnt(%0) lgkmcnt(0)\n\ts_barrier" ::"i"(HF ? 4 : 5) : "memory");
}

__global__ __launch_bounds__(512, 2) void mixer_mlp_kernel(const void* __restrict__ x, const void* __restrict__ w1, const void* __restrict__ b1,
                                                           const void* __restrict__ w2, const void* __restrict__ b2, void* __restrict__ out,
                                                           void* __restrict__ ws) {
  Ctx c;
  const unsigned blk = blockIdx.x, xg = blk & 7u, idx = blk >> 3;
  c.r = 8u * (xg >> 2) + (idx >> 2); c.j = xg & 3u; c.q = idx & 3u;
  c.w = rfl(threadIdx.x >> 6); c.t = c.w & 3u; c.hf = c.w >> 2;
  STAMP(0);
  const srd_t sx = __builtin_amdgcn_make_buffer_rsrc((void*)x, 0, S_ * D_ * 2, 0x00020000);
  c.s1 = __builtin_amdgcn_make_buffer_rsrc((void*)w1, 0, D_ * F_ * 2, 0x00020000);
  c.s2 = __builtin_amdgcn_make_buffer_rsrc((void*)w2, 0, D_ * F_ * 2, 0x00020000);
  c.sw = __builtin_amdgcn_make_buffer_rsrc(ws, 0, WS_USED, 0x00020000);
  const srd_t sb1 = __builtin_amdgcn_make_buffer_rsrc((void*)b1, 0, F_ * 2, 0x00020000);
  c.freg2 = (1152u * c.j + fstart(c.q, 0)) * 2u;
  c.f82 = (1152u * c.j + fstart(c.q, 8)) * 2u;
  c.sw1 = (72u * c.t + 40u * c.hf) * W1ROW + c.freg2;
  c.ldst = c.t * 9216u + c.hf * 5120u;
  if (c.hf == 0) block0<0>(c, sx, sb1); else block0<1>(c, sx, sb1);
  c.hbase = WS_H + ((c.r * 4u + c.j) * 144u + c.t) * 2048u;
  asm volatile("" : "+s"(c.sw1), "+s"(c.ldst), "+s"(c.hbase));
  STAMP(12);
  half_stage<0>(c); STAMP(13);
  half_stage<1>(c); STAMP(14);
  half_stage<2>(c); STAMP(15);
  half_stage<3>(c); STAMP(16);
  half_stage<4>(c); STAMP(17);
  half_stage<5>(c); STAMP(18);
  half_stage<6>(c); STAMP(19);
  half_stage<7>(c); STAMP(20);
  half_stage<8>(c); STAMP(21);
  half_stage<9>(c); STAMP(22);
  half_stage<10>(c); STAMP(23);
  half_stage<11>(c); STAMP(24);
  half_stage<12>(c); STAMP(25);
  half_stage<13>(c); STAMP(26);
  half_stage<14>(c); STAMP(27);

  const unsigned l = lane_id();
  {
    const unsigned wa = c.w * 9216u + l * 16u, ra = (c.w ^ 4u) * 9216u + l * 16u;
    if (c.hf) {
#pragma unroll
      for (int dt = 0; dt < 9; ++dt) *LDSP(f32x4, wa + dt * 1024u) = c.acc2[0][dt];
    } else {
#pragma unroll
      for (int dt = 0; dt < 9; ++dt) { *LDSP(f32x4, wa + dt * 1024u) = c.acc2[1][dt]; c.acc2[1][dt] = c.acc2[0][dt]; }
    }
    check_poll(c, c.polld, true, 2);
    BARRIER_LGKM();
#pragma unroll
    for (int dt = 0; dt < 9; ++dt) c.acc2[1][dt] += ldsf4(ra + dt * 1024u);
  }
  const f32x4* R = c.acc2[1];
  if (c.t != c.j) {
    const unsigned so = WS_P + (((c.r * 4u + c.q) * 4u + c.t) * 4u + c.j) * 9216u + c.hf * 4608u;
#pragma unroll
    for (int m = 0; m < 4; ++m) {
      u32x4 v = {cvtpk(R[2 * m][0], R[2 * m][1]), cvtpk(R[2 * m][2], R[2 * m][3]), cvtpk(R[2 * m + 1][0], R[2 * m + 1][1]), cvtpk(R[2 * m + 1][2], R[2 * m + 1][3])};
#pragma unroll
      for (int i = 0; i < 4; ++i) v[i] = v[i] < 0xfffffffeu ? v[i] : 0xfffffffeu;
      asm volatile("buffer_store_dwordx4 %0, %1, %2, %3 offen sc1\n\ts_nop 1" ::"v"(v), "v"(c.vhld), "s"(c.sw), "s"(so + m * 1024u));
    }
    u32x2 v = {cvtpk(R[8][0], R[8][1]), cvtpk(R[8][2], R[8][3])};
#pragma unroll
    for (int i = 0; i < 2; ++i) v[i] = v[i] < 0xfffffffeu ? v[i] : 0xfffffffeu;
    asm volatile("buffer_store_dwordx2 %0, %1, %2, %3 offen sc1" ::"v"(v), "v"(l * 8u), "s"(c.sw), "s"(so + 4096u));
  } else {
#pragma unroll
    for (int dt = 0; dt < 9; ++dt) *LDSP(f32x4, 3u * SLOT + c.hf * 9216u + dt * 1024u + l * 16u) = R[dt];
  }
  const unsigned short* b2p = (const unsigned short*)b2 + 144u * c.q + 4u * (l >> 4);
  const u32x2 bA = *(const u32x2*)(b2p + 32u * c.t), bB = *(const u32x2*)(b2p + 32u * c.t + 16u), bC = *(const u32x2*)(b2p + 128u);
  asm volatile("s_waitcnt vmcnt(0) lgkmcnt(0)\n\ts_barrier" ::: "memory");
  STAMP(28);
  {
    const unsigned sp = WS_P + ((c.r * 4u + c.q) * 4u + c.j) * 4u * 9216u + c.hf * 4608u;
    const unsigned s0 = sp + ((c.j + 1u) & 3u) * 9216u, s1 = sp + ((c.j + 2u) & 3u) * 9216u, s2 = sp + ((c.j + 3u) & 3u) * 9216u;
    const unsigned va = c.vhld + c.t * 1024u, vb = 4096u + l * 8u;
    u32x4 pa[3]; u32x2 pb[3];
    f32x4 o0 = ldsf4(3u * SLOT + c.hf * 9216u + c.t * 2048u + l * 16u), o1 = ldsf4(3u * SLOT + c.hf * 9216u + c.t * 2048u + 1024u + l * 16u);
    f32x4 o2 = ldsf4(3u * SLOT + c.hf * 9216u + 8192u + l * 16u);
    for (unsigned spin = 0; spin < (1u << 16); ++spin) {
      asm volatile("buffer_load_dwordx4 %0, %6, %8, %9 offen sc1\n\tbuffer_load_dwordx4 %1, %6, %8, %10 offen sc1\n\t"
                   "buffer_load_dwordx4 %2, %6, %8, %11 offen sc1\n\t"
                   "s_cmp_lg_u32 %12, 0\n\ts_cbranch_scc1 .Lskip%=\n\t"
                   "buffer_load_dwordx2 %3, %7, %8, %9 offen sc1\n\tbuffer_load_dwordx2 %4, %7, %8, %10 offen sc1\n\t"
                   "buffer_load_dwordx2 %5, %7, %8, %11 offen sc1\n.Lskip%=:\n\ts_waitcnt vmcnt(0)"
                   : "=&v"(pa[0]), "=&v"(pa[1]), "=&v"(pa[2]), "=&v"(pb[0]), "=&v"(pb[1]), "=&v"(pb[2])
                   : "v"(va), "v"(vb), "s"(c.sw), "s"(s0), "s"(s1), "s"(s2), "s"(c.t) : "memory", "scc");
      unsigned mx = 0;
#pragma unroll
      for (int k = 0; k < 3; ++k) {
#pragma unroll
        for (int i = 0; i < 4; ++i) mx = mx > pa[k][i] ? mx : pa[k][i];
        if (c.t == 0) { mx = mx > pb[k][0] ? mx : pb[k][0]; mx = mx > pb[k][1] ? mx : pb[k][1]; }
      }
      if (!__any(mx == ~0u)) break;
    }
    STAMP(29);
#pragma unroll
    for (int k = 0; k < 3; ++k)
#pragma unroll
      for (int i = 0; i < 2; ++i) {
        o0[2 * i] += __uint_as_float(pa[k][i] << 16); o0[2 * i + 1] += __uint_as_float(pa[k][i] & 0xffff0000u);
        o1[2 * i] += __uint_as_float(pa[k][2 + i] << 16); o1[2 * i + 1] += __uint_as_float(pa[k][2 + i] & 0xffff0000u);
      }
    unsigned short* op = (unsigned short*)out + (size_t)(128u * c.r + 32u * c.j + 16u * c.hf + (l & 15u)) * D_ + 144u * c.q;
    const unsigned e0 = cvtpk(__builtin_fmaf(o0[0], RKAP, __uint_as_float(bA[0] << 16)), __builtin_fmaf(o0[1], RKAP, __uint_as_float(bA[0] & 0xffff0000u)));
    const unsigned e1 = cvtpk(__builtin_fmaf(o0[2], RKAP, __uint_as_float(bA[1] << 16)), __builtin_fmaf(o0[3], RKAP, __uint_as_float(bA[1] & 0xffff0000u)));
    const unsigned f0 = cvtpk(__builtin_fmaf(o1[0], RKAP, __uint_as_float(bB[0] << 16)), __builtin_fmaf(o1[1], RKAP, __uint_as_float(bB[0] & 0xffff0000u)));
    const unsigned f1 = cvtpk(__builtin_fmaf(o1[2], RKAP, __uint_as_float(bB[1] << 16)), __builtin_fmaf(o1[3], RKAP, __uint_as_float(bB[1] & 0xffff0000u)));
    auto r0 = __builtin_amdgcn_permlane16_swap(e0, f0, false, false);
    auto r1 = __builtin_amdgcn_permlane16_swap(e1, f1, false, false);
    *(u32x4*)(op + 32u * c.t + 16u * ((l >> 4) & 1u) + 8u * (l >> 5)) = (u32x4){r0[0], r1[0], r0[1], r1[1]};
    if (c.t == 0) {
#pragma unroll
      for (int k = 0; k < 3; ++k)
#pragma unroll
        for (int i = 0; i < 2; ++i) { o2[2 * i] += __uint_as_float(pb[k][i] << 16); o2[2 * i + 1] += __uint_as_float(pb[k][i] & 0xffff0000u); }
      *(u32x2*)(op + 128u + 4u * (l >> 4)) =
          (u32x2){cvtpk(__builtin_fmaf(o2[0], RKAP, __uint_as_float(bC[0] << 16)), __builtin_fmaf(o2[1], RKAP, __uint_as_float(bC[0] & 0xffff0000u))),
                  cvtpk(__builtin_fmaf(o2[2], RKAP, __uint_as_float(bC[1] << 16)), __builtin_fmaf(o2[3], RKAP, __uint_as_float(bC[1] & 0xffff0000u)))};
    }
    if (c.w == 0 && c.j == 0 && c.q == 0 && l == 0)
      asm volatile("buffer_store_dword %0, off, %1, %2 sc1" ::"v"(c.tag), "s"(c.sw), "s"(WS_EPOCH + c.r * 128u));
  }
}

extern "C" void kernel_launch(void* const* d_in, const int* in_sizes, int n_in, void* d_out, int out_size, void* d_ws, size_t ws_size,
                              hipStream_t stream) {
  (void)in_sizes; (void)n_in; (void)out_size; (void)ws_size;
  (void)hipFuncSetAttribute((const void*)mixer_mlp_kernel, hipFuncAttributeMaxDynamicSharedMemorySize, LDS_BYTES);
  mixer_mlp_kernel<<<256, 512, LDS_BYTES, stream>>>(d_in[0], d_in[1], d_in[2], d_in[3], d_in[4], d_out, d_ws);
}
```

```cpp
#include <hip/hip_runtime.h>
#include <stdint.h>
#include <utility>

typedef __bf16 bf16x8 __attribute__((ext_vector_type(8)));
typedef __bf16 bf16x2 __attribute__((ext_vector_type(2)));
typedef short s16x4 __attribute__((ext_vector_type(4)));
typedef float f32x16 __attribute__((ext_vector_type(16)));
typedef float f32x4 __attribute__((ext_vector_type(4)));
typedef float f32x2 __attribute__((ext_vector_type(2)));
typedef unsigned u32x4 __attribute__((ext_vector_type(4)));
typedef unsigned u32x2 __attribute__((ext_vector_type(2)));
typedef __amdgpu_buffer_rsrc_t srd_t;
#define DI __device__ __forceinline__
#define LAS __attribute__((address_space(3)))
#define LDSP(T, a) ((LAS T*)(size_t)(a))

constexpr unsigned S_ = 2048, D_ = 576, F_ = 4608;
constexpr unsigned XROW = D_ * 2, W1ROW = F_ * 2, W2ROW = D_ * 2;
constexpr unsigned SLOT = 36864, RING = 4 * SLOT, SLICE = 28672;
constexpr unsigned L_B1 = RING, L_PAT = L_B1 + 1152, L_JUNK = L_PAT + 2304, L_EX = L_JUNK + 256, LDS_BYTES = L_EX + 8192;
static_assert(LDS_BYTES <= 163840, "LDS");
constexpr unsigned WS_EPOCH = 0, WS_FLAG = 4096, WS_Y = WS_FLAG + 256 * 8 * 128, WS_H = WS_Y + S_ * D_ * 2, H_BYTES = S_ * F_ * 2;
constexpr unsigned WS_P = WS_H + H_BYTES, P_BYTES = 16 * 4 * 4 * 4 * 9216;
constexpr unsigned WS_USED = WS_P + P_BYTES;
constexpr float KAP = 0.4686686930532063f, GAM = 4.91222953926424f, RKAP = 2.1337034344781243f;
#define KSW 10
#define PSLEEP 2
#define TOUCH 1
#define PREFH 1
#define HK2 2
#define CPREF 1
#define PREF 1
#define STAMP(i) do {} while (0)

DI unsigned rfl(unsigned x) { return __builtin_amdgcn_readfirstlane(x); }
DI s16x4 trr(unsigned a) { return __builtin_amdgcn_ds_read_tr16_b64_v4i16(LDSP(s16x4, a)); }
DI bf16x8 cat(s16x4 lo, s16x4 hi) { return __builtin_bit_cast(bf16x8, __builtin_shufflevector(lo, hi, 0, 1, 2, 3, 4, 5, 6, 7)); }
DI f32x4 ldsf4(unsigned a) { return *LDSP(f32x4, a); }
DI unsigned cvtpk(float lo, float hi) { f32x2 v = {lo, hi}; return __builtin_bit_cast(unsigned, __builtin_convertvector(v, bf16x2)); }
template <int CTRL> DI float dpp_add(float v) {
  return v + __builtin_bit_cast(float, __builtin_amdgcn_update_dpp(0, __builtin_bit_cast(int, v), CTRL, 0xf, 0xf, false));
}
#define MFMA32(a, b, c) __builtin_amdgcn_mfma_f32_32x32x16_bf16((a), (b), (c), 0, 0, 0)
#define MFMA16(a, b, c) __builtin_amdgcn_mfma_f32_16x16x32_bf16((a), (b), (c), 0, 0, 0)
DI void mfma32(f32x16& acc, bf16x8 a, bf16x8 b) { asm volatile("v_mfma_f32_32x32x16_bf16 %0, %1, %2, %0" : "+v"(acc) : "v"(a), "v"(b)); }
DI void mfma16(f32x4& acc, bf16x8 a, bf16x8 b) { asm volatile("v_mfma_f32_16x16x32_bf16 %0, %1, %2, %0" : "+v"(acc) : "v"(a), "v"(b)); }
DI void mfma16z(f32x4& acc, bf16x8 a, bf16x8 b) { asm volatile("v_mfma_f32_16x16x32_bf16 %0, %1, %2, 0" : "=&v"(acc) : "v"(a), "v"(b)); }
#define BARRIER_LGKM() asm volatile("s_waitcnt lgkmcnt(0)\n\ts_barrier" ::: "memory")
#define GAP() __builtin_amdgcn_sched_barrier(0)
#define PIN(x) asm volatile("" : "+v"(x))

struct Ctx {
  srd_t s1, s2, sw;
  unsigned w, t, hf, r, j, q;
  unsigned freg2, f82;
  unsigned tag, sdma, sw1, ldst, hq[4], h8[2], sw2[2], sw28, fown;
  u32x4 vpat;
  unsigned vpat4;
  unsigned wb;
  unsigned vhld, vhst, tb, pref, tch;
  float gz[2], gm, gz8, gm8;
  unsigned own[4][8], hv8[8];
  bf16x8 X[36];
  bf16x8 F[4];
  f32x16 acc[2];
  float a8[8];
  f32x4 acc2[2][9];
  u32x4 Hb[8][2];
  unsigned pollv;
};
DI unsigned lane_id() {
  unsigned l;
  asm volatile("v_mbcnt_lo_u32_b32 %0, -1, 0\n\tv_mbcnt_hi_u32_b32 %0, -1, %0" : "=v"(l));
  return l;
}
DI unsigned opq(unsigned v) { PIN(v); return v; }

constexpr int pair_of(int h) { return h < 8 ? 0 : (h - 7) / 2; }
constexpr int ph_of(int h) { return h < 8 ? h - 6 : (h - 7) % 2; }
constexpr bool own_unit(int n) { return n == 0 || n == 6 || n == 10 || n == 14; }
constexpr int hload_at(int h, int k) {
  if (h == 8 || h == 10 || h == 12) return k == 0 ? 4 * (h - 8) + 14 : k == HK2 ? 4 * (h - 8) + 15 : -1;
  const int slot = k == 0 ? 0 : k == 2 ? 1 : k == 10 ? 2 : k == 12 ? 3 : k == 1 ? 4 : k == 3 ? 5 : -1;
  if (slot < 0) return -1;
  if (h == 5) return slot < 4 ? 2 + slot : -1;
  if (h == 6) return k < 4 ? 6 + k : 8 + slot;
  if (h < 8 || h > 13 || slot > 3) return -1;
  return 12 + 4 * (h - 8) + slot;
}
constexpr int dma_at(int h, int k) {
  if (h == 0) return (k < 16 && (k & 1) == 0) ? (k < 8 ? 2 * 8 + (k + 2) / 2 : 3 * 8 + (k - 8) / 2) : (k == 15 ? 3 * 8 + 4 : -1);
  if (h + 3 > 14) return -1;
  const int i = h < 4 ? (k == -2 ? 0 : k == 2 ? 1 : k == 6 ? 2 : k == 10 ? 3 : k == 13 ? 4 : -1)
                      : (k == -2 ? 0 : k == 4 ? 1 : k == 6 ? 2 : k == 8 ? 3 : k == 14 ? 4 : -1);
  return i < 0 ? -1 : (h + 3) * 8 + i;
}
constexpr int poll_k(int h) { return h == 4 ? 5 : (h == 7 || h == 9 || h == 10 || h == 11) ? -1 : -9; }
constexpr bool poll_at(int h, int k) { return k == poll_k(h); }
constexpr int flag_at(int h) { return (h == 3 || h == 5 || h == 7 || h == 9) ? (h + 1) / 2 : 0; }
constexpr bool hst_at(int h, int k) { return (h < 8 && (h & 1) && (k == 0 || k == 15)) || (h == 1 && k == 12); }
constexpr bool pref_at(int h, int k) { return PREF && h < 6 && k == 15; }
constexpr bool touch_at(int h, int k) { return TOUCH && (h == 2 || h == 4 || h == 6 || h == 8) && k == 13; }
constexpr bool mark_at(int h, int k) { return (h == 2 && (k == 4 || k == 12)) || (h == 4 && k == 2); }
constexpr int nops(int h, int k, int n) {
  return (hload_at(h, k) >= 0 ? 1 : 0) + (hst_at(h, k) ? 1 : 0) + (mark_at(h, k) ? 1 : 0) +
         (dma_at(h, k) >= 0 && (dma_at(h, k) & 7) < n ? 1 : 0);
}
constexpr int between(int h0, int k0, int h1, int k1, int n) {
  int s = 0;
  for (int h = h0; h <= h1; ++h)
    for (int k = -2; k < 16; ++k)
      if ((h > h0 || k > k0) && (h < h1 || k < k1)) s += nops(h, k, n);
  return s;
}
constexpr int cmin(int a, int b) { return a < b ? a : b; }

DI unsigned fstart(unsigned qq, unsigned ch) { return 32u * (9u * qq + ch + (qq & 1u) * (1u - 9u * (ch >> 3))); }

DI void dma16(unsigned m0v, unsigned voff, srd_t srd, unsigned soff) {
  asm volatile("s_mov_b32 m0, %0\n\ts_nop 0\n\tbuffer_load_dwordx4 %1, %2, %3 offen lds" ::"s"(m0v), "v"(voff), "s"(srd), "s"(soff));
}
template <int HS, int I> DI void issue_piece(Ctx& c) {
  const srd_t sd = HS < 6 ? c.s1 : c.s2;
  if constexpr (I == 0) {
    if constexpr (HS < 6) c.sdma = c.sw1 + ((HS & 1) * 288u * W1ROW + ((HS >> 1) + 1) * 128u);
    else c.sdma = HS == 8 ? c.sw28 : c.sw2[ph_of(HS)] + pair_of(HS) * 64u * W2ROW;
    dma16((HS & 3) * SLOT + c.ldst, c.vpat[0], sd, c.sdma);
  } else if constexpr (I < 4) {
    asm volatile("buffer_load_dwordx4 %0, %1, %2 offen offset:%3 lds" ::"v"(c.vpat[I]), "s"(sd), "s"(c.sdma), "i"(1024 * I));
  } else {
    asm volatile("s_cmp_lg_u32 %4, 0\n\ts_cbranch_scc1 .Lskip%=\n\ts_mov_b32 m0, %0\n\ts_nop 0\n\tbuffer_load_dwordx4 %1, %2, %3 offen lds\n.Lskip%=:"
                 ::"s"((HS & 3) * SLOT + 4096u + c.ldst), "v"(c.vpat4), "s"(sd), "s"(c.sdma), "s"(c.hf) : "scc");
  }
}
template <int HS> DI void issue_slot(Ctx& c) {
  issue_piece<HS, 0>(c); issue_piece<HS, 1>(c); issue_piece<HS, 2>(c); issue_piece<HS, 3>(c); issue_piece<HS, 4>(c);
}
template <int N, int U> DI void issue_hload(Ctx& c) {
  constexpr int h = 6 + N / 2;
  const unsigned so = h == 8 ? c.h8[N & 1] : c.hq[2 * ph_of(h) + (N & 1)] + pair_of(h) * 16384u;
  asm volatile("buffer_load_dwordx4 %0, %1, %2, %3 offen offset:%4 sc1" : "=v"(c.Hb[N & 7][U]) : "v"(c.vhld), "s"(c.sw), "s"(so), "i"(1024 * U));
}
DI unsigned poll_off(const Ctx& c, bool dest) {
  const unsigned l = lane_id(), dp = (l & 3u) ? (l & 3u) : 1u;
  return WS_FLAG + ((dest ? (c.r * 4u + ((c.j + dp) & 3u)) * 4u + c.q : (c.r * 4u + c.j) * 4u + ((c.q + dp) & 3u)) << 10);
}
template <int EV> DI void store_flag(Ctx& c) {
  asm volatile("s_cmp_lg_u32 %3, 0\n\ts_cbranch_scc1 .Lskip%=\n\ts_mov_b64 exec, 1\n\t"
               "buffer_store_dword %0, off, %1, %2 offset:%4 sc1\n\ts_mov_b64 exec, -1\n.Lskip%=:"
               ::"v"(c.tag), "s"(c.sw), "s"(c.fown), "s"(c.w), "i"(EV * 128) : "scc");
}
template <int EV> DI void issue_poll(Ctx& c, unsigned& pv, unsigned voff) {
  asm volatile("s_cmp_lg_u32 %3, 7\n\ts_cbranch_scc1 .Lskip%=\n\tbuffer_load_dword %0, %1, %2, 0 offen offset:%4 sc1\n.Lskip%=:"
               : "+v"(pv) : "v"(voff), "s"(c.sw), "s"(c.w), "i"(EV * 128) : "scc");
}
template <int EV, int N> DI void check_poll(Ctx& c, unsigned& pv, bool dest) {
  if (c.w == 7) {
    asm volatile("s_waitcnt vmcnt(%1)" : "+v"(pv) : "i"(N) : "memory");
    if (__builtin_expect(__builtin_amdgcn_ballot_w64(pv != c.tag) != 0, 0)) {
      const unsigned voff = poll_off(c, dest);
      for (unsigned spin = 0; spin < (1u << 16); ++spin) {
        unsigned v;
        asm volatile("s_sleep 1\n\tbuffer_load_dword %0, %1, %2, 0 offen offset:%3 sc1\n\ts_waitcnt vmcnt(0)" : "=&v"(v) : "v"(voff), "s"(c.sw), "i"(EV * 128) : "memory");
        if (__builtin_amdgcn_ballot_w64(v != c.tag) == 0) break;
      }
    }
  }
}
template <int P> DI void store_h(Ctx& c, const unsigned (&hv)[8], unsigned so) {
  const u32x4 v = {hv[4 * P], hv[4 * P + 1], hv[4 * P + 2], hv[4 * P + 3]};
  asm volatile("buffer_store_dwordx4 %0, %1, %2, %3 offen sc1\n\ts_nop 1" ::"v"(v), "v"(c.vhst), "s"(c.sw), "s"(so));
}
template <int N0, int N1> DI void wait_vm(const Ctx&) {
  static_assert(N1 <= N0 && N1 >= 0 && N0 < 64, "vmcnt");
  asm volatile("s_waitcnt vmcnt(%0)" ::"i"(N1) : "memory");
}

template <int H> DI void set_bases(Ctx& c) {
  constexpr unsigned d = (H & 3) ? SLOT : 0u - 3u * SLOT;
  if constexpr (H == 6) {
    const unsigned l = lane_id(), g = l >> 4;
    c.wb = opq(2u * SLOT + (16u * (g >> 1) + 4u * (g & 1u) + ((l >> 2) & 3u)) * 288u + 8u * (l & 3u) + c.hf * 18432u);
  } else if constexpr (H > 0) c.wb = opq(c.wb + d);
}
template <int H, int S> DI void rd(Ctx& c) {
  constexpr unsigned o = H < 6 ? 2048u * S : 9216u * (S / 9) + 32u * (S % 9), e = H < 6 ? 512u : 2304u;
  c.F[(H * 18 + S) & 3] = cat(trr(c.wb + o), trr(c.wb + o + e));
}
template <int H, int S> DI void mm(Ctx& c) {
  const bf16x8 a = c.F[(H * 18 + S) & 3];
  if constexpr (H < 6) {
    constexpr int B = ((H >> 1) + 1) & 1;
    mfma32(c.acc[B], a, c.X[18 * (H & 1) + S]);
  } else {
    constexpr int N = 2 * (H - 6) + S / 9, dt = S % 9;
    u32x4 b0 = c.Hb[N & 7][0], b1 = c.Hb[N & 7][1];
    if constexpr (own_unit(N)) {
      const unsigned(&o)[8] = c.own[pair_of(H)];
      b0 = (u32x4){o[0], o[1], o[2], o[3]}; b1 = (u32x4){o[4], o[5], o[6], o[7]};
    }
    if constexpr (N == 0) {
      mfma16z(c.acc2[0][dt], a, __builtin_bit_cast(bf16x8, b0));
      mfma16z(c.acc2[1][dt], a, __builtin_bit_cast(bf16x8, b1));
    } else {
      mfma16(c.acc2[0][dt], a, __builtin_bit_cast(bf16x8, b0));
      mfma16(c.acc2[1][dt], a, __builtin_bit_cast(bf16x8, b1));
    }
  }
}
template <int Q> DI void bias_quad(f32x16& acc, unsigned a) {
  const f32x4 v = ldsf4(a + (Q >> 1) * 64u + (Q & 1) * 16u);
#pragma unroll
  for (int i = 0; i < 4; ++i) acc[4 * Q + i] = v[i];
}
template <int E, int ST> DI void gelu_stage(float s, float& z, float& m0, unsigned (&hv)[8]) {
  if constexpr (ST == 0) { z = s * __builtin_fmaf(s, s, GAM); PIN(z); }
  if constexpr (ST == 1) { z = __builtin_amdgcn_exp2f(-z); PIN(z); }
  if constexpr (ST == 2) { z = __builtin_amdgcn_rcpf(1.0f + z); PIN(z); }
  if constexpr (ST == 3) {
    if constexpr ((E & 1) == 0) { m0 = s * z; PIN(m0); }
    else { float m = s * z, mm0 = m0; PIN(m); PIN(mm0); hv[E >> 1] = cvtpk(mm0, m); PIN(hv[E >> 1]); }
  }
}
constexpr int gstart(int e) { return 1 + (e * 29) / 15; }
constexpr int gelem(int g, int st) {
  for (int e = 0; e < 16; ++e) if (gstart(e) + st == g) return e;
  return -1;
}
template <int B, int G, int ST> DI void gelu_part(Ctx& c) {
  constexpr int e = gelem(G, ST);
  if constexpr (e >= 0) gelu_stage<e, ST>(c.acc[B & 1][e], c.gz[e & 1], c.gm, c.own[B]);
}
template <int B, int G> DI void gelu_gap(Ctx& c) {
  if constexpr (G == 1) asm volatile("s_nop 7" : "+v"(c.acc[B & 1]));
  gelu_part<B, G, 3>(c); gelu_part<B, G, 2>(c); gelu_part<B, G, 1>(c); gelu_part<B, G, 0>(c);
  if constexpr (B < 2) {
    if constexpr (G == 27) { bias_quad<0>(c.acc[B & 1], c.tb + (B + 2) * 256u); bias_quad<1>(c.acc[B & 1], c.tb + (B + 2) * 256u); }
    if constexpr (G == 31) bias_quad<2>(c.acc[B & 1], c.tb + (B + 2) * 256u);
    if constexpr (G == 33) bias_quad<3>(c.acc[B & 1], c.tb + (B + 2) * 256u);
  }
}

template <int H0, int K0, int H1, int K1> DI void wait_after(const Ctx& c) {
  wait_vm<between(H0, K0, H1, K1, 5), between(H0, K0, H1, K1, 4)>(c);
}
template <int H, int K> DI void side(Ctx& c) {
  if constexpr (poll_at(H, K)) {
    if constexpr (H == 10) issue_poll<4>(c, c.pollv, poll_off(c, true));
    else issue_poll<(H == 4 ? 2 : (H - 1) / 2)>(c, c.pollv, c.pref);
  }
  if constexpr (K == -1 && flag_at(H)) store_flag<flag_at(H)>(c);
  if constexpr (H == 4 && K == 15) c.vhld = lane_id() * 16u;
  if constexpr (H == 3 && K == 11) { c.pref = poll_off(c, false); PIN(c.pref); }
  if constexpr (hload_at(H, K) >= 0) issue_hload<(hload_at(H, K) >> 1), (hload_at(H, K) & 1)>(c);
  if constexpr (pref_at(H, K)) {
    if (c.w == (unsigned)H) {
      const unsigned m = 64u * H + lane_id(), ln = ((c.r & 7u) * 4u + c.q) + 32u * (m < 324u ? m : 323u), rr = ln / 9u;
      asm volatile("s_mov_b32 m0, %0\n\ts_nop 0\n\tbuffer_load_dword %1, %2, %3 offen lds" ::"s"(L_JUNK),
                   "v"((288u * (rr & 3u) + (rr >> 2)) * W2ROW + (ln - rr * 9u) * 128u), "s"(c.s2), "s"(c.j * 1152u * W2ROW));
    }
  }
  if constexpr (mark_at(H, K)) {
    constexpr unsigned k = H == 4 ? 2u : K == 4 ? 0u : 1u;
    const u32x4 ones = {~0u, ~0u, ~0u, ~0u};
    const unsigned so = WS_P + (((c.r * 4u + c.q) * 4u + c.j) * 4u + ((c.j + 1u + k) & 3u)) * 9216u, vo = lane_id() * 16u;
    asm volatile("buffer_store_dwordx4 %0, %1, %2, %3 offen sc1\n\ts_cmp_lg_u32 %4, %5\n\ts_cbranch_scc1 .Lskip%=\n\t"
                 "buffer_store_dwordx4 %0, %1, %2, %6 offen sc1\n.Lskip%=:\n\ts_nop 1"
                 ::"v"(ones), "v"(vo), "s"(c.sw), "s"(so + c.w * 1024u), "s"(c.w), "i"(k), "s"(so + 8192u) : "scc");
  }
  if constexpr (dma_at(H, K) >= 0) issue_piece<(dma_at(H, K) >> 3), (dma_at(H, K) & 7)>(c);
  if constexpr (H == 2 && K == 14) {
    const unsigned l = lane_id();
#pragma unroll
    for (int i = 0; i < 4; ++i) c.vpat[i] = *LDSP(unsigned, L_PAT + (c.hf * 5u + i) * 256u + l * 4u);
    c.vpat4 = *LDSP(unsigned, L_PAT + 1024u + l * 4u);
  }
  if constexpr (H == 0 && K == -2) {
    if (c.hf) {
      const unsigned l = lane_id();
      const f32x4 e0 = ldsf4(L_EX + c.t * 2048u + l * 16u), e1 = ldsf4(L_EX + c.t * 2048u + 1024u + l * 16u);
#pragma unroll
      for (int i = 0; i < 4; ++i) { c.a8[i] = e0[i]; c.a8[4 + i] = e1[i]; }
    }
  }
  if constexpr (H < 8) gelu_gap<(H >> 1), K + 18 * (H & 1)>(c);
  if constexpr (H < 2) {
    constexpr int G = K + 18 * H - 1;
    if constexpr (G >= 0 && G < 28) gelu_stage<G / 4, (G & 3)>(c.a8[G / 4], c.gz8, c.gm8, c.hv8);
    if constexpr (G == 28) { gelu_stage<7, 0>(c.a8[7], c.gz8, c.gm8, c.hv8); gelu_stage<7, 1>(c.a8[7], c.gz8, c.gm8, c.hv8); }
    if constexpr (G == 29) { gelu_stage<7, 2>(c.a8[7], c.gz8, c.gm8, c.hv8); gelu_stage<7, 3>(c.a8[7], c.gz8, c.gm8, c.hv8); }
  }
  if constexpr (hst_at(H, K)) {
    if constexpr (K == 12) store_h<0>(c, c.hv8, c.hq[0] + 65536u - c.hf * 7936u);
    else store_h<(K == 0 ? 0 : 1)>(c, c.own[H >> 1], c.hq[0] + (H >> 1) * 16384u + (K == 0 ? 0u : 256u));
  }
  if constexpr (H >= 2 && H <= 8 && (H & 1) == 0 && (K == 1 || K == 3 || K == 5 || K == 7)) {
    unsigned(&o)[8] = c.own[(H >> 1) - 1];
    auto rr = __builtin_amdgcn_permlane16_swap(o[K >> 1], o[4 + (K >> 1)], false, false);
    o[K >> 1] = rr[0]; o[4 + (K >> 1)] = rr[1];
    PIN(o[K >> 1]); PIN(o[4 + (K >> 1)]);
  }
}
constexpr int hpos(int n) {
  for (int h = 4; h < 14; ++h) for (int k = -2; k < 16; ++k) if (hload_at(h, k) == 2 * n + 1) return h * 32 + k + 2;
  return 0;
}
constexpr int cmax(int a, int b) { return a > b ? a : b; }
template <int H> DI void end_hs(Ctx& c) {
  if constexpr (H < 14) {
    constexpr int p0 = H == 0 ? -1 : H == 1 ? 8 : (H - 2) * 32 + 17;
    constexpr int p1 = (H >= 5 && !own_unit(2 * (H - 5))) ? hpos(2 * (H - 5)) : 0, p2 = 0, p3 = flag_at(H + 1) ? (H - 1) * 32 + 17 : 0;
    constexpr int p = H == 0 ? -1 : cmax(cmax(p0, p1), cmax(p2, p3));
    wait_after<(p < 0 ? 0 : p / 32), (p < 0 ? -3 : p % 32 - 2), H, 16>(c);
    if constexpr (poll_k(H) > -9) check_poll<(H == 10 ? 4 : H == 4 ? 2 : (H - 1) / 2), between(H, poll_k(H), H, 16, 4)>(c, c.pollv, H == 10);
    BARRIER_LGKM();
  }
  if constexpr (H >= 5 && H < 14 && !own_unit(2 * (H - 5))) { PIN(c.Hb[(2 * (H - 5)) & 7][0]); PIN(c.Hb[(2 * (H - 5)) & 7][1]); }
  GAP();
}
template <int H> DI void mid_wait(Ctx& c) {
  if constexpr (H >= 6) {
    constexpr int p = hpos(2 * (H - 6) + 1);
    wait_after<p / 32, p % 32 - 2, H, 9>(c);
    PIN(c.Hb[(2 * (H - 6) + 1) & 7][0]); PIN(c.Hb[(2 * (H - 6) + 1) & 7][1]);
  }
}
#define GST(H, i) do {} while (0)
template <int H, int K> DI void step(Ctx& c) {
  GST(H, K + 2);
  if constexpr (K == 9) mid_wait<H>(c);
  if constexpr (K == KSW) asm volatile("s_setprio 0");
  mm<H, K>(c);
  if constexpr (K + 4 < 18) rd<H, K + 4>(c);
  side<H, K>(c);
  GAP();
}
template <int H, int... Ks> DI void steps(Ctx& c, std::integer_sequence<int, Ks...>) { (step<H, Ks>(c), ...); }
DI void prio_young(const Ctx& c) {
  asm volatile("s_cmp_eq_u32 %0, 0\n\ts_cbranch_scc1 .Lskip%=\n\ts_setprio 1\n.Lskip%=:" ::"s"(c.hf) : "scc");
}
template <int H> DI void half_stage(Ctx& c) {
  prio_young(c);
  set_bases<H>(c);
  rd<H, 0>(c); rd<H, 1>(c);
  GAP();
  GST(H, 0);
  if constexpr (H > 0) mm<H - 1, 16>(c); else mfma32(c.acc[0], c.F[2], c.X[34]);
  rd<H, 2>(c);
  side<H, -2>(c);
  GAP();
  GST(H, 1);
  if constexpr (H > 0) mm<H - 1, 17>(c); else mfma32(c.acc[0], c.F[3], c.X[35]);
  rd<H, 3>(c);
  side<H, -1>(c);
  GAP();
  steps<H>(c, std::make_integer_sequence<int, 16>{});
  if constexpr (H == 14) { mm<14, 16>(c); mm<14, 17>(c); GAP(); asm volatile("s_nop 15" ::: "memory"); GAP(); }
  GST(H, 18);
  end_hs<H>(c);
  GST(H, 20);
}

struct B0 {
  unsigned vy, vw1, vc8, xa[4], l8, l80, lx, lw, junk;
  bf16x8 G[4];
  f32x16 accB;
};
template <int S> DI void issue_y(Ctx& c, B0& b) {
  const unsigned dst = (S % 5) * SLICE + c.w * 1024u, so = WS_Y + c.r * 128u * XROW + S * 128u;
  asm volatile("s_mov_b32 m0, %0\n\ts_nop 0\n\tbuffer_load_dwordx4 %1, %2, %3 offen sc1 lds" ::"s"(dst), "v"(b.vy), "s"(c.sw), "s"(so));
  asm volatile("s_mov_b32 m0, %0\n\ts_nop 0\n\tbuffer_load_dwordx4 %1, %2, %3 offen sc1 lds" ::"s"(dst + 8192u), "v"(b.vy), "s"(c.sw), "s"(so + 64u * XROW));
}
template <int HF, int S> DI void issue_w(Ctx& c, B0& b) {
  const unsigned dst = (S % 5) * SLICE, kk = S * 64u;
  dma16(dst + c.w * 1024u + 16384u, b.vw1, c.s1, (kk + 8u * c.w) * W1ROW + c.freg2);
  if constexpr (HF == 0) dma16(dst + c.t * 1024u + 24576u, b.vc8, c.s1, (kk + 16u * c.t) * W1ROW + c.f82);
}
template <int HF, int S, int K> DI void rd0(Ctx& c, B0& b) {
  c.X[S * 4 + K] = *LDSP(bf16x8, b.xa[K]);
  c.F[K] = cat(trr(c.wb + 2048u * K), trr(c.wb + 2048u * K + 512u));
  if constexpr (HF == 0) b.G[K] = cat(trr(b.l8 + 1024u * K), trr(b.l8 + 1024u * K + 256u));
}
template <int S, int K> DI void mmA(Ctx& c) { mfma32(c.acc[0], c.F[K], c.X[S * 4 + K]); }
template <int HF, int S, int K> DI void mmB(Ctx& c, B0& b) {
  if constexpr (HF == 0) mfma32(b.accB, b.G[K], c.X[S * 4 + K]);
}
template <int HF, int S> DI void bases0(Ctx& c, B0& b) {
  constexpr unsigned base = (S % 5) * SLICE;
#pragma unroll
  for (int k = 0; k < 4; ++k) b.xa[k] = opq(base + (b.lx ^ (unsigned)(k << 5)));
  c.wb = opq(base + 16384u + b.lw);
  if constexpr (HF == 0) b.l8 = opq(base + 24576u + b.l80);
}
template <int HF, int S> DI void slice(Ctx& c, B0& b) {
  constexpr int n0 = HF ? 3 : 4, n = HF ? 4 : 5, M = 0;
  constexpr int N = S == 0 ? 2 : S == 1 ? 4 : S == 2 ? 4 + n0 : S == 3 ? 2 + 2 * n0 : S <= 5 ? 3 * n0 : S == 6 ? 2 * n0 + M : S == 7 ? n0 + M : M + n;
  asm volatile("s_waitcnt vmcnt(%0) lgkmcnt(0)\n\ts_barrier" ::"i"(N) : "memory");
  STAMP(3 + S);
  GAP();
  if constexpr (S > 0) mmA<S - 1, 2>(c);
  rd0<HF, S, 0>(c, b);
  GAP();
  if constexpr (S > 0) mmB<HF, S - 1, 2>(c, b);
  rd0<HF, S, 1>(c, b);
  GAP();
  if constexpr (S > 0) mmA<S - 1, 3>(c);
  if constexpr (S <= 1) issue_y<2 * S + 2>(c, b);
  if constexpr (S >= 2 && S <= 4) issue_y<S + 4>(c, b);
  if constexpr (S == 7) issue_slot<0>(c);
  if constexpr (S == 8) issue_slot<1>(c);
  GAP();
  if constexpr (S > 0) mmB<HF, S - 1, 3>(c, b);
  rd0<HF, S, 2>(c, b);
  GAP();
  mmA<S, 0>(c);
  rd0<HF, S, 3>(c, b);
  GAP();
  mmB<HF, S, 0>(c, b);
  if constexpr (S <= 1) issue_y<2 * S + 3>(c, b);
  if constexpr (S >= 1 && S <= 4) issue_w<HF, S + 4>(c, b);
  if constexpr (S == 6) {
#pragma unroll
    for (int i = 0; i < 4; ++i) c.vpat[i] = b.vw1 + i * (8u * W1ROW - 1024u);
    c.vpat4 = b.vw1 + 32u * W1ROW;
    bias_quad<0>(c.acc[1], c.tb + 256u); bias_quad<1>(c.acc[1], c.tb + 256u); bias_quad<2>(c.acc[1], c.tb + 256u); bias_quad<3>(c.acc[1], c.tb + 256u);
  }
  if constexpr (S < 8) bases0<HF, S + 1>(c, b);
  GAP();
  mmA<S, 1>(c);
  GAP();
  mmB<HF, S, 1>(c, b);
  GAP();
}
DI float wave_sum(float v) {
  v = dpp_add<0x121>(v); v = dpp_add<0x122>(v); v = dpp_add<0x124>(v); v = dpp_add<0x128>(v);
  auto a = __builtin_amdgcn_permlane16_swap(__float_as_uint(v), __float_as_uint(v), false, false);
  v = __uint_as_float(a[0]) + __uint_as_float(a[1]);
  auto b = __builtin_amdgcn_permlane32_swap(__float_as_uint(v), __float_as_uint(v), false, false);
  return __uint_as_float(b[0]) + __uint_as_float(b[1]);
}
template <int HF> DI void block0(Ctx& c, const srd_t sx, const srd_t sb1) {
  B0 b;
  const unsigned l = threadIdx.x & 63u, g = l >> 4, qq = (l >> 2) & 3u, pp = l & 3u, h = l >> 5;
  const unsigned row = (c.r * 128u + (c.j * 4u + c.q) * 8u + c.w) * XROW, vb = 1024u + (l & 31u) * 4u;
  u32x4 xa; unsigned xb, b1raw, epoch;
  asm volatile("buffer_load_dwordx4 %0, %1, %2, %3 offen" : "=v"(xa) : "v"(l * 16u), "s"(sx), "s"(row));
  asm volatile("buffer_load_dword %0, %1, %2, %3 offen" : "=v"(xb) : "v"(vb), "s"(sx), "s"(row));
  asm volatile("buffer_load_dword %0, off, %1, %2 sc1" : "=v"(epoch) : "s"(c.sw), "s"(WS_EPOCH + c.r * 128u));
  b.vw1 = (l >> 3) * W1ROW + (((l & 7u) ^ ((g & 1u) << 2)) << 4);
  b.vc8 = (l >> 2) * W1ROW + (pp << 4);
  issue_w<HF, 0>(c, b);
  {
    unsigned long long pc; unsigned len, o, junk;
    asm volatile("s_getpc_b64 %0\n.Lpf%=:\n\ts_mov_b32 %1, mixer_code_end-.Lpf%=-4\n\t"
                 "s_min_u32 %2, %4, %1\n\ts_load_dword %3, %0, %2\n\t"
                 "s_add_u32 %2, %4, 0x2000\n\ts_min_u32 %2, %2, %1\n\ts_load_dword %3, %0, %2\n\t"
                 "s_add_u32 %2, %4, 0x4000\n\ts_min_u32 %2, %2, %1\n\ts_load_dword %3, %0, %2\n\t"
                 "s_add_u32 %2, %4, 0x6000\n\ts_min_u32 %2, %2, %1\n\ts_load_dword %3, %0, %2"
                 : "=&s"(pc), "=&s"(len), "=&s"(o), "=&s"(b.junk) : "s"((((c.r & 1u) * 4u + c.q) + 8u * c.w) * 128u) : "scc");
    (void)junk;
  }
  GAP();
  asm volatile("s_waitcnt vmcnt(%2)" : "+v"(xa), "+v"(xb) : "i"(HF ? 2 : 3) : "memory");
  {
    float v[10];
#pragma unroll
    for (int i = 0; i < 4; ++i) { v[2 * i] = __uint_as_float(xa[i] << 16); v[2 * i + 1] = __uint_as_float(xa[i] & 0xffff0000u); }
    v[8] = __uint_as_float(xb << 16); v[9] = __uint_as_float(xb & 0xffff0000u);
    const float wt = h ? 0.f : 1.f;
    const float mean = wave_sum(((v[0] + v[1]) + (v[2] + v[3])) + ((v[4] + v[5]) + (v[6] + v[7])) + wt * (v[8] + v[9])) * (1.0f / 576.0f);
#pragma unroll
    for (int i = 0; i < 10; ++i) v[i] -= mean;
    float ss = wt * (v[8] * v[8] + v[9] * v[9]);
#pragma unroll
    for (int i = 0; i < 8; ++i) ss = __builtin_fmaf(v[i], v[i], ss);
    const float rs = KAP * __builtin_amdgcn_rsqf(wave_sum(ss) * (1.0f / 576.0f) + 1e-5f);
    const u32x4 ya = {cvtpk(v[0] * rs, v[1] * rs), cvtpk(v[2] * rs, v[3] * rs), cvtpk(v[4] * rs, v[5] * rs), cvtpk(v[6] * rs, v[7] * rs)};
    const unsigned yb = cvtpk(v[8] * rs, v[9] * rs);
    asm volatile("buffer_store_dwordx4 %0, %1, %2, %3 offen sc1\n\ts_nop 1" ::"v"(ya), "v"(l * 16u), "s"(c.sw), "s"(WS_Y + row));
    asm volatile("buffer_store_dword %0, %1, %2, %3 offen sc1" ::"v"(yb), "v"(vb), "s"(c.sw), "s"(WS_Y + row));
  }
  GAP();
  STAMP(1);
  b.vy = ((l >> 3) + 8u * c.w) * XROW + (((l & 7u) ^ ((g + 4u * c.w) & 7u)) << 4);
  b.lw = (8u * h + qq) * 128u + (((4u * HF + 2u * (g & 1u) + (pp & 1u)) ^ (((qq >> 1) & 1u) << 2)) << 4) + 8u * (pp >> 1);
  const unsigned xrow = 32u * c.t + (l & 31u);
  b.lx = xrow * 128u + ((h ^ ((xrow >> 1) & 7u)) << 4);
  b.l80 = (8u * h + qq) * 64u + 32u * (g & 1u) + 16u * (pp & 1u) + 8u * (pp >> 1);
  c.tb = L_B1 + HF * 128u + h * 32u;
  c.vhst = (l & 16u) * 64u + (l & 15u) * 16u + (l & 32u) * 16u;
  bases0<HF, 0>(c, b);
  GAP();
  asm volatile("s_waitcnt vmcnt(0)\n\ts_barrier" : "+v"(epoch)::"memory");
  c.tag = rfl(epoch) + 1u;
  STAMP(31);
  store_flag<1>(c);
  {
    const unsigned tid = threadIdx.x < 288u ? threadIdx.x : 287u;
    const unsigned off = c.freg2 + tid * 2u + (tid >> 8) * (c.f82 - c.freg2 - 512u);
    asm volatile("buffer_load_ushort %0, %1, %2, 0 offen" : "=v"(b1raw) : "v"(off), "s"(sb1));
  }
  issue_w<HF, 1>(c, b); issue_w<HF, 2>(c, b); issue_w<HF, 3>(c, b); issue_w<HF, 4>(c, b);
#pragma unroll
  for (int k = 0; k < 2; ++k) {
    const unsigned i = k ? 8u : c.w, nn = 64u * i + l, rho = nn / 18u, chn = nn - rho * 18u;
    const unsigned f = (rho & 3u) | ((rho & 4u) << 2) | ((rho & 24u) >> 1);
    const unsigned rel = i < 5u ? i : i - 5u;
    *LDSP(unsigned, L_PAT + i * 256u + l * 4u) = f * W2ROW + chn * 16u - (rel < 4u ? rel * 1024u : 0u);
  }
  asm volatile("s_waitcnt vmcnt(%1)" : "+v"(b1raw) : "i"(HF ? 4 : 8) : "memory");
  if (threadIdx.x < 288u) *LDSP(float, L_B1 + threadIdx.x * 4u) = __uint_as_float(b1raw << 16) * KAP;
  if (HF == 1 && c.w == 7) {
    const unsigned voff = WS_FLAG + 128u + ((c.r * 16u + (l & 15u)) << 10);
    for (unsigned spin = 0; spin < (1u << 16); ++spin) {
      unsigned v;
      asm volatile("s_sleep %3\n\tbuffer_load_dword %0, %1, %2, 0 offen sc1\n\ts_waitcnt vmcnt(0)" : "=&v"(v) : "v"(voff), "s"(c.sw), "i"(PSLEEP) : "memory");
      if (__builtin_amdgcn_ballot_w64(v != c.tag) == 0) break;
    }
  }
  BARRIER_LGKM();
  asm volatile("" ::"s"(b.junk));
  STAMP(2);
  issue_y<0>(c, b); issue_y<1>(c, b);
  bias_quad<0>(c.acc[0], c.tb); bias_quad<1>(c.acc[0], c.tb); bias_quad<2>(c.acc[0], c.tb); bias_quad<3>(c.acc[0], c.tb);
  if constexpr (HF == 0) { bias_quad<0>(b.accB, c.tb + 1024u); bias_quad<1>(b.accB, c.tb + 1024u); bias_quad<2>(b.accB, c.tb + 1024u); bias_quad<3>(b.accB, c.tb + 1024u); }
  slice<HF, 0>(c, b);
  slice<HF, 1>(c, b); slice<HF, 2>(c, b); slice<HF, 3>(c, b); slice<HF, 4>(c, b);
  slice<HF, 5>(c, b); slice<HF, 6>(c, b); slice<HF, 7>(c, b); slice<HF, 8>(c, b);
  mmB<HF, 8, 2>(c, b); mmB<HF, 8, 3>(c, b);
  asm volatile("s_nop 15" : "+v"(b.accB));
  c.wb = opq(b.lw);
  if constexpr (HF == 0) {
    const unsigned ll = lane_id();
    f32x4 e0, e1;
#pragma unroll
    for (int i = 0; i < 4; ++i) { e0[i] = b.accB[8 + i]; e1[i] = b.accB[12 + i]; }
    *LDSP(f32x4, L_EX + c.t * 2048u + ll * 16u) = e0;
    *LDSP(f32x4, L_EX + c.t * 2048u + 1024u + ll * 16u) = e1;
#pragma unroll
    for (int i = 0; i < 8; ++i) c.a8[i] = b.accB[i];
  } else {
#pragma unroll
    for (int i = 0; i < 8; ++i) c.a8[i] = 0.f;
  }
  asm volatile("s_waitcnt vmcnt(%0) lgkmcnt(0)\n\ts_barrier" ::"i"(HF ? 4 : 5) : "memory");
}

__global__ __launch_bounds__(512, 2) void mixer_mlp_kernel(const void* __restrict__ x, const void* __restrict__ w1, const void* __restrict__ b1,
                                                           const void* __restrict__ w2, const void* __restrict__ b2, void* __restrict__ out,
                                                           void* __restrict__ ws) {
  Ctx c;
  const unsigned blk = blockIdx.x, xg = blk & 7u, idx = blk >> 3;
  c.r = 8u * (xg >> 2) + (idx >> 2); c.j = xg & 3u; c.q = idx & 3u;
  c.w = rfl(threadIdx.x >> 6); c.t = c.w & 3u; c.hf = c.w >> 2;
  STAMP(0);
  const srd_t sx = __builtin_amdgcn_make_buffer_rsrc((void*)x, 0, S_ * D_ * 2, 0x00020000);
  c.s1 = __builtin_amdgcn_make_buffer_rsrc((void*)w1, 0, D_ * F_ * 2, 0x00020000);
  c.s2 = __builtin_amdgcn_make_buffer_rsrc((void*)w2, 0, D_ * F_ * 2, 0x00020000);
  c.sw = __builtin_amdgcn_make_buffer_rsrc(ws, 0, WS_USED, 0x00020000);
  const srd_t sb1 = __builtin_amdgcn_make_buffer_rsrc((void*)b1, 0, F_ * 2, 0x00020000);
  c.freg2 = (1152u * c.j + fstart(c.q, 0)) * 2u;
  c.f82 = (1152u * c.j + fstart(c.q, 8)) * 2u;
  c.fown = WS_FLAG + (((c.r * 4u + c.j) * 4u + c.q) << 10);
  c.pollv = 0u;
  c.sw1 = (72u * c.t + 40u * c.hf) * W1ROW + c.freg2;
  c.ldst = c.t * 9216u + c.hf * 5120u;
  if (c.hf == 0) block0<0>(c, sx, sb1); else block0<1>(c, sx, sb1);
  {
    const unsigned hb = WS_H + ((c.r * 4u + c.j) * 144u + c.t) * 2048u;
#pragma unroll
    for (int d = 0; d < 4; ++d) c.hq[d] = hb + ((c.q + d) & 3u) * 73728u + c.hf * 8192u;
#pragma unroll
    for (int e = 0; e < 2; ++e) {
      c.h8[e] = hb + ((c.q + 2u * c.hf + e) & 3u) * 73728u + 65536u;
      c.sw2[e] = (1152u * c.j + fstart((c.q + 2u * e + (c.t & 1u)) & 3u, c.t >> 1)) * W2ROW + 288u * c.q;
    }
    c.sw28 = (1152u * c.j + fstart((c.q + c.t) & 3u, 8)) * W2ROW + 288u * c.q;
  }
  asm volatile("" : "+s"(c.sw1), "+s"(c.ldst), "+s"(c.hq[0]), "+s"(c.hq[1]), "+s"(c.hq[2]), "+s"(c.hq[3]), "+s"(c.h8[0]), "+s"(c.h8[1]),
               "+s"(c.sw2[0]), "+s"(c.sw2[1]), "+s"(c.sw28));
  STAMP(12);
  half_stage<0>(c); STAMP(13);
  half_stage<1>(c); STAMP(14);
  half_stage<2>(c); STAMP(15);
  half_stage<3>(c); STAMP(16);
  half_stage<4>(c); STAMP(17);
  half_stage<5>(c); STAMP(18);
  half_stage<6>(c); STAMP(19);
  half_stage<7>(c); STAMP(20);
  half_stage<8>(c); STAMP(21);
  half_stage<9>(c); STAMP(22);
  half_stage<10>(c); STAMP(23);
  half_stage<11>(c); STAMP(24);
  half_stage<12>(c); STAMP(25);
  half_stage<13>(c); STAMP(26);
  half_stage<14>(c); STAMP(27);

  const unsigned l = lane_id();
  {
    const unsigned wa = c.w * 9216u + l * 16u, ra = (c.w ^ 4u) * 9216u + l * 16u;
    if (c.hf) {
#pragma unroll
      for (int dt = 0; dt < 9; ++dt) *LDSP(f32x4, wa + dt * 1024u) = c.acc2[0][dt];
    } else {
#pragma unroll
      for (int dt = 0; dt < 9; ++dt) { *LDSP(f32x4, wa + dt * 1024u) = c.acc2[1][dt]; c.acc2[1][dt] = c.acc2[0][dt]; }
    }
    BARRIER_LGKM();
#pragma unroll
    for (int dt = 0; dt < 9; ++dt) c.acc2[1][dt] += ldsf4(ra + dt * 1024u);
  }
  const f32x4* R = c.acc2[1];
  if (c.t != c.j) {
    const unsigned so = WS_P + (((c.r * 4u + c.q) * 4u + c.t) * 4u + c.j) * 9216u + c.hf * 4608u;
#pragma unroll
    for (int m = 0; m < 4; ++m) {
      u32x4 v = {cvtpk(R[2 * m][0], R[2 * m][1]), cvtpk(R[2 * m][2], R[2 * m][3]), cvtpk(R[2 * m + 1][0], R[2 * m + 1][1]), cvtpk(R[2 * m + 1][2], R[2 * m + 1][3])};
#pragma unroll
      for (int i = 0; i < 4; ++i) v[i] = v[i] < 0xfffffffeu ? v[i] : 0xfffffffeu;
      asm volatile("buffer_store_dwordx4 %0, %1, %2, %3 offen sc1\n\ts_nop 1" ::"v"(v), "v"(c.vhld), "s"(c.sw), "s"(so + m * 1024u));
    }
    u32x2 v = {cvtpk(R[8][0], R[8][1]), cvtpk(R[8][2], R[8][3])};
#pragma unroll
    for (int i = 0; i < 2; ++i) v[i] = v[i] < 0xfffffffeu ? v[i] : 0xfffffffeu;
    asm volatile("buffer_store_dwordx2 %0, %1, %2, %3 offen sc1" ::"v"(v), "v"(l * 8u), "s"(c.sw), "s"(so + 4096u));
  } else {
#pragma unroll
    for (int dt = 0; dt < 9; ++dt) *LDSP(f32x4, 3u * SLOT + c.hf * 9216u + dt * 1024u + l * 16u) = R[dt];
  }
  const unsigned short* b2p = (const unsigned short*)b2 + 144u * c.q + 4u * (l >> 4);
  const u32x2 bA = *(const u32x2*)(b2p + 32u * c.t), bB = *(const u32x2*)(b2p + 32u * c.t + 16u), bC = *(const u32x2*)(b2p + 128u);
  asm volatile("s_waitcnt vmcnt(0) lgkmcnt(0)\n\ts_barrier" ::: "memory");
  STAMP(28);
  {
    const unsigned sp = WS_P + ((c.r * 4u + c.q) * 4u + c.j) * 4u * 9216u + c.hf * 4608u;
    const unsigned s0 = sp + ((c.j + 1u) & 3u) * 9216u, s1 = sp + ((c.j + 2u) & 3u) * 9216u, s2 = sp + ((c.j + 3u) & 3u) * 9216u;
    const unsigned va = c.vhld + c.t * 1024u, vb = 4096u + l * 8u;
    u32x4 pa[3]; u32x2 pb[3];
    f32x4 o0 = ldsf4(3u * SLOT + c.hf * 9216u + c.t * 2048u + l * 16u), o1 = ldsf4(3u * SLOT + c.hf * 9216u + c.t * 2048u + 1024u + l * 16u);
    f32x4 o2 = ldsf4(3u * SLOT + c.hf * 9216u + 8192u + l * 16u);
    for (unsigned spin = 0; spin < (1u << 16); ++spin) {
      asm volatile("buffer_load_dwordx4 %0, %6, %8, %9 offen sc1\n\tbuffer_load_dwordx4 %1, %6, %8, %10 offen sc1\n\t"
                   "buffer_load_dwordx4 %2, %6, %8, %11 offen sc1\n\t"
                   "s_cmp_lg_u32 %12, 0\n\ts_cbranch_scc1 .Lskip%=\n\t"
                   "buffer_load_dwordx2 %3, %7, %8, %9 offen sc1\n\tbuffer_load_dwordx2 %4, %7, %8, %10 offen sc1\n\t"
                   "buffer_load_dwordx2 %5, %7, %8, %11 offen sc1\n.Lskip%=:\n\ts_waitcnt vmcnt(0)"
                   : "=&v"(pa[0]), "=&v"(pa[1]), "=&v"(pa[2]), "=&v"(pb[0]), "=&v"(pb[1]), "=&v"(pb[2])
                   : "v"(va), "v"(vb), "s"(c.sw), "s"(s0), "s"(s1), "s"(s2), "s"(c.t) : "memory", "scc");
      unsigned mx = 0;
#pragma unroll
      for (int k = 0; k < 3; ++k) {
#pragma unroll
        for (int i = 0; i < 4; ++i) mx = mx > pa[k][i] ? mx : pa[k][i];
        if (c.t == 0) { mx = mx > pb[k][0] ? mx : pb[k][0]; mx = mx > pb[k][1] ? mx : pb[k][1]; }
      }
      if (!__any(mx == ~0u)) break;
    }
    STAMP(29);
#pragma unroll
    for (int k = 0; k < 3; ++k)
#pragma unroll
      for (int i = 0; i < 2; ++i) {
        o0[2 * i] += __uint_as_float(pa[k][i] << 16); o0[2 * i + 1] += __uint_as_float(pa[k][i] & 0xffff0000u);
        o1[2 * i] += __uint_as_float(pa[k][2 + i] << 16); o1[2 * i + 1] += __uint_as_float(pa[k][2 + i] & 0xffff0000u);
      }
    unsigned short* op = (unsigned short*)out + (size_t)(128u * c.r + 32u * c.j + 16u * c.hf + (l & 15u)) * D_ + 144u * c.q;
    const unsigned e0 = cvtpk(__builtin_fmaf(o0[0], RKAP, __uint_as_float(bA[0] << 16)), __builtin_fmaf(o0[1], RKAP, __uint_as_float(bA[0] & 0xffff0000u)));
    const unsigned e1 = cvtpk(__builtin_fmaf(o0[2], RKAP, __uint_as_float(bA[1] << 16)), __builtin_fmaf(o0[3], RKAP, __uint_as_float(bA[1] & 0xffff0000u)));
    const unsigned f0 = cvtpk(__builtin_fmaf(o1[0], RKAP, __uint_as_float(bB[0] << 16)), __builtin_fmaf(o1[1], RKAP, __uint_as_float(bB[0] & 0xffff0000u)));
    const unsigned f1 = cvtpk(__builtin_fmaf(o1[2], RKAP, __uint_as_float(bB[1] << 16)), __builtin_fmaf(o1[3], RKAP, __uint_as_float(bB[1] & 0xffff0000u)));
    auto r0 = __builtin_amdgcn_permlane16_swap(e0, f0, false, false);
    auto r1 = __builtin_amdgcn_permlane16_swap(e1, f1, false, false);
    *(u32x4*)(op + 32u * c.t + 16u * ((l >> 4) & 1u) + 8u * (l >> 5)) = (u32x4){r0[0], r1[0], r0[1], r1[1]};
    if (c.t == 0) {
#pragma unroll
      for (int k = 0; k < 3; ++k)
#pragma unroll
        for (int i = 0; i < 2; ++i) { o2[2 * i] += __uint_as_float(pb[k][i] << 16); o2[2 * i + 1] += __uint_as_float(pb[k][i] & 0xffff0000u); }
      *(u32x2*)(op + 128u + 4u * (l >> 4)) =
          (u32x2){cvtpk(__builtin_fmaf(o2[0], RKAP, __uint_as_float(bC[0] << 16)), __builtin_fmaf(o2[1], RKAP, __uint_as_float(bC[0] & 0xffff0000u))),
                  cvtpk(__builtin_fmaf(o2[2], RKAP, __uint_as_float(bC[1] << 16)), __builtin_fmaf(o2[3], RKAP, __uint_as_float(bC[1] & 0xffff0000u)))};
    }
    asm volatile("mixer_code_end:");
    if (c.w == 0 && c.j == 0 && c.q == 0 && l == 0)
      asm volatile("buffer_store_dword %0, off, %1, %2 sc1" ::"v"(c.tag), "s"(c.sw), "s"(WS_EPOCH + c.r * 128u));
  }
}

extern "C" void kernel_launch(void* const* d_in, const int* in_sizes, int n_in, void* d_out, int out_size, void* d_ws, size_t ws_size,
                              hipStream_t stream) {
  (void)in_sizes; (void)n_in; (void)out_size; (void)ws_size;
  (void)hipFuncSetAttribute((const void*)mixer_mlp_kernel, hipFuncAttributeMaxDynamicSharedMemorySize, LDS_BYTES);
  mixer_mlp_kernel<<<256, 512, LDS_BYTES, stream>>>(d_in[0], d_in[1], d_in[2], d_in[3], d_in[4], d_out, d_ws);
}
```

```cpp
#include <hip/hip_runtime.h>
#include <stdint.h>
#include <utility>
#include <cstdio>
#define STAMPS 1

typedef __bf16 bf16x8 __attribute__((ext_vector_type(8)));
typedef __bf16 bf16x2 __attribute__((ext_vector_type(2)));
typedef short s16x4 __attribute__((ext_vector_type(4)));
typedef float f32x16 __attribute__((ext_vector_type(16)));
typedef float f32x4 __attribute__((ext_vector_type(4)));
typedef float f32x2 __attribute__((ext_vector_type(2)));
typedef unsigned u32x4 __attribute__((ext_vector_type(4)));
typedef unsigned u32x2 __attribute__((ext_vector_type(2)));
typedef __amdgpu_buffer_rsrc_t srd_t;
#define DI __device__ __forceinline__
#define LAS __attribute__((address_space(3)))
#define LDSP(T, a) ((LAS T*)(size_t)(a))

constexpr unsigned S_ = 2048, D_ = 576, F_ = 4608;
constexpr unsigned XROW = D_ * 2, W1ROW = F_ * 2, W2ROW = D_ * 2;
constexpr unsigned SLOT = 36864, RING = 4 * SLOT, SLICE = 28672;
constexpr unsigned L_B1 = RING, L_PAT = L_B1 + 1152, L_JUNK = L_PAT + 2304, L_EX = L_JUNK + 256, LDS_BYTES = L_EX + 8192;
static_assert(LDS_BYTES <= 163840, "LDS");
constexpr unsigned WS_EPOCH = 0, WS_FLAG = 4096, WS_Y = WS_FLAG + 256 * 8 * 128, WS_H = WS_Y + S_ * D_ * 2, H_BYTES = S_ * F_ * 2;
constexpr unsigned WS_P = WS_H + H_BYTES, P_BYTES = 16 * 4 * 4 * 4 * 9216;
constexpr unsigned WS_USED = WS_P + P_BYTES;
constexpr float KAP = 0.4686686930532063f, GAM = 4.91222953926424f, RKAP = 2.1337034344781243f;
#define KSW 10
#define PSLEEP 2
#define TOUCH 1
#define PREFH 1
#define HK2 2
#define CPREF 1
#define PREF 1
constexpr unsigned WS_STAMP = WS_USED;
#define NSTAMP 32
#define STAMP(i) do { unsigned long long t_; asm volatile("s_memtime %0\n\ts_waitcnt lgkmcnt(0)" : "=s"(t_) :: "memory"); c.st[i] = (unsigned)t_; } while (0)

DI unsigned rfl(unsigned x) { return __builtin_amdgcn_readfirstlane(x); }
DI s16x4 trr(unsigned a) { return __builtin_amdgcn_ds_read_tr16_b64_v4i16(LDSP(s16x4, a)); }
DI bf16x8 cat(s16x4 lo, s16x4 hi) { return __builtin_bit_cast(bf16x8, __builtin_shufflevector(lo, hi, 0, 1, 2, 3, 4, 5, 6, 7)); }
DI f32x4 ldsf4(unsigned a) { return *LDSP(f32x4, a); }
DI unsigned cvtpk(float lo, float hi) { f32x2 v = {lo, hi}; return __builtin_bit_cast(unsigned, __builtin_convertvector(v, bf16x2)); }
template <int CTRL> DI float dpp_add(float v) {
  return v + __builtin_bit_cast(float, __builtin_amdgcn_update_dpp(0, __builtin_bit_cast(int, v), CTRL, 0xf, 0xf, false));
}
#define MFMA32(a, b, c) __builtin_amdgcn_mfma_f32_32x32x16_bf16((a), (b), (c), 0, 0, 0)
#define MFMA16(a, b, c) __builtin_amdgcn_mfma_f32_16x16x32_bf16((a), (b), (c), 0, 0, 0)
DI void mfma32(f32x16& acc, bf16x8 a, bf16x8 b) { asm volatile("v_mfma_f32_32x32x16_bf16 %0, %1, %2, %0" : "+v"(acc) : "v"(a), "v"(b)); }
DI void mfma16(f32x4& acc, bf16x8 a, bf16x8 b) { asm volatile("v_mfma_f32_16x16x32_bf16 %0, %1, %2, %0" : "+v"(acc) : "v"(a), "v"(b)); }
DI void mfma16z(f32x4& acc, bf16x8 a, bf16x8 b) { asm volatile("v_mfma_f32_16x16x32_bf16 %0, %1, %2, 0" : "=&v"(acc) : "v"(a), "v"(b)); }
#define BARRIER_LGKM() asm volatile("s_waitcnt lgkmcnt(0)\n\ts_barrier" ::: "memory")
#define GAP() __builtin_amdgcn_sched_barrier(0)
#define PIN(x) asm volatile("" : "+v"(x))

struct Ctx {
  srd_t s1, s2, sw;
  unsigned w, t, hf, r, j, q;
  unsigned freg2, f82;
  unsigned tag, sdma, sw1, ldst, hq[4], h8[2], sw2[2], sw28, fown;
  u32x4 vpat;
  unsigned vpat4;
  unsigned wb;
  unsigned vhld, vhst, tb, pref, tch;
  float gz[2], gm, gz8, gm8;
  unsigned own[4][8], hv8[8];
  bf16x8 X[36];
  bf16x8 F[4];
  f32x16 acc[2];
  float a8[8];
  f32x4 acc2[2][9];
  u32x4 Hb[8][2];
  unsigned pollv;
  unsigned st[NSTAMP];
};
DI unsigned lane_id() {
  unsigned l;
  asm volatile("v_mbcnt_lo_u32_b32 %0, -1, 0\n\tv_mbcnt_hi_u32_b32 %0, -1, %0" : "=v"(l));
  return l;
}
DI unsigned opq(unsigned v) { PIN(v); return v; }

constexpr int pair_of(int h) { return h < 8 ? 0 : (h - 7) / 2; }
constexpr int ph_of(int h) { return h < 8 ? h - 6 : (h - 7) % 2; }
constexpr bool own_unit(int n) { return n == 0 || n == 6 || n == 10 || n == 14; }
constexpr int hload_at(int h, int k) {
  if (h == 8 || h == 10 || h == 12) return k == 0 ? 4 * (h - 8) + 14 : k == HK2 ? 4 * (h - 8) + 15 : -1;
  const int slot = k == 0 ? 0 : k == 2 ? 1 : k == 10 ? 2 : k == 12 ? 3 : k == 1 ? 4 : k == 3 ? 5 : -1;
  if (slot < 0) return -1;
  if (h == 5) return slot < 4 ? 2 + slot : -1;
  if (h == 6) return k < 4 ? 6 + k : 8 + slot;
  if (h < 8 || h > 13 || slot > 3) return -1;
  return 12 + 4 * (h - 8) + slot;
}
constexpr int dma_at(int h, int k) {
  if (h == 0) return (k < 16 && (k & 1) == 0) ? (k < 8 ? 2 * 8 + (k + 2) / 2 : 3 * 8 + (k - 8) / 2) : (k == 15 ? 3 * 8 + 4 : -1);
  if (h + 3 > 14) return -1;
  const int i = h < 4 ? (k == -2 ? 0 : k == 2 ? 1 : k == 6 ? 2 : k == 10 ? 3 : k == 13 ? 4 : -1)
                      : (k == -2 ? 0 : k == 4 ? 1 : k == 6 ? 2 : k == 8 ? 3 : k == 14 ? 4 : -1);
  return i < 0 ? -1 : (h + 3) * 8 + i;
}
constexpr int poll_k(int h) { return h == 4 ? 5 : (h == 7 || h == 9 || h == 10 || h == 11) ? -1 : -9; }
constexpr bool poll_at(int h, int k) { return k == poll_k(h); }
constexpr int flag_at(int h) { return (h == 3 || h == 5 || h == 7 || h == 9) ? (h + 1) / 2 : 0; }
constexpr bool hst_at(int h, int k) { return (h < 8 && (h & 1) && (k == 0 || k == 15)) || (h == 1 && k == 12); }
constexpr bool pref_at(int h, int k) { return PREF && h < 6 && k == 15; }
constexpr bool touch_at(int h, int k) { return TOUCH && (h == 2 || h == 4 || h == 6 || h == 8) && k == 13; }
constexpr bool mark_at(int h, int k) { return (h == 2 && (k == 4 || k == 12)) || (h == 4 && k == 2); }
constexpr int nops(int h, int k, int n) {
  return (hload_at(h, k) >= 0 ? 1 : 0) + (hst_at(h, k) ? 1 : 0) + (mark_at(h, k) ? 1 : 0) +
         (dma_at(h, k) >= 0 && (dma_at(h, k) & 7) < n ? 1 : 0);
}
constexpr int between(int h0, int k0, int h1, int k1, int n) {
  int s = 0;
  for (int h = h0; h <= h1; ++h)
    for (int k = -2; k < 16; ++k)
      if ((h > h0 || k > k0) && (h < h1 || k < k1)) s += nops(h, k, n);
  return s;
}
constexpr int cmin(int a, int b) { return a < b ? a : b; }

DI unsigned fstart(unsigned qq, unsigned ch) { return 32u * (9u * qq + ch + (qq & 1u) * (1u - 9u * (ch >> 3))); }

DI void dma16(unsigned m0v, unsigned voff, srd_t srd, unsigned soff) {
  asm volatile("s_mov_b32 m0, %0\n\ts_nop 0\n\tbuffer_load_dwordx4 %1, %2, %3 offen lds" ::"s"(m0v), "v"(voff), "s"(srd), "s"(soff));
}
template <int HS, int I> DI void issue_piece(Ctx& c) {
  const srd_t sd = HS < 6 ? c.s1 : c.s2;
  if constexpr (I == 0) {
    if constexpr (HS < 6) c.sdma = c.sw1 + ((HS & 1) * 288u * W1ROW + ((HS >> 1) + 1) * 128u);
    else c.sdma = HS == 8 ? c.sw28 : c.sw2[ph_of(HS)] + pair_of(HS) * 64u * W2ROW;
    dma16((HS & 3) * SLOT + c.ldst, c.vpat[0], sd, c.sdma);
  } else if constexpr (I < 4) {
    asm volatile("buffer_load_dwordx4 %0, %1, %2 offen offset:%3 lds" ::"v"(c.vpat[I]), "s"(sd), "s"(c.sdma), "i"(1024 * I));
  } else {
    asm volatile("s_cmp_lg_u32 %4, 0\n\ts_cbranch_scc1 .Lskip%=\n\ts_mov_b32 m0, %0\n\ts_nop 0\n\tbuffer_load_dwordx4 %1, %2, %3 offen lds\n.Lskip%=:"
                 ::"s"((HS & 3) * SLOT + 4096u + c.ldst), "v"(c.vpat4), "s"(sd), "s"(c.sdma), "s"(c.hf) : "scc");
  }
}
template <int HS> DI void issue_slot(Ctx& c) {
  issue_piece<HS, 0>(c); issue_piece<HS, 1>(c); issue_piece<HS, 2>(c); issue_piece<HS, 3>(c); issue_piece<HS, 4>(c);
}
template <int N, int U> DI void issue_hload(Ctx& c) {
  constexpr int h = 6 + N / 2;
  const unsigned so = h == 8 ? c.h8[N & 1] : c.hq[2 * ph_of(h) + (N & 1)] + pair_of(h) * 16384u;
  asm volatile("buffer_load_dwordx4 %0, %1, %2, %3 offen offset:%4 sc1" : "=v"(c.Hb[N & 7][U]) : "v"(c.vhld), "s"(c.sw), "s"(so), "i"(1024 * U));
}
DI unsigned poll_off(const Ctx& c, bool dest) {
  const unsigned l = lane_id(), dp = (l & 3u) ? (l & 3u) : 1u;
  return WS_FLAG + ((dest ? (c.r * 4u + ((c.j + dp) & 3u)) * 4u + c.q : (c.r * 4u + c.j) * 4u + ((c.q + dp) & 3u)) << 10);
}
template <int EV> DI void store_flag(Ctx& c) {
  asm volatile("s_cmp_lg_u32 %3, 0\n\ts_cbranch_scc1 .Lskip%=\n\ts_mov_b64 exec, 1\n\t"
               "buffer_store_dword %0, off, %1, %2 offset:%4 sc1\n\ts_mov_b64 exec, -1\n.Lskip%=:"
               ::"v"(c.tag), "s"(c.sw), "s"(c.fown), "s"(c.w), "i"(EV * 128) : "scc");
}
template <int EV> DI void issue_poll(Ctx& c, unsigned& pv, unsigned voff) {
  asm volatile("s_cmp_lg_u32 %3, 7\n\ts_cbranch_scc1 .Lskip%=\n\tbuffer_load_dword %0, %1, %2, 0 offen offset:%4 sc1\n.Lskip%=:"
               : "+v"(pv) : "v"(voff), "s"(c.sw), "s"(c.w), "i"(EV * 128) : "scc");
}
template <int EV, int N> DI void check_poll(Ctx& c, unsigned& pv, bool dest) {
  if (c.w == 7) {
    asm volatile("s_waitcnt vmcnt(%1)" : "+v"(pv) : "i"(N) : "memory");
    if (__builtin_expect(__builtin_amdgcn_ballot_w64(pv != c.tag) != 0, 0)) {
      const unsigned voff = poll_off(c, dest);
      for (unsigned spin = 0; spin < (1u << 16); ++spin) {
        unsigned v;
        asm volatile("s_sleep 1\n\tbuffer_load_dword %0, %1, %2, 0 offen offset:%3 sc1\n\ts_waitcnt vmcnt(0)" : "=&v"(v) : "v"(voff), "s"(c.sw), "i"(EV * 128) : "memory");
        if (__builtin_amdgcn_ballot_w64(v != c.tag) == 0) break;
      }
    }
  }
}
template <int P> DI void store_h(Ctx& c, const unsigned (&hv)[8], unsigned so) {
  const u32x4 v = {hv[4 * P], hv[4 * P + 1], hv[4 * P + 2], hv[4 * P + 3]};
  asm volatile("buffer_store_dwordx4 %0, %1, %2, %3 offen sc1\n\ts_nop 1" ::"v"(v), "v"(c.vhst), "s"(c.sw), "s"(so));
}
template <int N0, int N1> DI void wait_vm(const Ctx&) {
  static_assert(N1 <= N0 && N1 >= 0 && N0 < 64, "vmcnt");
  asm volatile("s_waitcnt vmcnt(%0)" ::"i"(N1) : "memory");
}

template <int H> DI void set_bases(Ctx& c) {
  constexpr unsigned d = (H & 3) ? SLOT : 0u - 3u * SLOT;
  if constexpr (H == 6) {
    const unsigned l = lane_id(), g = l >> 4;
    c.wb = opq(2u * SLOT + (16u * (g >> 1) + 4u * (g & 1u) + ((l >> 2) & 3u)) * 288u + 8u * (l & 3u) + c.hf * 18432u);
  } else if constexpr (H > 0) c.wb = opq(c.wb + d);
}
template <int H, int S> DI void rd(Ctx& c) {
  constexpr unsigned o = H < 6 ? 2048u * S : 9216u * (S / 9) + 32u * (S % 9), e = H < 6 ? 512u : 2304u;
  c.F[(H * 18 + S) & 3] = cat(trr(c.wb + o), trr(c.wb + o + e));
}
template <int H, int S> DI void mm(Ctx& c) {
  const bf16x8 a = c.F[(H * 18 + S) & 3];
  if constexpr (H < 6) {
    constexpr int B = ((H >> 1) + 1) & 1;
    mfma32(c.acc[B], a, c.X[18 * (H & 1) + S]);
  } else {
    constexpr int N = 2 * (H - 6) + S / 9, dt = S % 9;
    u32x4 b0 = c.Hb[N & 7][0], b1 = c.Hb[N & 7][1];
    if constexpr (own_unit(N)) {
      const unsigned(&o)[8] = c.own[pair_of(H)];
      b0 = (u32x4){o[0], o[1], o[2], o[3]}; b1 = (u32x4){o[4], o[5], o[6], o[7]};
    }
    if constexpr (N == 0) {
      mfma16z(c.acc2[0][dt], a, __builtin_bit_cast(bf16x8, b0));
      mfma16z(c.acc2[1][dt], a, __builtin_bit_cast(bf16x8, b1));
    } else {
      mfma16(c.acc2[0][dt], a, __builtin_bit_cast(bf16x8, b0));
      mfma16(c.acc2[1][dt], a, __builtin_bit_cast(bf16x8, b1));
    }
  }
}
template <int Q> DI void bias_quad(f32x16& acc, unsigned a) {
  const f32x4 v = ldsf4(a + (Q >> 1) * 64u + (Q & 1) * 16u);
#pragma unroll
  for (int i = 0; i < 4; ++i) acc[4 * Q + i] = v[i];
}
template <int E, int ST> DI void gelu_stage(float s, float& z, float& m0, unsigned (&hv)[8]) {
  if constexpr (ST == 0) { z = s * __builtin_fmaf(s, s, GAM); PIN(z); }
  if constexpr (ST == 1) { z = __builtin_amdgcn_exp2f(-z); PIN(z); }
  if constexpr (ST == 2) { z = __builtin_amdgcn_rcpf(1.0f + z); PIN(z); }
  if constexpr (ST == 3) {
    if constexpr ((E & 1) == 0) { m0 = s * z; PIN(m0); }
    else { float m = s * z, mm0 = m0; PIN(m); PIN(mm0); hv[E >> 1] = cvtpk(mm0, m); PIN(hv[E >> 1]); }
  }
}
constexpr int gstart(int e) { return 1 + (e * 29) / 15; }
constexpr int gelem(int g, int st) {
  for (int e = 0; e < 16; ++e) if (gstart(e) + st == g) return e;
  return -1;
}
template <int B, int G, int ST> DI void gelu_part(Ctx& c) {
  constexpr int e = gelem(G, ST);
  if constexpr (e >= 0) gelu_stage<e, ST>(c.acc[B & 1][e], c.gz[e & 1], c.gm, c.own[B]);
}
template <int B, int G> DI void gelu_gap(Ctx& c) {
  if constexpr (G == 1) asm volatile("s_nop 7" : "+v"(c.acc[B & 1]));
  gelu_part<B, G, 3>(c); gelu_part<B, G, 2>(c); gelu_part<B, G, 1>(c); gelu_part<B, G, 0>(c);
  if constexpr (B < 2) {
    if constexpr (G == 27) { bias_quad<0>(c.acc[B & 1], c.tb + (B + 2) * 256u); bias_quad<1>(c.acc[B & 1], c.tb + (B + 2) * 256u); }
    if constexpr (G == 31) bias_quad<2>(c.acc[B & 1], c.tb + (B + 2) * 256u);
    if constexpr (G == 33) bias_quad<3>(c.acc[B & 1], c.tb + (B + 2) * 256u);
  }
}

template <int H0, int K0, int H1, int K1> DI void wait_after(const Ctx& c) {
  wait_vm<between(H0, K0, H1, K1, 5), between(H0, K0, H1, K1, 4)>(c);
}
template <int H, int K> DI void side(Ctx& c) {
  if constexpr (poll_at(H, K)) {
    if constexpr (H == 10) issue_poll<4>(c, c.pollv, poll_off(c, true));
    else issue_poll<(H == 4 ? 2 : (H - 1) / 2)>(c, c.pollv, c.pref);
  }
  if constexpr (K == -1 && flag_at(H)) store_flag<flag_at(H)>(c);
  if constexpr (H == 4 && K == 15) c.vhld = lane_id() * 16u;
  if constexpr (H == 3 && K == 11) { c.pref = poll_off(c, false); PIN(c.pref); }
  if constexpr (hload_at(H, K) >= 0) issue_hload<(hload_at(H, K) >> 1), (hload_at(H, K) & 1)>(c);
  if constexpr (pref_at(H, K)) {
    if (c.w == (unsigned)H) {
      const unsigned m = 64u * H + lane_id(), ln = ((c.r & 7u) * 4u + c.q) + 32u * (m < 324u ? m : 323u), rr = ln / 9u;
      asm volatile("s_mov_b32 m0, %0\n\ts_nop 0\n\tbuffer_load_dword %1, %2, %3 offen lds" ::"s"(L_JUNK),
                   "v"((288u * (rr & 3u) + (rr >> 2)) * W2ROW + (ln - rr * 9u) * 128u), "s"(c.s2), "s"(c.j * 1152u * W2ROW));
    }
  }
  if constexpr (mark_at(H, K)) {
    constexpr unsigned k = H == 4 ? 2u : K == 4 ? 0u : 1u;
    const u32x4 ones = {~0u, ~0u, ~0u, ~0u};
    const unsigned so = WS_P + (((c.r * 4u + c.q) * 4u + c.j) * 4u + ((c.j + 1u + k) & 3u)) * 9216u, vo = lane_id() * 16u;
    asm volatile("buffer_store_dwordx4 %0, %1, %2, %3 offen sc1\n\ts_cmp_lg_u32 %4, %5\n\ts_cbranch_scc1 .Lskip%=\n\t"
                 "buffer_store_dwordx4 %0, %1, %2, %6 offen sc1\n.Lskip%=:\n\ts_nop 1"
                 ::"v"(ones), "v"(vo), "s"(c.sw), "s"(so + c.w * 1024u), "s"(c.w), "i"(k), "s"(so + 8192u) : "scc");
  }
  if constexpr (dma_at(H, K) >= 0) issue_piece<(dma_at(H, K) >> 3), (dma_at(H, K) & 7)>(c);
  if constexpr (H == 2 && K == 14) {
    const unsigned l = lane_id();
#pragma unroll
    for (int i = 0; i < 4; ++i) c.vpat[i] = *LDSP(unsigned, L_PAT + (c.hf * 5u + i) * 256u + l * 4u);
    c.vpat4 = *LDSP(unsigned, L_PAT + 1024u + l * 4u);
  }
  if constexpr (H == 0 && K == -2) {
    if (c.hf) {
      const unsigned l = lane_id();
      const f32x4 e0 = ldsf4(L_EX + c.t * 2048u + l * 16u), e1 = ldsf4(L_EX + c.t * 2048u + 1024u + l * 16u);
#pragma unroll
      for (int i = 0; i < 4; ++i) { c.a8[i] = e0[i]; c.a8[4 + i] = e1[i]; }
    }
  }
  if constexpr (H < 8) gelu_gap<(H >> 1), K + 18 * (H & 1)>(c);
  if constexpr (H < 2) {
    constexpr int G = K + 18 * H - 1;
    if constexpr (G >= 0 && G < 28) gelu_stage<G / 4, (G & 3)>(c.a8[G / 4], c.gz8, c.gm8, c.hv8);
    if constexpr (G == 28) { gelu_stage<7, 0>(c.a8[7], c.gz8, c.gm8, c.hv8); gelu_stage<7, 1>(c.a8[7], c.gz8, c.gm8, c.hv8); }
    if constexpr (G == 29) { gelu_stage<7, 2>(c.a8[7], c.gz8, c.gm8, c.hv8); gelu_stage<7, 3>(c.a8[7], c.gz8, c.gm8, c.hv8); }
  }
  if constexpr (hst_at(H, K)) {
    if constexpr (K == 12) store_h<0>(c, c.hv8, c.hq[0] + 65536u - c.hf * 7936u);
    else store_h<(K == 0 ? 0 : 1)>(c, c.own[H >> 1], c.hq[0] + (H >> 1) * 16384u + (K == 0 ? 0u : 256u));
  }
  if constexpr (H >= 2 && H <= 8 && (H & 1) == 0 && (K == 1 || K == 3 || K == 5 || K == 7)) {
    unsigned(&o)[8] = c.own[(H >> 1) - 1];
    auto rr = __builtin_amdgcn_permlane16_swap(o[K >> 1], o[4 + (K >> 1)], false, false);
    o[K >> 1] = rr[0]; o[4 + (K >> 1)] = rr[1];
    PIN(o[K >> 1]); PIN(o[4 + (K >> 1)]);
  }
}
constexpr int hpos(int n) {
  for (int h = 4; h < 14; ++h) for (int k = -2; k < 16; ++k) if (hload_at(h, k) == 2 * n + 1) return h * 32 + k + 2;
  return 0;
}
constexpr int cmax(int a, int b) { return a > b ? a : b; }
template <int H> DI void end_hs(Ctx& c) {
  if constexpr (H < 14) {
    constexpr int p0 = H == 0 ? -1 : H == 1 ? 8 : (H - 2) * 32 + 17;
    constexpr int p1 = (H >= 5 && !own_unit(2 * (H - 5))) ? hpos(2 * (H - 5)) : 0, p2 = 0, p3 = flag_at(H + 1) ? (H - 1) * 32 + 17 : 0;
    constexpr int p = H == 0 ? -1 : cmax(cmax(p0, p1), cmax(p2, p3));
    wait_after<(p < 0 ? 0 : p / 32), (p < 0 ? -3 : p % 32 - 2), H, 16>(c);
    if constexpr (poll_k(H) > -9) check_poll<(H == 10 ? 4 : H == 4 ? 2 : (H - 1) / 2), between(H, poll_k(H), H, 16, 4)>(c, c.pollv, H == 10);
    BARRIER_LGKM();
  }
  if constexpr (H >= 5 && H < 14 && !own_unit(2 * (H - 5))) { PIN(c.Hb[(2 * (H - 5)) & 7][0]); PIN(c.Hb[(2 * (H - 5)) & 7][1]); }
  GAP();
}
template <int H> DI void mid_wait(Ctx& c) {
  if constexpr (H >= 6) {
    constexpr int p = hpos(2 * (H - 6) + 1);
    wait_after<p / 32, p % 32 - 2, H, 9>(c);
    PIN(c.Hb[(2 * (H - 6) + 1) & 7][0]); PIN(c.Hb[(2 * (H - 6) + 1) & 7][1]);
  }
}
#define GST(H, i) do {} while (0)
template <int H, int K> DI void step(Ctx& c) {
  GST(H, K + 2);
  if constexpr (K == 9) mid_wait<H>(c);
  if constexpr (K == KSW) asm volatile("s_setprio 0");
  mm<H, K>(c);
  if constexpr (K + 4 < 18) rd<H, K + 4>(c);
  side<H, K>(c);
  GAP();
}
template <int H, int... Ks> DI void steps(Ctx& c, std::integer_sequence<int, Ks...>) { (step<H, Ks>(c), ...); }
DI void prio_young(const Ctx& c) {
  asm volatile("s_cmp_eq_u32 %0, 0\n\ts_cbranch_scc1 .Lskip%=\n\ts_setprio 1\n.Lskip%=:" ::"s"(c.hf) : "scc");
}
template <int H> DI void half_stage(Ctx& c) {
  prio_young(c);
  set_bases<H>(c);
  rd<H, 0>(c); rd<H, 1>(c);
  GAP();
  GST(H, 0);
  if constexpr (H > 0) mm<H - 1, 16>(c); else mfma32(c.acc[0], c.F[2], c.X[34]);
  rd<H, 2>(c);
  side<H, -2>(c);
  GAP();
  GST(H, 1);
  if constexpr (H > 0) mm<H - 1, 17>(c); else mfma32(c.acc[0], c.F[3], c.X[35]);
  rd<H, 3>(c);
  side<H, -1>(c);
  GAP();
  steps<H>(c, std::make_integer_sequence<int, 16>{});
  if constexpr (H == 14) { mm<14, 16>(c); mm<14, 17>(c); GAP(); asm volatile("s_nop 15" ::: "memory"); GAP(); }
  GST(H, 18);
  end_hs<H>(c);
  GST(H, 20);
}

struct B0 {
  unsigned vy, vw1, vc8, xa[4], l8, l80, lx, lw, junk;
  bf16x8 G[4];
  f32x16 accB;
};
template <int S> DI void issue_y(Ctx& c, B0& b) {
  const unsigned dst = (S % 5) * SLICE + c.w * 1024u, so = WS_Y + c.r * 128u * XROW + S * 128u;
  asm volatile("s_mov_b32 m0, %0\n\ts_nop 0\n\tbuffer_load_dwordx4 %1, %2, %3 offen sc1 lds" ::"s"(dst), "v"(b.vy), "s"(c.sw), "s"(so));
  asm volatile("s_mov_b32 m0, %0\n\ts_nop 0\n\tbuffer_load_dwordx4 %1, %2, %3 offen sc1 lds" ::"s"(dst + 8192u), "v"(b.vy), "s"(c.sw), "s"(so + 64u * XROW));
}
template <int HF, int S> DI void issue_w(Ctx& c, B0& b) {
  const unsigned dst = (S % 5) * SLICE, kk = S * 64u;
  dma16(dst + c.w * 1024u + 16384u, b.vw1, c.s1, (kk + 8u * c.w) * W1ROW + c.freg2);
  if constexpr (HF == 0) dma16(dst + c.t * 1024u + 24576u, b.vc8, c.s1, (kk + 16u * c.t) * W1ROW + c.f82);
}
template <int HF, int S, int K> DI void rd0(Ctx& c, B0& b) {
  c.X[S * 4 + K] = *LDSP(bf16x8, b.xa[K]);
  c.F[K] = cat(trr(c.wb + 2048u * K), trr(c.wb + 2048u * K + 512u));
  if constexpr (HF == 0) b.G[K] = cat(trr(b.l8 + 1024u * K), trr(b.l8 + 1024u * K + 256u));
}
template <int S, int K> DI void mmA(Ctx& c) { mfma32(c.acc[0], c.F[K], c.X[S * 4 + K]); }
template <int HF, int S, int K> DI void mmB(Ctx& c, B0& b) {
  if constexpr (HF == 0) mfma32(b.accB, b.G[K], c.X[S * 4 + K]);
}
template <int HF, int S> DI void bases0(Ctx& c, B0& b) {
  constexpr unsigned base = (S % 5) * SLICE;
#pragma unroll
  for (int k = 0; k < 4; ++k) b.xa[k] = opq(base + (b.lx ^ (unsigned)(k << 5)));
  c.wb = opq(base + 16384u + b.lw);
  if constexpr (HF == 0) b.l8 = opq(base + 24576u + b.l80);
}
template <int HF, int S> DI void slice(Ctx& c, B0& b) {
  constexpr int n0 = HF ? 3 : 4, n = HF ? 4 : 5, M = 0;
  constexpr int N = S == 0 ? 2 : S == 1 ? 4 : S == 2 ? 4 + n0 : S == 3 ? 2 + 2 * n0 : S <= 5 ? 3 * n0 : S == 6 ? 2 * n0 + M : S == 7 ? n0 + M : M + n;
  asm volatile("s_waitcnt vmcnt(%0) lgkmcnt(0)\n\ts_barrier" ::"i"(N) : "memory");
  STAMP(3 + S);
  GAP();
  if constexpr (S > 0) mmA<S - 1, 2>(c);
  rd0<HF, S, 0>(c, b);
  GAP();
  if constexpr (S > 0) mmB<HF, S - 1, 2>(c, b);
  rd0<HF, S, 1>(c, b);
  GAP();
  if constexpr (S > 0) mmA<S - 1, 3>(c);
  if constexpr (S <= 1) issue_y<2 * S + 2>(c, b);
  if constexpr (S >= 2 && S <= 4) issue_y<S + 4>(c, b);
  if constexpr (S == 7) issue_slot<0>(c);
  if constexpr (S == 8) issue_slot<1>(c);
  GAP();
  if constexpr (S > 0) mmB<HF, S - 1, 3>(c, b);
  rd0<HF, S, 2>(c, b);
  GAP();
  mmA<S, 0>(c);
  rd0<HF, S, 3>(c, b);
  GAP();
  mmB<HF, S, 0>(c, b);
  if constexpr (S <= 1) issue_y<2 * S + 3>(c, b);
  if constexpr (S >= 1 && S <= 4) issue_w<HF, S + 4>(c, b);
  if constexpr (S == 6) {
#pragma unroll
    for (int i = 0; i < 4; ++i) c.vpat[i] = b.vw1 + i * (8u * W1ROW - 1024u);
    c.vpat4 = b.vw1 + 32u * W1ROW;
    bias_quad<0>(c.acc[1], c.tb + 256u); bias_quad<1>(c.acc[1], c.tb + 256u); bias_quad<2>(c.acc[1], c.tb + 256u); bias_quad<3>(c.acc[1], c.tb + 256u);
  }
  if constexpr (S < 8) bases0<HF, S + 1>(c, b);
  GAP();
  mmA<S, 1>(c);
  GAP();
  mmB<HF, S, 1>(c, b);
  GAP();
}
DI float wave_sum(float v) {
  v = dpp_add<0x121>(v); v = dpp_add<0x122>(v); v = dpp_add<0x124>(v); v = dpp_add<0x128>(v);
  auto a = __builtin_amdgcn_permlane16_swap(__float_as_uint(v), __float_as_uint(v), false, false);
  v = __uint_as_float(a[0]) + __uint_as_float(a[1]);
  auto b = __builtin_amdgcn_permlane32_swap(__float_as_uint(v), __float_as_uint(v), false, false);
  return __uint_as_float(b[0]) + __uint_as_float(b[1]);
}
template <int HF> DI void block0(Ctx& c, const srd_t sx, const srd_t sb1) {
  B0 b;
  const unsigned l = threadIdx.x & 63u, g = l >> 4, qq = (l >> 2) & 3u, pp = l & 3u, h = l >> 5;
  const unsigned row = (c.r * 128u + (c.j * 4u + c.q) * 8u + c.w) * XROW, vb = 1024u + (l & 31u) * 4u;
  u32x4 xa; unsigned xb, b1raw, epoch;
  asm volatile("buffer_load_dwordx4 %0, %1, %2, %3 offen" : "=v"(xa) : "v"(l * 16u), "s"(sx), "s"(row));
  asm volatile("buffer_load_dword %0, %1, %2, %3 offen" : "=v"(xb) : "v"(vb), "s"(sx), "s"(row));
  asm volatile("buffer_load_dword %0, off, %1, %2 sc1" : "=v"(epoch) : "s"(c.sw), "s"(WS_EPOCH + c.r * 128u));
  b.vw1 = (l >> 3) * W1ROW + (((l & 7u) ^ ((g & 1u) << 2)) << 4);
  b.vc8 = (l >> 2) * W1ROW + (pp << 4);
  issue_w<HF, 0>(c, b);
  {
    unsigned long long pc; unsigned len, o, junk;
    asm volatile("s_getpc_b64 %0\n.Lpf%=:\n\ts_mov_b32 %1, mixer_code_end-.Lpf%=-4\n\t"
                 "s_min_u32 %2, %4, %1\n\ts_load_dword %3, %0, %2\n\t"
                 "s_add_u32 %2, %4, 0x2000\n\ts_min_u32 %2, %2, %1\n\ts_load_dword %3, %0, %2\n\t"
                 "s_add_u32 %2, %4, 0x4000\n\ts_min_u32 %2, %2, %1\n\ts_load_dword %3, %0, %2\n\t"
                 "s_add_u32 %2, %4, 0x6000\n\ts_min_u32 %2, %2, %1\n\ts_load_dword %3, %0, %2"
                 : "=&s"(pc), "=&s"(len), "=&s"(o), "=&s"(b.junk) : "s"((((c.r & 1u) * 4u + c.q) + 8u * c.w) * 128u) : "scc");
    (void)junk;
  }
  GAP();
  asm volatile("s_waitcnt vmcnt(%2)" : "+v"(xa), "+v"(xb) : "i"(HF ? 2 : 3) : "memory");
  {
    float v[10];
#pragma unroll
    for (int i = 0; i < 4; ++i) { v[2 * i] = __uint_as_float(xa[i] << 16); v[2 * i + 1] = __uint_as_float(xa[i] & 0xffff0000u); }
    v[8] = __uint_as_float(xb << 16); v[9] = __uint_as_float(xb & 0xffff0000u);
    const float wt = h ? 0.f : 1.f;
    const float mean = wave_sum(((v[0] + v[1]) + (v[2] + v[3])) + ((v[4] + v[5]) + (v[6] + v[7])) + wt * (v[8] + v[9])) * (1.0f / 576.0f);
#pragma unroll
    for (int i = 0; i < 10; ++i) v[i] -= mean;
    float ss = wt * (v[8] * v[8] + v[9] * v[9]);
#pragma unroll
    for (int i = 0; i < 8; ++i) ss = __builtin_fmaf(v[i], v[i], ss);
    const float rs = KAP * __builtin_amdgcn_rsqf(wave_sum(ss) * (1.0f / 576.0f) + 1e-5f);
    const u32x4 ya = {cvtpk(v[0] * rs, v[1] * rs), cvtpk(v[2] * rs, v[3] * rs), cvtpk(v[4] * rs, v[5] * rs), cvtpk(v[6] * rs, v[7] * rs)};
    const unsigned yb = cvtpk(v[8] * rs, v[9] * rs);
    asm volatile("buffer_store_dwordx4 %0, %1, %2, %3 offen sc1\n\ts_nop 1" ::"v"(ya), "v"(l * 16u), "s"(c.sw), "s"(WS_Y + row));
    asm volatile("buffer_store_dword %0, %1, %2, %3 offen sc1" ::"v"(yb), "v"(vb), "s"(c.sw), "s"(WS_Y + row));
  }
  GAP();
  STAMP(1);
  b.vy = ((l >> 3) + 8u * c.w) * XROW + (((l & 7u) ^ ((g + 4u * c.w) & 7u)) << 4);
  b.lw = (8u * h + qq) * 128u + (((4u * HF + 2u * (g & 1u) + (pp & 1u)) ^ (((qq >> 1) & 1u) << 2)) << 4) + 8u * (pp >> 1);
  const unsigned xrow = 32u * c.t + (l & 31u);
  b.lx = xrow * 128u + ((h ^ ((xrow >> 1) & 7u)) << 4);
  b.l80 = (8u * h + qq) * 64u + 32u * (g & 1u) + 16u * (pp & 1u) + 8u * (pp >> 1);
  c.tb = L_B1 + HF * 128u + h * 32u;
  c.vhst = (l & 16u) * 64u + (l & 15u) * 16u + (l & 32u) * 16u;
  bases0<HF, 0>(c, b);
  GAP();
  asm volatile("s_waitcnt vmcnt(0)\n\ts_barrier" : "+v"(epoch)::"memory");
  c.tag = rfl(epoch) + 1u;
  STAMP(31);
  store_flag<1>(c);
  {
    const unsigned tid = threadIdx.x < 288u ? threadIdx.x : 287u;
    const unsigned off = c.freg2 + tid * 2u + (tid >> 8) * (c.f82 - c.freg2 - 512u);
    asm volatile("buffer_load_ushort %0, %1, %2, 0 offen" : "=v"(b1raw) : "v"(off), "s"(sb1));
  }
  issue_w<HF, 1>(c, b); issue_w<HF, 2>(c, b); issue_w<HF, 3>(c, b); issue_w<HF, 4>(c, b);
#pragma unroll
  for (int k = 0; k < 2; ++k) {
    const unsigned i = k ? 8u : c.w, nn = 64u * i + l, rho = nn / 18u, chn = nn - rho * 18u;
    const unsigned f = (rho & 3u) | ((rho & 4u) << 2) | ((rho & 24u) >> 1);
    const unsigned rel = i < 5u ? i : i - 5u;
    *LDSP(unsigned, L_PAT + i * 256u + l * 4u) = f * W2ROW + chn * 16u - (rel < 4u ? rel * 1024u : 0u);
  }
  asm volatile("s_waitcnt vmcnt(%1)" : "+v"(b1raw) : "i"(HF ? 4 : 8) : "memory");
  if (threadIdx.x < 288u) *LDSP(float, L_B1 + threadIdx.x * 4u) = __uint_as_float(b1raw << 16) * KAP;
  if (HF == 1 && c.w == 7) {
    const unsigned voff = WS_FLAG + 128u + ((c.r * 16u + (l & 15u)) << 10);
    for (unsigned spin = 0; spin < (1u << 16); ++spin) {
      unsigned v;
      asm volatile("s_sleep %3\n\tbuffer_load_dword %0, %1, %2, 0 offen sc1\n\ts_waitcnt vmcnt(0)" : "=&v"(v) : "v"(voff), "s"(c.sw), "i"(PSLEEP) : "memory");
      if (__builtin_amdgcn_ballot_w64(v != c.tag) == 0) break;
    }
  }
  BARRIER_LGKM();
  asm volatile("" ::"s"(b.junk));
  STAMP(2);
  issue_y<0>(c, b); issue_y<1>(c, b);
  bias_quad<0>(c.acc[0], c.tb); bias_quad<1>(c.acc[0], c.tb); bias_quad<2>(c.acc[0], c.tb); bias_quad<3>(c.acc[0], c.tb);
  if constexpr (HF == 0) { bias_quad<0>(b.accB, c.tb + 1024u); bias_quad<1>(b.accB, c.tb + 1024u); bias_quad<2>(b.accB, c.tb + 1024u); bias_quad<3>(b.accB, c.tb + 1024u); }
  slice<HF, 0>(c, b);
  slice<HF, 1>(c, b); slice<HF, 2>(c, b); slice<HF, 3>(c, b); slice<HF, 4>(c, b);
  slice<HF, 5>(c, b); slice<HF, 6>(c, b); slice<HF, 7>(c, b); slice<HF, 8>(c, b);
  mmB<HF, 8, 2>(c, b); mmB<HF, 8, 3>(c, b);
  asm volatile("s_nop 15" : "+v"(b.accB));
  c.wb = opq(b.lw);
  if constexpr (HF == 0) {
    const unsigned ll = lane_id();
    f32x4 e0, e1;
#pragma unroll
    for (int i = 0; i < 4; ++i) { e0[i] = b.accB[8 + i]; e1[i] = b.accB[12 + i]; }
    *LDSP(f32x4, L_EX + c.t * 2048u + ll * 16u) = e0;
    *LDSP(f32x4, L_EX + c.t * 2048u + 1024u + ll * 16u) = e1;
#pragma unroll
    for (int i = 0; i < 8; ++i) c.a8[i] = b.accB[i];
  } else {
#pragma unroll
    for (int i = 0; i < 8; ++i) c.a8[i] = 0.f;
  }
  asm volatile("s_waitcnt vmcnt(%0) lgkmcnt(0)\n\ts_barrier" ::"i"(HF ? 4 : 5) : "memory");
}

__global__ __launch_bounds__(512, 2) void mixer_mlp_kernel(const void* __restrict__ x, const void* __restrict__ w1, const void* __restrict__ b1,
                                                           const void* __restrict__ w2, const void* __restrict__ b2, void* __restrict__ out,
                                                           void* __restrict__ ws) {
  Ctx c;
  const unsigned blk = blockIdx.x, xg = blk & 7u, idx = blk >> 3;
  c.r = 8u * (xg >> 2) + (idx >> 2); c.j = xg & 3u; c.q = idx & 3u;
  c.w = rfl(threadIdx.x >> 6); c.t = c.w & 3u; c.hf = c.w >> 2;
  STAMP(0);
  const unsigned long long rt0 = __builtin_amdgcn_s_memrealtime();
  const srd_t sx = __builtin_amdgcn_make_buffer_rsrc((void*)x, 0, S_ * D_ * 2, 0x00020000);
  c.s1 = __builtin_amdgcn_make_buffer_rsrc((void*)w1, 0, D_ * F_ * 2, 0x00020000);
  c.s2 = __builtin_amdgcn_make_buffer_rsrc((void*)w2, 0, D_ * F_ * 2, 0x00020000);
  c.sw = __builtin_amdgcn_make_buffer_rsrc(ws, 0, WS_USED, 0x00020000);
  const srd_t sb1 = __builtin_amdgcn_make_buffer_rsrc((void*)b1, 0, F_ * 2, 0x00020000);
  c.freg2 = (1152u * c.j + fstart(c.q, 0)) * 2u;
  c.f82 = (1152u * c.j + fstart(c.q, 8)) * 2u;
  c.fown = WS_FLAG + (((c.r * 4u + c.j) * 4u + c.q) << 10);
  c.pollv = 0u;
  c.sw1 = (72u * c.t + 40u * c.hf) * W1ROW + c.freg2;
  c.ldst = c.t * 9216u + c.hf * 5120u;
  if (c.hf == 0) block0<0>(c, sx, sb1); else block0<1>(c, sx, sb1);
  {
    const unsigned hb = WS_H + ((c.r * 4u + c.j) * 144u + c.t) * 2048u;
#pragma unroll
    for (int d = 0; d < 4; ++d) c.hq[d] = hb + ((c.q + d) & 3u) * 73728u + c.hf * 8192u;
#pragma unroll
    for (int e = 0; e < 2; ++e) {
      c.h8[e] = hb + ((c.q + 2u * c.hf + e) & 3u) * 73728u + 65536u;
      c.sw2[e] = (1152u * c.j + fstart((c.q + 2u * e + (c.t & 1u)) & 3u, c.t >> 1)) * W2ROW + 288u * c.q;
    }
    c.sw28 = (1152u * c.j + fstart((c.q + c.t) & 3u, 8)) * W2ROW + 288u * c.q;
  }
  asm volatile("" : "+s"(c.sw1), "+s"(c.ldst), "+s"(c.hq[0]), "+s"(c.hq[1]), "+s"(c.hq[2]), "+s"(c.hq[3]), "+s"(c.h8[0]), "+s"(c.h8[1]),
               "+s"(c.sw2[0]), "+s"(c.sw2[1]), "+s"(c.sw28));
  STAMP(12);
  half_stage<0>(c); STAMP(13);
  half_stage<1>(c); STAMP(14);
  half_stage<2>(c); STAMP(15);
  half_stage<3>(c); STAMP(16);
  half_stage<4>(c); STAMP(17);
  half_stage<5>(c); STAMP(18);
  half_stage<6>(c); STAMP(19);
  half_stage<7>(c); STAMP(20);
  half_stage<8>(c); STAMP(21);
  half_stage<9>(c); STAMP(22);
  half_stage<10>(c); STAMP(23);
  half_stage<11>(c); STAMP(24);
  half_stage<12>(c); STAMP(25);
  half_stage<13>(c); STAMP(26);
  half_stage<14>(c); STAMP(27);

  const unsigned l = lane_id();
  {
    const unsigned wa = c.w * 9216u + l * 16u, ra = (c.w ^ 4u) * 9216u + l * 16u;
    if (c.hf) {
#pragma unroll
      for (int dt = 0; dt < 9; ++dt) *LDSP(f32x4, wa + dt * 1024u) = c.acc2[0][dt];
    } else {
#pragma unroll
      for (int dt = 0; dt < 9; ++dt) { *LDSP(f32x4, wa + dt * 1024u) = c.acc2[1][dt]; c.acc2[1][dt] = c.acc2[0][dt]; }
    }
    BARRIER_LGKM();
#pragma unroll
    for (int dt = 0; dt < 9; ++dt) c.acc2[1][dt] += ldsf4(ra + dt * 1024u);
  }
  const f32x4* R = c.acc2[1];
  if (c.t != c.j) {
    const unsigned so = WS_P + (((c.r * 4u + c.q) * 4u + c.t) * 4u + c.j) * 9216u + c.hf * 4608u;
#pragma unroll
    for (int m = 0; m < 4; ++m) {
      u32x4 v = {cvtpk(R[2 * m][0], R[2 * m][1]), cvtpk(R[2 * m][2], R[2 * m][3]), cvtpk(R[2 * m + 1][0], R[2 * m + 1][1]), cvtpk(R[2 * m + 1][2], R[2 * m + 1][3])};
#pragma unroll
      for (int i = 0; i < 4; ++i) v[i] = v[i] < 0xfffffffeu ? v[i] : 0xfffffffeu;
      asm volatile("buffer_store_dwordx4 %0, %1, %2, %3 offen sc1\n\ts_nop 1" ::"v"(v), "v"(c.vhld), "s"(c.sw), "s"(so + m * 1024u));
    }
    u32x2 v = {cvtpk(R[8][0], R[8][1]), cvtpk(R[8][2], R[8][3])};
#pragma unroll
    for (int i = 0; i < 2; ++i) v[i] = v[i] < 0xfffffffeu ? v[i] : 0xfffffffeu;
    asm volatile("buffer_store_dwordx2 %0, %1, %2, %3 offen sc1" ::"v"(v), "v"(l * 8u), "s"(c.sw), "s"(so + 4096u));
  } else {
#pragma unroll
    for (int dt = 0; dt < 9; ++dt) *LDSP(f32x4, 3u * SLOT + c.hf * 9216u + dt * 1024u + l * 16u) = R[dt];
  }
  const unsigned short* b2p = (const unsigned short*)b2 + 144u * c.q + 4u * (l >> 4);
  const u32x2 bA = *(const u32x2*)(b2p + 32u * c.t), bB = *(const u32x2*)(b2p + 32u * c.t + 16u), bC = *(const u32x2*)(b2p + 128u);
  asm volatile("s_waitcnt vmcnt(0) lgkmcnt(0)\n\ts_barrier" ::: "memory");
  STAMP(28);
  {
    const unsigned sp = WS_P + ((c.r * 4u + c.q) * 4u + c.j) * 4u * 9216u + c.hf * 4608u;
    const unsigned s0 = sp + ((c.j + 1u) & 3u) * 9216u, s1 = sp + ((c.j + 2u) & 3u) * 9216u, s2 = sp + ((c.j + 3u) & 3u) * 9216u;
    const unsigned va = c.vhld + c.t * 1024u, vb = 4096u + l * 8u;
    u32x4 pa[3]; u32x2 pb[3];
    f32x4 o0 = ldsf4(3u * SLOT + c.hf * 9216u + c.t * 2048u + l * 16u), o1 = ldsf4(3u * SLOT + c.hf * 9216u + c.t * 2048u + 1024u + l * 16u);
    f32x4 o2 = ldsf4(3u * SLOT + c.hf * 9216u + 8192u + l * 16u);
    for (unsigned spin = 0; spin < (1u << 16); ++spin) {
      asm volatile("buffer_load_dwordx4 %0, %6, %8, %9 offen sc1\n\tbuffer_load_dwordx4 %1, %6, %8, %10 offen sc1\n\t"
                   "buffer_load_dwordx4 %2, %6, %8, %11 offen sc1\n\t"
                   "s_cmp_lg_u32 %12, 0\n\ts_cbranch_scc1 .Lskip%=\n\t"
                   "buffer_load_dwordx2 %3, %7, %8, %9 offen sc1\n\tbuffer_load_dwordx2 %4, %7, %8, %10 offen sc1\n\t"
                   "buffer_load_dwordx2 %5, %7, %8, %11 offen sc1\n.Lskip%=:\n\ts_waitcnt vmcnt(0)"
                   : "=&v"(pa[0]), "=&v"(pa[1]), "=&v"(pa[2]), "=&v"(pb[0]), "=&v"(pb[1]), "=&v"(pb[2])
                   : "v"(va), "v"(vb), "s"(c.sw), "s"(s0), "s"(s1), "s"(s2), "s"(c.t) : "memory", "scc");
      unsigned mx = 0;
#pragma unroll
      for (int k = 0; k < 3; ++k) {
#pragma unroll
        for (int i = 0; i < 4; ++i) mx = mx > pa[k][i] ? mx : pa[k][i];
        if (c.t == 0) { mx = mx > pb[k][0] ? mx : pb[k][0]; mx = mx > pb[k][1] ? mx : pb[k][1]; }
      }
      if (!__any(mx == ~0u)) break;
    }
    STAMP(29);
#pragma unroll
    for (int k = 0; k < 3; ++k)
#pragma unroll
      for (int i = 0; i < 2; ++i) {
        o0[2 * i] += __uint_as_float(pa[k][i] << 16); o0[2 * i + 1] += __uint_as_float(pa[k][i] & 0xffff0000u);
        o1[2 * i] += __uint_as_float(pa[k][2 + i] << 16); o1[2 * i + 1] += __uint_as_float(pa[k][2 + i] & 0xffff0000u);
      }
    unsigned short* op = (unsigned short*)out + (size_t)(128u * c.r + 32u * c.j + 16u * c.hf + (l & 15u)) * D_ + 144u * c.q;
    const unsigned e0 = cvtpk(__builtin_fmaf(o0[0], RKAP, __uint_as_float(bA[0] << 16)), __builtin_fmaf(o0[1], RKAP, __uint_as_float(bA[0] & 0xffff0000u)));
    const unsigned e1 = cvtpk(__builtin_fmaf(o0[2], RKAP, __uint_as_float(bA[1] << 16)), __builtin_fmaf(o0[3], RKAP, __uint_as_float(bA[1] & 0xffff0000u)));
    const unsigned f0 = cvtpk(__builtin_fmaf(o1[0], RKAP, __uint_as_float(bB[0] << 16)), __builtin_fmaf(o1[1], RKAP, __uint_as_float(bB[0] & 0xffff0000u)));
    const unsigned f1 = cvtpk(__builtin_fmaf(o1[2], RKAP, __uint_as_float(bB[1] << 16)), __builtin_fmaf(o1[3], RKAP, __uint_as_float(bB[1] & 0xffff0000u)));
    auto r0 = __builtin_amdgcn_permlane16_swap(e0, f0, false, false);
    auto r1 = __builtin_amdgcn_permlane16_swap(e1, f1, false, false);
    *(u32x4*)(op + 32u * c.t + 16u * ((l >> 4) & 1u) + 8u * (l >> 5)) = (u32x4){r0[0], r1[0], r0[1], r1[1]};
    if (c.t == 0) {
#pragma unroll
      for (int k = 0; k < 3; ++k)
#pragma unroll
        for (int i = 0; i < 2; ++i) { o2[2 * i] += __uint_as_float(pb[k][i] << 16); o2[2 * i + 1] += __uint_as_float(pb[k][i] & 0xffff0000u); }
      *(u32x2*)(op + 128u + 4u * (l >> 4)) =
          (u32x2){cvtpk(__builtin_fmaf(o2[0], RKAP, __uint_as_float(bC[0] << 16)), __builtin_fmaf(o2[1], RKAP, __uint_as_float(bC[0] & 0xffff0000u))),
                  cvtpk(__builtin_fmaf(o2[2], RKAP, __uint_as_float(bC[1] << 16)), __builtin_fmaf(o2[3], RKAP, __uint_as_float(bC[1] & 0xffff0000u)))};
    }
    asm volatile("mixer_code_end:");
    if (c.w == 0 && c.j == 0 && c.q == 0 && l == 0)
      asm volatile("buffer_store_dword %0, off, %1, %2 sc1" ::"v"(c.tag), "s"(c.sw), "s"(WS_EPOCH + c.r * 128u));
  }
  asm volatile("s_waitcnt vmcnt(0)" ::: "memory");
  STAMP(30);
  if (threadIdx.x == 0 && (blk < 4 || blk == 100 || blk == 255)) {
    unsigned xcc = (unsigned)__builtin_amdgcn_s_getreg(20 | (3 << 11)) & 0xfu;
    printf("blk %u xcc %u: %u %u %u | %u %u %u %u %u %u %u %u %u | %u %u %u %u %u %u | %u %u %u %u %u %u %u %u %u | %u %u %u tot %u rt %u\n", blk, xcc,
      c.st[1]-c.st[0], c.st[2]-c.st[1], c.st[3]-c.st[2], c.st[4]-c.st[3], c.st[5]-c.st[4], c.st[6]-c.st[5], c.st[7]-c.st[6], c.st[8]-c.st[7], c.st[9]-c.st[8], c.st[10]-c.st[9], c.st[11]-c.st[10], c.st[12]-c.st[11],
      c.st[13]-c.st[12], c.st[14]-c.st[13], c.st[15]-c.st[14], c.st[16]-c.st[15], c.st[17]-c.st[16], c.st[18]-c.st[17],
      c.st[19]-c.st[18], c.st[20]-c.st[19], c.st[21]-c.st[20], c.st[22]-c.st[21], c.st[23]-c.st[22], c.st[24]-c.st[23], c.st[25]-c.st[24], c.st[26]-c.st[25], c.st[27]-c.st[26],
      c.st[28]-c.st[27], c.st[29]-c.st[28], c.st[30]-c.st[29], c.st[30]-c.st[0], (unsigned)(__builtin_amdgcn_s_memrealtime()-rt0));
  }
  if (threadIdx.x == 0) {
#pragma unroll
    for (int i = 0; i < NSTAMP; ++i) ((unsigned long long*)((char*)ws + WS_STAMP))[blk * 40 + i] = c.st[i];
    ((unsigned long long*)((char*)ws + WS_STAMP))[blk * 40 + 38] = rt0;
    ((unsigned long long*)((char*)ws + WS_STAMP))[blk * 40 + 39] = __builtin_amdgcn_s_memrealtime();
  }
}

extern "C" void kernel_launch(void* const* d_in, const int* in_sizes, int n_in, void* d_out, int out_size, void* d_ws, size_t ws_size,
                              hipStream_t stream) {
  (void)in_sizes; (void)n_in; (void)out_size; (void)ws_size;
  (void)hipFuncSetAttribute((const void*)mixer_mlp_kernel, hipFuncAttributeMaxDynamicSharedMemorySize, LDS_BYTES);
  mixer_mlp_kernel<<<256, 512, LDS_BYTES, stream>>>(d_in[0], d_in[1], d_in[2], d_in[3], d_in[4], d_out, d_ws);
}
```

```cpp
#include <hip/hip_runtime.h>
#include <stdint.h>
#include <utility>

typedef __bf16 bf16x8 __attribute__((ext_vector_type(8)));
typedef __bf16 bf16x2 __attribute__((ext_vector_type(2)));
typedef short s16x4 __attribute__((ext_vector_type(4)));
typedef float f32x16 __attribute__((ext_vector_type(16)));
typedef float f32x4 __attribute__((ext_vector_type(4)));
typedef float f32x2 __attribute__((ext_vector_type(2)));
typedef unsigned u32x4 __attribute__((ext_vector_type(4)));
typedef unsigned u32x2 __attribute__((ext_vector_type(2)));
typedef __amdgpu_buffer_rsrc_t srd_t;
#define DI __device__ __forceinline__
#define LAS __attribute__((address_space(3)))
#define LDSP(T, a) ((LAS T*)(size_t)(a))

constexpr unsigned S_ = 2048, D_ = 576, F_ = 4608;
constexpr unsigned XROW = D_ * 2, W1ROW = F_ * 2, W2ROW = D_ * 2;
constexpr unsigned SLOT = 36864, RING = 4 * SLOT, SLICE = 28672;
constexpr unsigned L_B1 = RING, L_PAT = L_B1 + 1152, L_JUNK = L_PAT + 2304, L_EX = L_JUNK + 256, LDS_BYTES = L_EX + 8192;
static_assert(LDS_BYTES <= 163840, "LDS");
constexpr unsigned WS_EPOCH = 0, WS_FLAG = 4096, WS_Y = WS_FLAG + 256 * 8 * 128, WS_H = WS_Y + S_ * D_ * 2, H_BYTES = S_ * F_ * 2;
constexpr unsigned WS_P = WS_H + H_BYTES, P_BYTES = 16 * 4 * 4 * 4 * 9216;
constexpr unsigned WS_USED = WS_P + P_BYTES;
constexpr float KAP = 0.4686686930532063f, GAM = 4.91222953926424f, RKAP = 2.1337034344781243f;
#define KSW 10
#define PSLEEP 1
#define TOUCH 1
#define PREFH 1
#define HK2 2
#define CPREF 1
#define W1PREF 1
#define PREF 1
#define STAMP(i) do {} while (0)

DI unsigned rfl(unsigned x) { return __builtin_amdgcn_readfirstlane(x); }
DI s16x4 trr(unsigned a) { return __builtin_amdgcn_ds_read_tr16_b64_v4i16(LDSP(s16x4, a)); }
DI bf16x8 cat(s16x4 lo, s16x4 hi) { return __builtin_bit_cast(bf16x8, __builtin_shufflevector(lo, hi, 0, 1, 2, 3, 4, 5, 6, 7)); }
DI f32x4 ldsf4(unsigned a) { return *LDSP(f32x4, a); }
DI unsigned cvtpk(float lo, float hi) { f32x2 v = {lo, hi}; return __builtin_bit_cast(unsigned, __builtin_convertvector(v, bf16x2)); }
template <int CTRL> DI float dpp_add(float v) {
  return v + __builtin_bit_cast(float, __builtin_amdgcn_update_dpp(0, __builtin_bit_cast(int, v), CTRL, 0xf, 0xf, false));
}
#define MFMA32(a, b, c) __builtin_amdgcn_mfma_f32_32x32x16_bf16((a), (b), (c), 0, 0, 0)
#define MFMA16(a, b, c) __builtin_amdgcn_mfma_f32_16x16x32_bf16((a), (b), (c), 0, 0, 0)
DI void mfma32(f32x16& acc, bf16x8 a, bf16x8 b) { asm volatile("v_mfma_f32_32x32x16_bf16 %0, %1, %2, %0" : "+v"(acc) : "v"(a), "v"(b)); }
DI void mfma16(f32x4& acc, bf16x8 a, bf16x8 b) { asm volatile("v_mfma_f32_16x16x32_bf16 %0, %1, %2, %0" : "+v"(acc) : "v"(a), "v"(b)); }
DI void mfma16z(f32x4& acc, bf16x8 a, bf16x8 b) { asm volatile("v_mfma_f32_16x16x32_bf16 %0, %1, %2, 0" : "=&v"(acc) : "v"(a), "v"(b)); }
#define BARRIER_LGKM() asm volatile("s_waitcnt lgkmcnt(0)\n\ts_barrier" ::: "memory")
#define GAP() __builtin_amdgcn_sched_barrier(0)
#define PIN(x) asm volatile("" : "+v"(x))

struct Ctx {
  srd_t s1, s2, sw;
  unsigned long long pc;
  unsigned w, t, hf, r, j, q;
  unsigned freg2, f82;
  unsigned tag, sdma, sw1, ldst, hq[4], h8[2], sw2[2], sw28, fown;
  u32x4 vpat;
  unsigned vpat4;
  unsigned wb;
  unsigned vhld, vhst, tb, pref, tch;
  float gz[2], gm, gz8, gm8;
  unsigned own[4][8], hv8[8];
  bf16x8 X[36];
  bf16x8 F[4];
  f32x16 acc[2];
  float a8[8];
  f32x4 acc2[2][9];
  u32x4 Hb[8][2];
  unsigned pollv;
};
DI unsigned lane_id() {
  unsigned l;
  asm volatile("v_mbcnt_lo_u32_b32 %0, -1, 0\n\tv_mbcnt_hi_u32_b32 %0, -1, %0" : "=v"(l));
  return l;
}
DI unsigned opq(unsigned v) { PIN(v); return v; }

constexpr int pair_of(int h) { return h < 8 ? 0 : (h - 7) / 2; }
constexpr int ph_of(int h) { return h < 8 ? h - 6 : (h - 7) % 2; }
constexpr bool own_unit(int n) { return n == 0 || n == 6 || n == 10 || n == 14; }
constexpr int hload_at(int h, int k) {
  if (h == 8 || h == 10 || h == 12) return k == 0 ? 4 * (h - 8) + 14 : k == HK2 ? 4 * (h - 8) + 15 : -1;
  const int slot = k == 0 ? 0 : k == 2 ? 1 : k == 10 ? 2 : k == 12 ? 3 : k == 1 ? 4 : k == 3 ? 5 : -1;
  if (slot < 0) return -1;
  if (h == 5) return slot < 4 ? 2 + slot : -1;
  if (h == 6) return k < 4 ? 6 + k : 8 + slot;
  if (h < 8 || h > 13 || slot > 3) return -1;
  return 12 + 4 * (h - 8) + slot;
}
constexpr int dma_at(int h, int k) {
  if (h == 0) return (k < 16 && (k & 1) == 0) ? (k < 8 ? 2 * 8 + (k + 2) / 2 : 3 * 8 + (k - 8) / 2) : (k == 15 ? 3 * 8 + 4 : -1);
  if (h + 3 > 14) return -1;
  const int i = h < 4 ? (k == -2 ? 0 : k == 2 ? 1 : k == 6 ? 2 : k == 10 ? 3 : k == 13 ? 4 : -1)
                      : (k == -2 ? 0 : k == 4 ? 1 : k == 6 ? 2 : k == 8 ? 3 : k == 14 ? 4 : -1);
  return i < 0 ? -1 : (h + 3) * 8 + i;
}
constexpr int poll_k(int h) { return h == 4 ? 5 : (h == 7 || h == 9 || h == 10 || h == 11) ? -1 : -9; }
constexpr bool poll_at(int h, int k) { return k == poll_k(h); }
constexpr int flag_at(int h) { return (h == 3 || h == 5 || h == 7 || h == 9) ? (h + 1) / 2 : 0; }
constexpr bool hst_at(int h, int k) { return (h < 8 && (h & 1) && (k == 0 || k == 15)) || (h == 1 && k == 12); }
constexpr bool pref_at(int h, int k) { return PREF && h < 6 && k == 15; }
constexpr bool touch_at(int h, int k) { return TOUCH && (h == 2 || h == 4 || h == 6 || h == 8) && k == 13; }
constexpr bool mark_at(int h, int k) { return (h == 2 && (k == 4 || k == 12)) || (h == 4 && k == 2); }
constexpr int nops(int h, int k, int n) {
  return (hload_at(h, k) >= 0 ? 1 : 0) + (hst_at(h, k) ? 1 : 0) + (mark_at(h, k) ? 1 : 0) +
         (dma_at(h, k) >= 0 && (dma_at(h, k) & 7) < n ? 1 : 0);
}
constexpr int between(int h0, int k0, int h1, int k1, int n) {
  int s = 0;
  for (int h = h0; h <= h1; ++h)
    for (int k = -2; k < 16; ++k)
      if ((h > h0 || k > k0) && (h < h1 || k < k1)) s += nops(h, k, n);
  for (int h = h0; h <= h1; ++h)
    if (pref_at(h, 15) && (h < h1 || k1 == 16)) s += 1;
  return s;
}
constexpr int cmin(int a, int b) { return a < b ? a : b; }

DI unsigned fstart(unsigned qq, unsigned ch) { return 32u * (9u * qq + ch + (qq & 1u) * (1u - 9u * (ch >> 3))); }

DI void dma16(unsigned m0v, unsigned voff, srd_t srd, unsigned soff) {
  asm volatile("s_mov_b32 m0, %0\n\ts_nop 0\n\tbuffer_load_dwordx4 %1, %2, %3 offen lds" ::"s"(m0v), "v"(voff), "s"(srd), "s"(soff));
}
template <int HS, int I> DI void issue_piece(Ctx& c) {
  const srd_t sd = HS < 6 ? c.s1 : c.s2;
  if constexpr (I == 0) {
    if constexpr (HS < 6) c.sdma = c.sw1 + ((HS & 1) * 288u * W1ROW + ((HS >> 1) + 1) * 128u);
    else c.sdma = HS == 8 ? c.sw28 : c.sw2[ph_of(HS)] + pair_of(HS) * 64u * W2ROW;
    dma16((HS & 3) * SLOT + c.ldst, c.vpat[0], sd, c.sdma);
  } else if constexpr (I < 4) {
    asm volatile("buffer_load_dwordx4 %0, %1, %2 offen offset:%3 lds" ::"v"(c.vpat[I]), "s"(sd), "s"(c.sdma), "i"(1024 * I));
  } else {
    asm volatile("s_cmp_lg_u32 %4, 0\n\ts_cbranch_scc1 .Lskip%=\n\ts_mov_b32 m0, %0\n\ts_nop 0\n\tbuffer_load_dwordx4 %1, %2, %3 offen lds\n.Lskip%=:"
                 ::"s"((HS & 3) * SLOT + 4096u + c.ldst), "v"(c.vpat4), "s"(sd), "s"(c.sdma), "s"(c.hf) : "scc");
  }
}
template <int HS> DI void issue_slot(Ctx& c) {
  issue_piece<HS, 0>(c); issue_piece<HS, 1>(c); issue_piece<HS, 2>(c); issue_piece<HS, 3>(c); issue_piece<HS, 4>(c);
}
template <int N, int U> DI void issue_hload(Ctx& c) {
  constexpr int h = 6 + N / 2;
  const unsigned so = h == 8 ? c.h8[N & 1] : c.hq[2 * ph_of(h) + (N & 1)] + pair_of(h) * 16384u;
  asm volatile("buffer_load_dwordx4 %0, %1, %2, %3 offen offset:%4 sc1" : "=v"(c.Hb[N & 7][U]) : "v"(c.vhld), "s"(c.sw), "s"(so), "i"(1024 * U));
}
DI unsigned poll_off(const Ctx& c, bool dest) {
  const unsigned l = lane_id(), dp = (l & 3u) ? (l & 3u) : 1u;
  return WS_FLAG + ((dest ? (c.r * 4u + ((c.j + dp) & 3u)) * 4u + c.q : (c.r * 4u + c.j) * 4u + ((c.q + dp) & 3u)) << 10);
}
template <int EV> DI void store_flag(Ctx& c) {
  asm volatile("s_cmp_lg_u32 %3, 0\n\ts_cbranch_scc1 .Lskip%=\n\ts_mov_b64 exec, 1\n\t"
               "buffer_store_dword %0, off, %1, %2 offset:%4 sc1\n\ts_mov_b64 exec, -1\n.Lskip%=:"
               ::"v"(c.tag), "s"(c.sw), "s"(c.fown), "s"(c.w), "i"(EV * 128) : "scc");
}
template <int EV> DI void issue_poll(Ctx& c, unsigned& pv, unsigned voff) {
  asm volatile("s_cmp_lg_u32 %3, 7\n\ts_cbranch_scc1 .Lskip%=\n\tbuffer_load_dword %0, %1, %2, 0 offen offset:%4 sc1\n.Lskip%=:"
               : "+v"(pv) : "v"(voff), "s"(c.sw), "s"(c.w), "i"(EV * 128) : "scc");
}
template <int EV, int N> DI void check_poll(Ctx& c, unsigned& pv, bool dest) {
  if (c.w == 7) {
    asm volatile("s_waitcnt vmcnt(%1)" : "+v"(pv) : "i"(N) : "memory");
    if (__builtin_expect(__builtin_amdgcn_ballot_w64(pv != c.tag) != 0, 0)) {
      const unsigned voff = poll_off(c, dest);
      for (unsigned spin = 0; spin < (1u << 16); ++spin) {
        unsigned v;
        asm volatile("s_sleep 1\n\tbuffer_load_dword %0, %1, %2, 0 offen offset:%3 sc1\n\ts_waitcnt vmcnt(0)" : "=&v"(v) : "v"(voff), "s"(c.sw), "i"(EV * 128) : "memory");
        if (__builtin_amdgcn_ballot_w64(v != c.tag) == 0) break;
      }
    }
  }
}
template <int P> DI void store_h(Ctx& c, const unsigned (&hv)[8], unsigned so) {
  const u32x4 v = {hv[4 * P], hv[4 * P + 1], hv[4 * P + 2], hv[4 * P + 3]};
  asm volatile("buffer_store_dwordx4 %0, %1, %2, %3 offen sc1\n\ts_nop 1" ::"v"(v), "v"(c.vhst), "s"(c.sw), "s"(so));
}
template <int N0, int N1> DI void wait_vm(const Ctx&) {
  static_assert(N1 <= N0 && N1 >= 0 && N0 < 64, "vmcnt");
  asm volatile("s_waitcnt vmcnt(%0)" ::"i"(N1) : "memory");
}

template <int H> DI void set_bases(Ctx& c) {
  constexpr unsigned d = (H & 3) ? SLOT : 0u - 3u * SLOT;
  if constexpr (H == 6) {
    const unsigned l = lane_id(), g = l >> 4;
    c.wb = opq(2u * SLOT + (16u * (g >> 1) + 4u * (g & 1u) + ((l >> 2) & 3u)) * 288u + 8u * (l & 3u) + c.hf * 18432u);
  } else if constexpr (H > 0) c.wb = opq(c.wb + d);
}
template <int H, int S> DI void rd(Ctx& c) {
  constexpr unsigned o = H < 6 ? 2048u * S : 9216u * (S / 9) + 32u * (S % 9), e = H < 6 ? 512u : 2304u;
  c.F[(H * 18 + S) & 3] = cat(trr(c.wb + o), trr(c.wb + o + e));
}
template <int H, int S> DI void mm(Ctx& c) {
  const bf16x8 a = c.F[(H * 18 + S) & 3];
  if constexpr (H < 6) {
    constexpr int B = ((H >> 1) + 1) & 1;
    mfma32(c.acc[B], a, c.X[18 * (H & 1) + S]);
  } else {
    constexpr int N = 2 * (H - 6) + S / 9, dt = S % 9;
    u32x4 b0 = c.Hb[N & 7][0], b1 = c.Hb[N & 7][1];
    if constexpr (own_unit(N)) {
      const unsigned(&o)[8] = c.own[pair_of(H)];
      b0 = (u32x4){o[0], o[1], o[2], o[3]}; b1 = (u32x4){o[4], o[5], o[6], o[7]};
    }
    if constexpr (N == 0) {
      mfma16z(c.acc2[0][dt], a, __builtin_bit_cast(bf16x8, b0));
      mfma16z(c.acc2[1][dt], a, __builtin_bit_cast(bf16x8, b1));
    } else {
      mfma16(c.acc2[0][dt], a, __builtin_bit_cast(bf16x8, b0));
      mfma16(c.acc2[1][dt], a, __builtin_bit_cast(bf16x8, b1));
    }
  }
}
template <int Q> DI void bias_quad(f32x16& acc, unsigned a) {
  const f32x4 v = ldsf4(a + (Q >> 1) * 64u + (Q & 1) * 16u);
#pragma unroll
  for (int i = 0; i < 4; ++i) acc[4 * Q + i] = v[i];
}
template <int E, int ST> DI void gelu_stage(float s, float& z, float& m0, unsigned (&hv)[8]) {
  if constexpr (ST == 0) { z = s * __builtin_fmaf(s, s, GAM); PIN(z); }
  if constexpr (ST == 1) { z = __builtin_amdgcn_exp2f(-z); PIN(z); }
  if constexpr (ST == 2) { z = __builtin_amdgcn_rcpf(1.0f + z); PIN(z); }
  if constexpr (ST == 3) {
    if constexpr ((E & 1) == 0) { m0 = s * z; PIN(m0); }
    else { float m = s * z, mm0 = m0; PIN(m); PIN(mm0); hv[E >> 1] = cvtpk(mm0, m); PIN(hv[E >> 1]); }
  }
}
constexpr int gstart(int e) { return 1 + (e * 29) / 15; }
constexpr int gelem(int g, int st) {
  for (int e = 0; e < 16; ++e) if (gstart(e) + st == g) return e;
  return -1;
}
template <int B, int G, int ST> DI void gelu_part(Ctx& c) {
  constexpr int e = gelem(G, ST);
  if constexpr (e >= 0) gelu_stage<e, ST>(c.acc[B & 1][e], c.gz[e & 1], c.gm, c.own[B]);
}
template <int B, int G> DI void gelu_gap(Ctx& c) {
  if constexpr (G == 1) asm volatile("s_nop 7" : "+v"(c.acc[B & 1]));
  gelu_part<B, G, 3>(c); gelu_part<B, G, 2>(c); gelu_part<B, G, 1>(c); gelu_part<B, G, 0>(c);
  if constexpr (B < 2) {
    if constexpr (G == 27) { bias_quad<0>(c.acc[B & 1], c.tb + (B + 2) * 256u); bias_quad<1>(c.acc[B & 1], c.tb + (B + 2) * 256u); }
    if constexpr (G == 31) bias_quad<2>(c.acc[B & 1], c.tb + (B + 2) * 256u);
    if constexpr (G == 33) bias_quad<3>(c.acc[B & 1], c.tb + (B + 2) * 256u);
  }
}

template <int H0, int K0, int H1, int K1> DI void wait_after(const Ctx& c) {
  wait_vm<between(H0, K0, H1, K1, 5), between(H0, K0, H1, K1, 4)>(c);
}
template <int H, int K> DI void side(Ctx& c) {
  if constexpr (poll_at(H, K)) {
    if constexpr (H == 10) issue_poll<4>(c, c.pollv, poll_off(c, true));
    else issue_poll<(H == 4 ? 2 : (H - 1) / 2)>(c, c.pollv, c.pref);
  }
  if constexpr (K == -1 && flag_at(H)) store_flag<flag_at(H)>(c);
  if constexpr (H == 4 && K == 15) c.vhld = lane_id() * 16u;
  if constexpr (H == 3 && K == 11) { c.pref = poll_off(c, false); PIN(c.pref); }
  if constexpr (hload_at(H, K) >= 0) issue_hload<(hload_at(H, K) >> 1), (hload_at(H, K) & 1)>(c);
  if constexpr (mark_at(H, K)) {
    constexpr unsigned k = H == 4 ? 2u : K == 4 ? 0u : 1u;
    const u32x4 ones = {~0u, ~0u, ~0u, ~0u};
    const unsigned so = WS_P + (((c.r * 4u + c.q) * 4u + c.j) * 4u + ((c.j + 1u + k) & 3u)) * 9216u, vo = lane_id() * 16u;
    asm volatile("buffer_store_dwordx4 %0, %1, %2, %3 offen sc1\n\ts_cmp_lg_u32 %4, %5\n\ts_cbranch_scc1 .Lskip%=\n\t"
                 "buffer_store_dwordx4 %0, %1, %2, %6 offen sc1\n.Lskip%=:\n\ts_nop 1"
                 ::"v"(ones), "v"(vo), "s"(c.sw), "s"(so + c.w * 1024u), "s"(c.w), "i"(k), "s"(so + 8192u) : "scc");
  }
  if constexpr (dma_at(H, K) >= 0) issue_piece<(dma_at(H, K) >> 3), (dma_at(H, K) & 7)>(c);
  if constexpr (H == 2 && K == 14) {
    const unsigned l = lane_id();
#pragma unroll
    for (int i = 0; i < 4; ++i) c.vpat[i] = *LDSP(unsigned, L_PAT + (c.hf * 5u + i) * 256u + l * 4u);
    c.vpat4 = *LDSP(unsigned, L_PAT + 1024u + l * 4u);
  }
  if constexpr (H == 0 && K == -2) {
    if (c.hf) {
      const unsigned l = lane_id();
      const f32x4 e0 = ldsf4(L_EX + c.t * 2048u + l * 16u), e1 = ldsf4(L_EX + c.t * 2048u + 1024u + l * 16u);
#pragma unroll
      for (int i = 0; i < 4; ++i) { c.a8[i] = e0[i]; c.a8[4 + i] = e1[i]; }
    }
  }
  if constexpr (H < 8) gelu_gap<(H >> 1), K + 18 * (H & 1)>(c);
  if constexpr (H < 2) {
    constexpr int G = K + 18 * H - 1;
    if constexpr (G >= 0 && G < 28) gelu_stage<G / 4, (G & 3)>(c.a8[G / 4], c.gz8, c.gm8, c.hv8);
    if constexpr (G == 28) { gelu_stage<7, 0>(c.a8[7], c.gz8, c.gm8, c.hv8); gelu_stage<7, 1>(c.a8[7], c.gz8, c.gm8, c.hv8); }
    if constexpr (G == 29) { gelu_stage<7, 2>(c.a8[7], c.gz8, c.gm8, c.hv8); gelu_stage<7, 3>(c.a8[7], c.gz8, c.gm8, c.hv8); }
  }
  if constexpr (hst_at(H, K)) {
    if constexpr (K == 12) store_h<0>(c, c.hv8, c.hq[0] + 65536u - c.hf * 7936u);
    else store_h<(K == 0 ? 0 : 1)>(c, c.own[H >> 1], c.hq[0] + (H >> 1) * 16384u + (K == 0 ? 0u : 256u));
  }
  if constexpr (pref_at(H, K)) {
    const unsigned m = 64u * H + 8u * c.w + (lane_id() & 7u), ln = ((c.r & 7u) * 4u + c.q) + 32u * (m < 324u ? m : 323u), rr = ln / 9u;
    asm volatile("s_mov_b32 m0, %0\n\ts_nop 0\n\tbuffer_load_dword %1, %2, %3 offen lds" ::"s"(L_JUNK),
                 "v"((288u * (rr & 3u) + (rr >> 2)) * W2ROW + (ln - rr * 9u) * 128u), "s"(c.s2), "s"(c.j * 1152u * W2ROW));
  }
  if constexpr (H >= 2 && H <= 8 && (H & 1) == 0 && (K == 1 || K == 3 || K == 5 || K == 7)) {
    unsigned(&o)[8] = c.own[(H >> 1) - 1];
    auto rr = __builtin_amdgcn_permlane16_swap(o[K >> 1], o[4 + (K >> 1)], false, false);
    o[K >> 1] = rr[0]; o[4 + (K >> 1)] = rr[1];
    PIN(o[K >> 1]); PIN(o[4 + (K >> 1)]);
  }
}
constexpr int hpos(int n) {
  for (int h = 4; h < 14; ++h) for (int k = -2; k < 16; ++k) if (hload_at(h, k) == 2 * n + 1) return h * 32 + k + 2;
  return 0;
}
constexpr int cmax(int a, int b) { return a > b ? a : b; }
template <int H> DI void end_hs(Ctx& c) {
  if constexpr (H < 14) {
    constexpr int p0 = H == 0 ? -1 : H == 1 ? 8 : (H - 2) * 32 + 17;
    constexpr int p1 = (H >= 5 && !own_unit(2 * (H - 5))) ? hpos(2 * (H - 5)) : 0, p2 = 0, p3 = flag_at(H + 1) ? (H - 1) * 32 + 17 : 0;
    constexpr int p = H == 0 ? -1 : cmax(cmax(p0, p1), cmax(p2, p3));
    wait_after<(p < 0 ? 0 : p / 32), (p < 0 ? -3 : p % 32 - 2), H, 16>(c);
    if constexpr (poll_k(H) > -9) check_poll<(H == 10 ? 4 : H == 4 ? 2 : (H - 1) / 2), between(H, poll_k(H), H, 16, 4)>(c, c.pollv, H == 10);
    BARRIER_LGKM();
  }
  if constexpr (H >= 5 && H < 14 && !own_unit(2 * (H - 5))) { PIN(c.Hb[(2 * (H - 5)) & 7][0]); PIN(c.Hb[(2 * (H - 5)) & 7][1]); }
  GAP();
}
template <int H> DI void mid_wait(Ctx& c) {
  if constexpr (H >= 6) {
    constexpr int p = hpos(2 * (H - 6) + 1);
    wait_after<p / 32, p % 32 - 2, H, 9>(c);
    PIN(c.Hb[(2 * (H - 6) + 1) & 7][0]); PIN(c.Hb[(2 * (H - 6) + 1) & 7][1]);
  }
}
#define GST(H, i) do {} while (0)
template <int H, int K> DI void step(Ctx& c) {
  GST(H, K + 2);
  if constexpr (K == 9) mid_wait<H>(c);
  if constexpr (K == KSW) asm volatile("s_setprio 0");
  mm<H, K>(c);
  if constexpr (K + 4 < 18) rd<H, K + 4>(c);
  side<H, K>(c);
  GAP();
}
template <int H, int... Ks> DI void steps(Ctx& c, std::integer_sequence<int, Ks...>) { (step<H, Ks>(c), ...); }
DI void prio_young(const Ctx& c) {
  asm volatile("s_cmp_eq_u32 %0, 0\n\ts_cbranch_scc1 .Lskip%=\n\ts_setprio 1\n.Lskip%=:" ::"s"(c.hf) : "scc");
}
template <int H> DI void half_stage(Ctx& c) {
  prio_young(c);
  set_bases<H>(c);
  rd<H, 0>(c); rd<H, 1>(c);
  GAP();
  GST(H, 0);
  if constexpr (H > 0) mm<H - 1, 16>(c); else mfma32(c.acc[0], c.F[2], c.X[34]);
  rd<H, 2>(c);
  side<H, -2>(c);
  GAP();
  GST(H, 1);
  if constexpr (H > 0) mm<H - 1, 17>(c); else mfma32(c.acc[0], c.F[3], c.X[35]);
  rd<H, 3>(c);
  side<H, -1>(c);
  GAP();
  steps<H>(c, std::make_integer_sequence<int, 16>{});
  if constexpr (H == 14) { mm<14, 16>(c); mm<14, 17>(c); GAP(); asm volatile("s_nop 15" ::: "memory"); GAP(); }
  GST(H, 18);
  end_hs<H>(c);
  GST(H, 20);
}

struct B0 {
  unsigned vy, vw1, vc8, xa[4], l8, l80, lx, lw;
  bf16x8 G[4];
  f32x16 accB;
};
template <int S> DI void issue_y(Ctx& c, B0& b) {
  const unsigned dst = (S % 5) * SLICE + c.w * 1024u, so = WS_Y + c.r * 128u * XROW + S * 128u;
  asm volatile("s_mov_b32 m0, %0\n\ts_nop 0\n\tbuffer_load_dwordx4 %1, %2, %3 offen sc1 lds" ::"s"(dst), "v"(b.vy), "s"(c.sw), "s"(so));
  asm volatile("s_mov_b32 m0, %0\n\ts_nop 0\n\tbuffer_load_dwordx4 %1, %2, %3 offen sc1 lds" ::"s"(dst + 8192u), "v"(b.vy), "s"(c.sw), "s"(so + 64u * XROW));
}
template <int HF, int S> DI void issue_w(Ctx& c, B0& b) {
  const unsigned dst = (S % 5) * SLICE, kk = S * 64u;
  dma16(dst + c.w * 1024u + 16384u, b.vw1, c.s1, (kk + 8u * c.w) * W1ROW + c.freg2);
  if constexpr (HF == 0) dma16(dst + c.t * 1024u + 24576u, b.vc8, c.s1, (kk + 16u * c.t) * W1ROW + c.f82);
}
template <int HF, int S, int K> DI void rd0(Ctx& c, B0& b) {
  c.X[S * 4 + K] = *LDSP(bf16x8, b.xa[K]);
  c.F[K] = cat(trr(c.wb + 2048u * K), trr(c.wb + 2048u * K + 512u));
  if constexpr (HF == 0) b.G[K] = cat(trr(b.l8 + 1024u * K), trr(b.l8 + 1024u * K + 256u));
}
template <int S, int K> DI void mmA(Ctx& c) { mfma32(c.acc[0], c.F[K], c.X[S * 4 + K]); }
template <int HF, int S, int K> DI void mmB(Ctx& c, B0& b) {
  if constexpr (HF == 0) mfma32(b.accB, b.G[K], c.X[S * 4 + K]);
}
template <int HF, int S> DI void bases0(Ctx& c, B0& b) {
  constexpr unsigned base = (S % 5) * SLICE;
#pragma unroll
  for (int k = 0; k < 4; ++k) b.xa[k] = opq(base + (b.lx ^ (unsigned)(k << 5)));
  c.wb = opq(base + 16384u + b.lw);
  if constexpr (HF == 0) b.l8 = opq(base + 24576u + b.l80);
}
template <int HF, int S> DI void slice(Ctx& c, B0& b) {
  constexpr int n0 = HF ? 3 : 4, n = HF ? 4 : 5, M = 0;
  constexpr int N = S == 0 ? 2 : S == 1 ? 4 : S == 2 ? 4 + n0 : S == 3 ? 2 + 2 * n0 : S <= 5 ? 3 * n0 : S == 6 ? 2 * n0 + M : S == 7 ? n0 + M : M + n;
  asm volatile("s_waitcnt vmcnt(%0) lgkmcnt(0)\n\ts_barrier" ::"i"(N) : "memory");
  STAMP(3 + S);
  GAP();
  if constexpr (S > 0) mmA<S - 1, 2>(c);
  rd0<HF, S, 0>(c, b);
  GAP();
  if constexpr (S > 0) mmB<HF, S - 1, 2>(c, b);
  rd0<HF, S, 1>(c, b);
  GAP();
  if constexpr (S > 0) mmA<S - 1, 3>(c);
  if constexpr (S <= 1) issue_y<2 * S + 2>(c, b);
  if constexpr (S >= 2 && S <= 4) issue_y<S + 4>(c, b);
  if constexpr (S == 7) issue_slot<0>(c);
  if constexpr (S == 8) issue_slot<1>(c);
  GAP();
  if constexpr (S > 0) mmB<HF, S - 1, 3>(c, b);
  rd0<HF, S, 2>(c, b);
  GAP();
  mmA<S, 0>(c);
  rd0<HF, S, 3>(c, b);
  GAP();
  mmB<HF, S, 0>(c, b);
  if constexpr (S <= 1) issue_y<2 * S + 3>(c, b);
  if constexpr (S >= 1 && S <= 4) issue_w<HF, S + 4>(c, b);
  if constexpr (S == 6) {
#pragma unroll
    for (int i = 0; i < 4; ++i) c.vpat[i] = b.vw1 + i * (8u * W1ROW - 1024u);
    c.vpat4 = b.vw1 + 32u * W1ROW;
    bias_quad<0>(c.acc[1], c.tb + 256u); bias_quad<1>(c.acc[1], c.tb + 256u); bias_quad<2>(c.acc[1], c.tb + 256u); bias_quad<3>(c.acc[1], c.tb + 256u);
  }
  if constexpr (S < 8) bases0<HF, S + 1>(c, b);
  GAP();
  mmA<S, 1>(c);
  GAP();
  mmB<HF, S, 1>(c, b);
  GAP();
}
DI float half_sum(float v) {
  v = dpp_add<0x121>(v); v = dpp_add<0x122>(v); v = dpp_add<0x124>(v); v = dpp_add<0x128>(v);
  auto a = __builtin_amdgcn_permlane16_swap(__float_as_uint(v), __float_as_uint(v), false, false);
  return __uint_as_float(a[0]) + __uint_as_float(a[1]);
}
template <int HF> DI void block0(Ctx& c, const srd_t sx, const srd_t sb1) {
  B0 b;
  const unsigned l = threadIdx.x & 63u, g = l >> 4, qq = (l >> 2) & 3u, pp = l & 3u, h = l >> 5;
  unsigned epoch;
  b.vw1 = (l >> 3) * W1ROW + (((l & 7u) ^ ((g & 1u) << 2)) << 4);
  b.vc8 = (l >> 2) * W1ROW + (pp << 4);
  if constexpr (HF == 0) {
    const unsigned row = (c.r * 128u + (c.j * 4u + c.q) * 8u + 2u * c.t) * XROW, va = h * XROW + (l & 31u) * 16u, vb = h * XROW + 1024u + (l & 31u) * 4u;
    u32x4 xa, xc; unsigned xb;
    asm volatile("buffer_load_dwordx4 %0, %1, %2, %3 offen" : "=v"(xa) : "v"(va), "s"(sx), "s"(row));
    asm volatile("buffer_load_dwordx4 %0, %1, %2, %3 offen offset:512" : "=v"(xc) : "v"(va), "s"(sx), "s"(row));
    asm volatile("buffer_load_dword %0, %1, %2, %3 offen" : "=v"(xb) : "v"(vb), "s"(sx), "s"(row));
    asm volatile("buffer_load_dword %0, off, %1, %2 sc1" : "=v"(epoch) : "s"(c.sw), "s"(WS_EPOCH + c.r * 128u));
    GAP();
    asm volatile("s_waitcnt vmcnt(1)" : "+v"(xa), "+v"(xb), "+v"(xc)::"memory");
    float v[18];
#pragma unroll
    for (int i = 0; i < 4; ++i) {
      v[2 * i] = __uint_as_float(xa[i] << 16); v[2 * i + 1] = __uint_as_float(xa[i] & 0xffff0000u);
      v[8 + 2 * i] = __uint_as_float(xc[i] << 16); v[9 + 2 * i] = __uint_as_float(xc[i] & 0xffff0000u);
    }
    v[16] = __uint_as_float(xb << 16); v[17] = __uint_as_float(xb & 0xffff0000u);
    float s0 = v[16], s1 = v[17];
#pragma unroll
    for (int i = 0; i < 8; ++i) { s0 += v[2 * i]; s1 += v[2 * i + 1]; }
    const float mean = half_sum(s0 + s1) * (1.0f / 576.0f);
#pragma unroll
    for (int i = 0; i < 18; ++i) v[i] -= mean;
    s0 = v[16] * v[16]; s1 = v[17] * v[17];
#pragma unroll
    for (int i = 0; i < 8; ++i) { s0 = __builtin_fmaf(v[2 * i], v[2 * i], s0); s1 = __builtin_fmaf(v[2 * i + 1], v[2 * i + 1], s1); }
    const float rs = KAP * __builtin_amdgcn_rsqf(half_sum(s0 + s1) * (1.0f / 576.0f) + 1e-5f);
    u32x4 ya, yc;
#pragma unroll
    for (int i = 0; i < 4; ++i) { ya[i] = cvtpk(v[2 * i] * rs, v[2 * i + 1] * rs); yc[i] = cvtpk(v[8 + 2 * i] * rs, v[9 + 2 * i] * rs); }
    const unsigned yb = cvtpk(v[16] * rs, v[17] * rs);
    asm volatile("buffer_store_dwordx4 %0, %1, %2, %3 offen sc1" ::"v"(ya), "v"(va), "s"(c.sw), "s"(WS_Y + row));
    asm volatile("buffer_store_dwordx4 %0, %1, %2, %3 offen offset:512 sc1" ::"v"(yc), "v"(va), "s"(c.sw), "s"(WS_Y + row));
    asm volatile("buffer_store_dword %0, %1, %2, %3 offen sc1\n\ts_nop 1" ::"v"(yb), "v"(vb), "s"(c.sw), "s"(WS_Y + row));
    GAP();
    STAMP(1);
  b.vy = ((l >> 3) + 8u * c.w) * XROW + (((l & 7u) ^ ((g + 4u * c.w) & 7u)) << 4);
  b.lw = (8u * h + qq) * 128u + (((4u * HF + 2u * (g & 1u) + (pp & 1u)) ^ (((qq >> 1) & 1u) << 2)) << 4) + 8u * (pp >> 1);
  const unsigned xrow = 32u * c.t + (l & 31u);
  b.lx = xrow * 128u + ((h ^ ((xrow >> 1) & 7u)) << 4);
  b.l80 = (8u * h + qq) * 64u + 32u * (g & 1u) + 16u * (pp & 1u) + 8u * (pp >> 1);
  c.tb = L_B1 + HF * 128u + h * 32u;
  c.vhst = (l & 16u) * 64u + (l & 15u) * 16u + (l & 32u) * 16u;
  bases0<HF, 0>(c, b);
    GAP();
    asm volatile("s_waitcnt vmcnt(0)\n\ts_barrier" : "+v"(epoch)::"memory");
    c.tag = rfl(epoch) + 1u;
    STAMP(31);
    store_flag<1>(c);
    if (c.w == 1) {
      const unsigned voff = WS_FLAG + 128u + ((c.r * 16u + (l & 15u)) << 10);
      for (unsigned spin = 0; spin < (1u << 16); ++spin) {
        unsigned pv;
        asm volatile("s_sleep %3\n\tbuffer_load_dword %0, %1, %2, 0 offen sc1\n\ts_waitcnt vmcnt(0)" : "=&v"(pv) : "v"(voff), "s"(c.sw), "i"(PSLEEP) : "memory");
        if (__builtin_amdgcn_ballot_w64(pv != c.tag) == 0) break;
      }
    }
    issue_w<HF, 0>(c, b); issue_w<HF, 1>(c, b); issue_w<HF, 2>(c, b); issue_w<HF, 3>(c, b); issue_w<HF, 4>(c, b);
  } else {
    unsigned b1a, b1b;
    {
      unsigned len;
      asm volatile("s_mov_b32 %0, mixer_code_end-mixer_code_begin-4" : "=s"(len));
      const unsigned off = (c.t * 64u + l) * 128u;
      asm volatile("s_mov_b32 m0, %0\n\ts_nop 0\n\tglobal_load_lds_dword %1, %2" ::"s"(L_JUNK), "v"(off < len ? off : len), "s"(c.pc));
    }
    asm volatile("buffer_load_dword %0, off, %1, %2 sc1" : "=v"(epoch) : "s"(c.sw), "s"(WS_EPOCH + c.r * 128u));
    asm volatile("buffer_load_ushort %0, %1, %2, %3 offen" : "=v"(b1a) : "v"((c.t * 64u + l) * 2u), "s"(sb1), "s"(c.freg2));
    asm volatile("buffer_load_ushort %0, %1, %2, %3 offen" : "=v"(b1b) : "v"((l & 31u) * 2u), "s"(sb1), "s"(c.f82));
    issue_w<HF, 0>(c, b); issue_w<HF, 1>(c, b); issue_w<HF, 2>(c, b); issue_w<HF, 3>(c, b); issue_w<HF, 4>(c, b);
    STAMP(1);
  b.vy = ((l >> 3) + 8u * c.w) * XROW + (((l & 7u) ^ ((g + 4u * c.w) & 7u)) << 4);
  b.lw = (8u * h + qq) * 128u + (((4u * HF + 2u * (g & 1u) + (pp & 1u)) ^ (((qq >> 1) & 1u) << 2)) << 4) + 8u * (pp >> 1);
  const unsigned xrow = 32u * c.t + (l & 31u);
  b.lx = xrow * 128u + ((h ^ ((xrow >> 1) & 7u)) << 4);
  b.l80 = (8u * h + qq) * 64u + 32u * (g & 1u) + 16u * (pp & 1u) + 8u * (pp >> 1);
  c.tb = L_B1 + HF * 128u + h * 32u;
  c.vhst = (l & 16u) * 64u + (l & 15u) * 16u + (l & 32u) * 16u;
  bases0<HF, 0>(c, b);
#pragma unroll
    for (int k = 0; k < 3; ++k) {
      const unsigned i = k < 2 ? 4u * k + c.t : 8u, nn = 64u * i + l, rho = nn / 18u, chn = nn - rho * 18u;
      const unsigned f = (rho & 3u) | ((rho & 4u) << 2) | ((rho & 24u) >> 1);
      const unsigned rel = i < 5u ? i : i - 5u;
      *LDSP(unsigned, L_PAT + i * 256u + l * 4u) = f * W2ROW + chn * 16u - (rel < 4u ? rel * 1024u : 0u);
    }
    asm volatile("s_barrier" ::: "memory");
    STAMP(31);
#pragma unroll
    for (int k = 0; k < 2; ++k) {
      const unsigned m = (k * 4u + c.t) * 64u + l, ln = ((c.r & 7u) * 4u + c.q) + 32u * (m < 324u ? m : 323u), rw = ln / 18u;
      if (k == 0 || c.t < 2)
        asm volatile("s_mov_b32 m0, %0\n\ts_nop 0\n\tbuffer_load_dword %1, %2, %3 offen lds" ::"s"(L_JUNK),
                     "v"(rw * W1ROW + (ln - rw * 18u) * 128u), "s"(c.s1), "s"(c.j * 2304u));
    }
    asm volatile("s_waitcnt vmcnt(%3)" : "+v"(b1a), "+v"(b1b), "+v"(epoch) : "i"(5 + W1PREF) : "memory");
    c.tag = rfl(epoch) + 1u;
    *LDSP(float, L_B1 + (c.t * 64u + l) * 4u) = __uint_as_float(b1a << 16) * KAP;
    *LDSP(float, L_B1 + 1024u + (l & 31u) * 4u) = __uint_as_float(b1b << 16) * KAP;
  }
  BARRIER_LGKM();
  STAMP(2);
  issue_y<0>(c, b); issue_y<1>(c, b);
  bias_quad<0>(c.acc[0], c.tb); bias_quad<1>(c.acc[0], c.tb); bias_quad<2>(c.acc[0], c.tb); bias_quad<3>(c.acc[0], c.tb);
  if constexpr (HF == 0) { bias_quad<0>(b.accB, c.tb + 1024u); bias_quad<1>(b.accB, c.tb + 1024u); bias_quad<2>(b.accB, c.tb + 1024u); bias_quad<3>(b.accB, c.tb + 1024u); }
  slice<HF, 0>(c, b);
  slice<HF, 1>(c, b); slice<HF, 2>(c, b); slice<HF, 3>(c, b); slice<HF, 4>(c, b);
  slice<HF, 5>(c, b); slice<HF, 6>(c, b); slice<HF, 7>(c, b); slice<HF, 8>(c, b);
  mmB<HF, 8, 2>(c, b); mmB<HF, 8, 3>(c, b);
  asm volatile("s_nop 15" : "+v"(b.accB));
  c.wb = opq(b.lw);
  if constexpr (HF == 0) {
    const unsigned ll = lane_id();
    f32x4 e0, e1;
#pragma unroll
    for (int i = 0; i < 4; ++i) { e0[i] = b.accB[8 + i]; e1[i] = b.accB[12 + i]; }
    *LDSP(f32x4, L_EX + c.t * 2048u + ll * 16u) = e0;
    *LDSP(f32x4, L_EX + c.t * 2048u + 1024u + ll * 16u) = e1;
#pragma unroll
    for (int i = 0; i < 8; ++i) c.a8[i] = b.accB[i];
  } else {
#pragma unroll
    for (int i = 0; i < 8; ++i) c.a8[i] = 0.f;
  }
  asm volatile("s_waitcnt vmcnt(%0) lgkmcnt(0)\n\ts_barrier" ::"i"(HF ? 4 : 5) : "memory");
}

__global__ __launch_bounds__(512, 2) void mixer_mlp_kernel(const void* __restrict__ x, const void* __restrict__ w1, const void* __restrict__ b1,
                                                           const void* __restrict__ w2, const void* __restrict__ b2, void* __restrict__ out,
                                                           void* __restrict__ ws) {
  Ctx c;
  asm volatile("s_getpc_b64 %0\nmixer_code_begin:" : "=s"(c.pc));
  const unsigned blk = blockIdx.x, xg = blk & 7u, idx = blk >> 3;
  c.r = 8u * (xg >> 2) + (idx >> 2); c.j = xg & 3u; c.q = idx & 3u;
  c.w = rfl(threadIdx.x >> 6); c.t = c.w & 3u; c.hf = c.w >> 2;
  STAMP(0);
  const srd_t sx = __builtin_amdgcn_make_buffer_rsrc((void*)x, 0, S_ * D_ * 2, 0x00020000);
  c.s1 = __builtin_amdgcn_make_buffer_rsrc((void*)w1, 0, D_ * F_ * 2, 0x00020000);
  c.s2 = __builtin_amdgcn_make_buffer_rsrc((void*)w2, 0, D_ * F_ * 2, 0x00020000);
  c.sw = __builtin_amdgcn_make_buffer_rsrc(ws, 0, WS_USED, 0x00020000);
  const srd_t sb1 = __builtin_amdgcn_make_buffer_rsrc((void*)b1, 0, F_ * 2, 0x00020000);
  c.freg2 = (1152u * c.j + fstart(c.q, 0)) * 2u;
  c.f82 = (1152u * c.j + fstart(c.q, 8)) * 2u;
  c.fown = WS_FLAG + (((c.r * 4u + c.j) * 4u + c.q) << 10);
  c.pollv = 0u;
  c.sw1 = (72u * c.t + 40u * c.hf) * W1ROW + c.freg2;
  c.ldst = c.t * 9216u + c.hf * 5120u;
  if (c.hf == 0) block0<0>(c, sx, sb1); else block0<1>(c, sx, sb1);
  {
    const unsigned hb = WS_H + ((c.r * 4u + c.j) * 144u + c.t) * 2048u;
#pragma unroll
    for (int d = 0; d < 4; ++d) c.hq[d] = hb + ((c.q + d) & 3u) * 73728u + c.hf * 8192u;
#pragma unroll
    for (int e = 0; e < 2; ++e) {
      c.h8[e] = hb + ((c.q + 2u * c.hf + e) & 3u) * 73728u + 65536u;
      c.sw2[e] = (1152u * c.j + fstart((c.q + 2u * e + (c.t & 1u)) & 3u, c.t >> 1)) * W2ROW + 288u * c.q;
    }
    c.sw28 = (1152u * c.j + fstart((c.q + c.t) & 3u, 8)) * W2ROW + 288u * c.q;
  }
  asm volatile("" : "+s"(c.sw1), "+s"(c.ldst), "+s"(c.hq[0]), "+s"(c.hq[1]), "+s"(c.hq[2]), "+s"(c.hq[3]), "+s"(c.h8[0]), "+s"(c.h8[1]),
               "+s"(c.sw2[0]), "+s"(c.sw2[1]), "+s"(c.sw28));
  STAMP(12);
  half_stage<0>(c); STAMP(13);
  half_stage<1>(c); STAMP(14);
  half_stage<2>(c); STAMP(15);
  half_stage<3>(c); STAMP(16);
  half_stage<4>(c); STAMP(17);
  half_stage<5>(c); STAMP(18);
  half_stage<6>(c); STAMP(19);
  half_stage<7>(c); STAMP(20);
  half_stage<8>(c); STAMP(21);
  half_stage<9>(c); STAMP(22);
  half_stage<10>(c); STAMP(23);
  half_stage<11>(c); STAMP(24);
  half_stage<12>(c); STAMP(25);
  half_stage<13>(c); STAMP(26);
  half_stage<14>(c); STAMP(27);

  const unsigned l = lane_id();
  {
    const unsigned wa = c.w * 9216u + l * 16u, ra = (c.w ^ 4u) * 9216u + l * 16u;
    if (c.hf) {
#pragma unroll
      for (int dt = 0; dt < 9; ++dt) *LDSP(f32x4, wa + dt * 1024u) = c.acc2[0][dt];
    } else {
#pragma unroll
      for (int dt = 0; dt < 9; ++dt) { *LDSP(f32x4, wa + dt * 1024u) = c.acc2[1][dt]; c.acc2[1][dt] = c.acc2[0][dt]; }
    }
    BARRIER_LGKM();
#pragma unroll
    for (int dt = 0; dt < 9; ++dt) c.acc2[1][dt] += ldsf4(ra + dt * 1024u);
  }
  const f32x4* R = c.acc2[1];
  if (c.t != c.j) {
    const unsigned so = WS_P + (((c.r * 4u + c.q) * 4u + c.t) * 4u + c.j) * 9216u + c.hf * 4608u;
#pragma unroll
    for (int m = 0; m < 4; ++m) {
      u32x4 v = {cvtpk(R[2 * m][0], R[2 * m][1]), cvtpk(R[2 * m][2], R[2 * m][3]), cvtpk(R[2 * m + 1][0], R[2 * m + 1][1]), cvtpk(R[2 * m + 1][2], R[2 * m + 1][3])};
#pragma unroll
      for (int i = 0; i < 4; ++i) v[i] = v[i] < 0xfffffffeu ? v[i] : 0xfffffffeu;
      asm volatile("buffer_store_dwordx4 %0, %1, %2, %3 offen sc1\n\ts_nop 1" ::"v"(v), "v"(c.vhld), "s"(c.sw), "s"(so + m * 1024u));
    }
    u32x2 v = {cvtpk(R[8][0], R[8][1]), cvtpk(R[8][2], R[8][3])};
#pragma unroll
    for (int i = 0; i < 2; ++i) v[i] = v[i] < 0xfffffffeu ? v[i] : 0xfffffffeu;
    asm volatile("buffer_store_dwordx2 %0, %1, %2, %3 offen sc1" ::"v"(v), "v"(l * 8u), "s"(c.sw), "s"(so + 4096u));
  } else {
#pragma unroll
    for (int dt = 0; dt < 9; ++dt) *LDSP(f32x4, 3u * SLOT + c.hf * 9216u + dt * 1024u + l * 16u) = R[dt];
  }
  const unsigned short* b2p = (const unsigned short*)b2 + 144u * c.q + 4u * (l >> 4);
  const u32x2 bA = *(const u32x2*)(b2p + 32u * c.t), bB = *(const u32x2*)(b2p + 32u * c.t + 16u), bC = *(const u32x2*)(b2p + 128u);
  asm volatile("s_waitcnt vmcnt(0) lgkmcnt(0)\n\ts_barrier" ::: "memory");
  STAMP(28);
  {
    const unsigned sp = WS_P + ((c.r * 4u + c.q) * 4u + c.j) * 4u * 9216u + c.hf * 4608u;
    const unsigned s0 = sp + ((c.j + 1u) & 3u) * 9216u, s1 = sp + ((c.j + 2u) & 3u) * 9216u, s2 = sp + ((c.j + 3u) & 3u) * 9216u;
    const unsigned va = c.vhld + c.t * 1024u, vb = 4096u + l * 8u;
    u32x4 pa[3]; u32x2 pb[3];
    f32x4 o0 = ldsf4(3u * SLOT + c.hf * 9216u + c.t * 2048u + l * 16u), o1 = ldsf4(3u * SLOT + c.hf * 9216u + c.t * 2048u + 1024u + l * 16u);
    f32x4 o2 = ldsf4(3u * SLOT + c.hf * 9216u + 8192u + l * 16u);
    for (unsigned spin = 0; spin < (1u << 16); ++spin) {
      asm volatile("buffer_load_dwordx4 %0, %6, %8, %9 offen sc1\n\tbuffer_load_dwordx4 %1, %6, %8, %10 offen sc1\n\t"
                   "buffer_load_dwordx4 %2, %6, %8, %11 offen sc1\n\t"
                   "s_cmp_lg_u32 %12, 0\n\ts_cbranch_scc1 .Lskip%=\n\t"
                   "buffer_load_dwordx2 %3, %7, %8, %9 offen sc1\n\tbuffer_load_dwordx2 %4, %7, %8, %10 offen sc1\n\t"
                   "buffer_load_dwordx2 %5, %7, %8, %11 offen sc1\n.Lskip%=:\n\ts_waitcnt vmcnt(0)"
                   : "=&v"(pa[0]), "=&v"(pa[1]), "=&v"(pa[2]), "=&v"(pb[0]), "=&v"(pb[1]), "=&v"(pb[2])
                   : "v"(va), "v"(vb), "s"(c.sw), "s"(s0), "s"(s1), "s"(s2), "s"(c.t) : "memory", "scc");
      unsigned mx = 0;
#pragma unroll
      for (int k = 0; k < 3; ++k) {
#pragma unroll
        for (int i = 0; i < 4; ++i) mx = mx > pa[k][i] ? mx : pa[k][i];
        if (c.t == 0) { mx = mx > pb[k][0] ? mx : pb[k][0]; mx = mx > pb[k][1] ? mx : pb[k][1]; }
      }
      if (!__any(mx == ~0u)) break;
    }
    STAMP(29);
#pragma unroll
    for (int k = 0; k < 3; ++k)
#pragma unroll
      for (int i = 0; i < 2; ++i) {
        o0[2 * i] += __uint_as_float(pa[k][i] << 16); o0[2 * i + 1] += __uint_as_float(pa[k][i] & 0xffff0000u);
        o1[2 * i] += __uint_as_float(pa[k][2 + i] << 16); o1[2 * i + 1] += __uint_as_float(pa[k][2 + i] & 0xffff0000u);
      }
    unsigned short* op = (unsigned short*)out + (size_t)(128u * c.r + 32u * c.j + 16u * c.hf + (l & 15u)) * D_ + 144u * c.q;
    const unsigned e0 = cvtpk(__builtin_fmaf(o0[0], RKAP, __uint_as_float(bA[0] << 16)), __builtin_fmaf(o0[1], RKAP, __uint_as_float(bA[0] & 0xffff0000u)));
    const unsigned e1 = cvtpk(__builtin_fmaf(o0[2], RKAP, __uint_as_float(bA[1] << 16)), __builtin_fmaf(o0[3], RKAP, __uint_as_float(bA[1] & 0xffff0000u)));
    const unsigned f0 = cvtpk(__builtin_fmaf(o1[0], RKAP, __uint_as_float(bB[0] << 16)), __builtin_fmaf(o1[1], RKAP, __uint_as_float(bB[0] & 0xffff0000u)));
    const unsigned f1 = cvtpk(__builtin_fmaf(o1[2], RKAP, __uint_as_float(bB[1] << 16)), __builtin_fmaf(o1[3], RKAP, __uint_as_float(bB[1] & 0xffff0000u)));
    auto r0 = __builtin_amdgcn_permlane16_swap(e0, f0, false, false);
    auto r1 = __builtin_amdgcn_permlane16_swap(e1, f1, false, false);
    *(u32x4*)(op + 32u * c.t + 16u * ((l >> 4) & 1u) + 8u * (l >> 5)) = (u32x4){r0[0], r1[0], r0[1], r1[1]};
    if (c.t == 0) {
#pragma unroll
      for (int k = 0; k < 3; ++k)
#pragma unroll
        for (int i = 0; i < 2; ++i) { o2[2 * i] += __uint_as_float(pb[k][i] << 16); o2[2 * i + 1] += __uint_as_float(pb[k][i] & 0xffff0000u); }
      *(u32x2*)(op + 128u + 4u * (l >> 4)) =
          (u32x2){cvtpk(__builtin_fmaf(o2[0], RKAP, __uint_as_float(bC[0] << 16)), __builtin_fmaf(o2[1], RKAP, __uint_as_float(bC[0] & 0xffff0000u))),
                  cvtpk(__builtin_fmaf(o2[2], RKAP, __uint_as_float(bC[1] << 16)), __builtin_fmaf(o2[3], RKAP, __uint_as_float(bC[1] & 0xffff0000u)))};
    }
    asm volatile("mixer_code_end:");
    if (c.w == 0 && c.j == 0 && c.q == 0 && l == 0)
      asm volatile("buffer_store_dword %0, off, %1, %2 sc1" ::"v"(c.tag), "s"(c.sw), "s"(WS_EPOCH + c.r * 128u));
  }
}

extern "C" void kernel_launch(void* const* d_in, const int* in_sizes, int n_in, void* d_out, int out_size, void* d_ws, size_t ws_size,
                              hipStream_t stream) {
  (void)in_sizes; (void)n_in; (void)out_size; (void)ws_size;
  (void)hipFuncSetAttribute((const void*)mixer_mlp_kernel, hipFuncAttributeMaxDynamicSharedMemorySize, LDS_BYTES);
  mixer_mlp_kernel<<<256, 512, LDS_BYTES, stream>>>(d_in[0], d_in[1], d_in[2], d_in[3], d_in[4], d_out, d_ws);
}
```

```cpp
#include <hip/hip_runtime.h>
#include <stdint.h>
#include <utility>

typedef __bf16 bf16x8 __attribute__((ext_vector_type(8)));
typedef __bf16 bf16x2 __attribute__((ext_vector_type(2)));
typedef short s16x4 __attribute__((ext_vector_type(4)));
typedef float f32x16 __attribute__((ext_vector_type(16)));
typedef float f32x4 __attribute__((ext_vector_type(4)));
typedef float f32x2 __attribute__((ext_vector_type(2)));
typedef unsigned u32x4 __attribute__((ext_vector_type(4)));
typedef unsigned u32x2 __attribute__((ext_vector_type(2)));
typedef __amdgpu_buffer_rsrc_t srd_t;
#define DI __device__ __forceinline__
#define LAS __attribute__((address_space(3)))
#define LDSP(T, a) ((LAS T*)(size_t)(a))

constexpr unsigned S_ = 2048, D_ = 576, F_ = 4608;
constexpr unsigned XROW = D_ * 2, W1ROW = F_ * 2, W2ROW = D_ * 2;
constexpr unsigned SLOT = 36864, RING = 4 * SLOT, SLICE = 28672;
constexpr unsigned L_B1 = RING, L_PAT = L_B1 + 1152, L_JUNK = L_PAT + 4608, L_PROG = L_JUNK + 256, LDS_BYTES = L_PROG + 64;
static_assert(LDS_BYTES <= 163840, "LDS");
constexpr unsigned WS_EPOCH = 0, WS_FLAG = 4096, WS_Y = WS_FLAG + 256 * 8 * 128, WS_H = WS_Y + S_ * D_ * 2, H_BYTES = S_ * F_ * 2;
constexpr unsigned WS_P = WS_H + H_BYTES, P_BYTES = 16 * 4 * 4 * 4 * 9216;
constexpr unsigned WS_USED = WS_P + P_BYTES;
constexpr float KAP = 0.4686686930532063f, GAM = 4.91222953926424f, RKAP = 2.1337034344781243f;
#define KSW 10
#define PSLEEP 1
#define TOUCH 1
#define PREFH 1
#define HK2 2
#define CPREF 1
#define W1PREF 3
#define PDELAY 10
#define TDELAY 8
#define PREF 1
#define STAMP(i) do {} while (0)

DI unsigned rfl(unsigned x) { return __builtin_amdgcn_readfirstlane(x); }
DI s16x4 trr(unsigned a) { return __builtin_amdgcn_ds_read_tr16_b64_v4i16(LDSP(s16x4, a)); }
DI bf16x8 cat(s16x4 lo, s16x4 hi) { return __builtin_bit_cast(bf16x8, __builtin_shufflevector(lo, hi, 0, 1, 2, 3, 4, 5, 6, 7)); }
DI f32x4 ldsf4(unsigned a) { return *LDSP(f32x4, a); }
DI unsigned cvtpk(float lo, float hi) { f32x2 v = {lo, hi}; return __builtin_bit_cast(unsigned, __builtin_convertvector(v, bf16x2)); }
template <int CTRL> DI float dpp_add(float v) {
  return v + __builtin_bit_cast(float, __builtin_amdgcn_update_dpp(0, __builtin_bit_cast(int, v), CTRL, 0xf, 0xf, false));
}
DI void mfma16(f32x4& acc, bf16x8 a, bf16x8 b) { asm volatile("v_mfma_f32_16x16x32_bf16 %0, %1, %2, %0" : "+v"(acc) : "v"(a), "v"(b)); }
DI void mfma16z(f32x4& acc, bf16x8 a, bf16x8 b) { asm volatile("v_mfma_f32_16x16x32_bf16 %0, %1, %2, 0" : "=&v"(acc) : "v"(a), "v"(b)); }
#define BARRIER_LGKM() asm volatile("s_waitcnt lgkmcnt(0)\n\ts_barrier" ::: "memory")
#define GAP() __builtin_amdgcn_sched_barrier(0)
#define PIN(x) asm volatile("" : "+v"(x))

struct Ctx {
  srd_t s1, s2, sw;
  unsigned long long pc;
  unsigned w, t, hf, r, j, q;
  unsigned freg2, f82;
  unsigned tag, sdma, sw1, ldst, hq[4], h8[2], sw2[2], sw28, fown;
  u32x4 vpat;
  unsigned vpat4;
  unsigned wb, wb2;
  unsigned vhld[2], tb, pref, vtch, xw, xr, pg, sp;
  u32x2 b2v[3];
  f32x4 xg[2];
  srd_t sb2;
  float gz[2], gm, gz8, gm8;
  unsigned own[4][8], hv8[8];
  bf16x8 X[36];
  bf16x8 F[4];
  f32x4 acc[2][4];
  f32x4 acc8[2];
  f32x4 acc2[2][9];
  u32x4 Hb[8][2];
  unsigned pollv;
};
DI unsigned lane_id() {
  unsigned l;
  asm volatile("v_mbcnt_lo_u32_b32 %0, -1, 0\n\tv_mbcnt_hi_u32_b32 %0, -1, %0" : "=v"(l));
  return l;
}
DI unsigned opq(unsigned v) { PIN(v); return v; }

constexpr int pair_of(int h) { return h < 8 ? 0 : (h - 7) / 2; }
constexpr int ph_of(int h) { return h < 8 ? h - 6 : (h - 7) % 2; }
constexpr bool own_unit(int n) { return n == 0 || n == 6 || n == 10 || n == 14; }
constexpr int hload_at(int h, int k) {
  if (h == 8 || h == 10) return k == 0 ? 4 * (h - 8) + 14 : k == 2 ? 4 * (h - 8) + 15 : -1;
  if (h == 11 && (k == 1 || k == 3)) return 30 + (k == 3);
  if (h == 12) return k == -2 ? 32 : k == 0 ? 33 : k == 2 ? 34 : k == 4 ? 35 : -1;
  const int slot = k == 0 ? 0 : k == 2 ? 1 : k == 10 ? 2 : k == 12 ? 3 : k == 1 ? 4 : k == 3 ? 5 : -1;
  if (slot < 0) return -1;
  if (h == 5) return slot < 4 ? 2 + slot : -1;
  if (h == 6) return k < 4 ? 6 + k : 8 + slot;
  if (h < 8 || h > 11 || slot > 3) return -1;
  return 12 + 4 * (h - 8) + slot;
}
constexpr bool b2_at(int h, int k) { return h == 12 && k == 6; }
constexpr int dma_at(int h, int k) {
  if (h == 0) return (k < 16 && (k & 1) == 0) ? (k < 8 ? 2 * 8 + (k + 2) / 2 : 3 * 8 + (k - 8) / 2) : (k == 15 ? 3 * 8 + 4 : -1);
  if (h + 3 > 14) return -1;
  const int i = h < 4 ? (k == -2 ? 0 : k == 2 ? 1 : k == 6 ? 2 : k == 10 ? 3 : k == 13 ? 4 : -1)
                      : (k == -2 ? 0 : k == 4 ? 1 : k == 6 ? 2 : k == 8 ? 3 : k == 14 ? 4 : -1);
  return i < 0 ? -1 : (h + 3) * 8 + i;
}
constexpr int poll_k(int h) { return h == 4 ? 5 : h == 10 ? 5 : (h == 7 || h == 9 || h == 11) ? -1 : -9; }
constexpr int poll_ev(int h) { return h == 4 ? 2 : h == 7 ? 3 : h == 10 ? 5 : 4; }
constexpr bool poll_at(int h, int k) { return k == poll_k(h); }
constexpr int flag_at(int h) { return (h == 3 || h == 5 || h == 7 || h == 9) ? (h + 1) / 2 : 0; }
constexpr bool hst_at(int h, int k) { return (h < 8 && (h & 1) && (k == 0 || k == 15)) || (h == 1 && k == 12); }
constexpr bool pref_at(int h, int k) { return PREF && h < 6 && k == 15; }
constexpr bool touch_at(int h, int k) { return TOUCH && (h == 2 || h == 4 || h == 6 || h == 8) && k == 13; }
constexpr bool mark_at(int h, int k) { return (h == 2 && (k == 4 || k == 12)) || (h == 4 && k == 2); }
constexpr int nops(int h, int k, int n) {
  return (b2_at(h, k) ? 3 : 0) + (hload_at(h, k) >= 0 ? 1 : 0) + (hst_at(h, k) ? 1 : 0) + (mark_at(h, k) ? 1 : 0) +
         (dma_at(h, k) >= 0 && (dma_at(h, k) & 7) < n ? 1 : 0);
}
constexpr int between(int h0, int k0, int h1, int k1, int n) {
  int s = 0;
  for (int h = h0; h <= h1; ++h)
    for (int k = -2; k < 16; ++k)
      if ((h > h0 || k > k0) && (h < h1 || k < k1)) s += nops(h, k, n);
  for (int h = h0; h <= h1; ++h)
    if (pref_at(h, 15) && (h < h1 || k1 == 16)) s += 1;
  return s;
}
constexpr int cmin(int a, int b) { return a < b ? a : b; }

DI unsigned fstart(unsigned qq, unsigned ch) { return 32u * (9u * qq + ch + (qq & 1u) * (1u - 9u * (ch >> 3))); }

DI void dma16(unsigned m0v, unsigned voff, srd_t srd, unsigned soff) {
  asm volatile("s_mov_b32 m0, %0\n\ts_nop 0\n\tbuffer_load_dwordx4 %1, %2, %3 offen lds" ::"s"(m0v), "v"(voff), "s"(srd), "s"(soff));
}
template <int HS, int I> DI void issue_piece(Ctx& c) {
  const srd_t sd = HS < 6 ? c.s1 : c.s2;
  if constexpr (I == 0) {
    if constexpr (HS < 6) c.sdma = c.sw1 + ((HS & 1) * 288u * W1ROW + ((HS >> 1) + 1) * 128u);
    else c.sdma = HS == 8 ? c.sw28 : c.sw2[ph_of(HS)] + pair_of(HS) * 64u * W2ROW;
    dma16((HS & 3) * SLOT + c.ldst, c.vpat[0], sd, c.sdma);
  } else if constexpr (I < 4) {
    asm volatile("buffer_load_dwordx4 %0, %1, %2 offen offset:%3 lds" ::"v"(c.vpat[I]), "s"(sd), "s"(c.sdma), "i"(1024 * I));
  } else {
    asm volatile("s_cmp_lg_u32 %4, 0\n\ts_cbranch_scc1 .Lskip%=\n\ts_mov_b32 m0, %0\n\ts_nop 0\n\tbuffer_load_dwordx4 %1, %2, %3 offen lds\n.Lskip%=:"
                 ::"s"((HS & 3) * SLOT + 4096u + c.ldst), "v"(c.vpat4), "s"(sd), "s"(c.sdma), "s"(c.hf) : "scc");
  }
}
template <int HS> DI void issue_slot(Ctx& c) {
  issue_piece<HS, 0>(c); issue_piece<HS, 1>(c); issue_piece<HS, 2>(c); issue_piece<HS, 3>(c); issue_piece<HS, 4>(c);
}
template <int N, int U> DI void issue_hload(Ctx& c) {
  constexpr int h = 6 + N / 2;
  const unsigned so = h == 8 ? c.h8[N & 1] : c.hq[2 * ph_of(h) + (N & 1)] + pair_of(h) * 16384u;
  asm volatile("buffer_load_dwordx4 %0, %1, %2, %3 offen sc1" : "=v"(c.Hb[N & 7][U]) : "v"(c.vhld[U]), "s"(c.sw), "s"(so));
}
DI unsigned poll_off(const Ctx& c, bool dest) {
  const unsigned l = lane_id(), dp = (l & 3u) ? (l & 3u) : 1u;
  return WS_FLAG + ((dest ? (c.r * 4u + ((c.j + dp) & 3u)) * 4u + c.q : (c.r * 4u + c.j) * 4u + ((c.q + dp) & 3u)) << 10);
}
template <int EV> DI void store_flag(Ctx& c) {
  asm volatile("s_cmp_lg_u32 %3, 0\n\ts_cbranch_scc1 .Lskip%=\n\ts_mov_b64 exec, 1\n\t"
               "buffer_store_dword %0, off, %1, %2 offset:%4 sc1\n\ts_mov_b64 exec, -1\n.Lskip%=:"
               ::"v"(c.tag), "s"(c.sw), "s"(c.fown), "s"(c.w), "i"(EV * 128) : "scc");
}
template <int EV> DI void issue_poll(Ctx& c, unsigned& pv, unsigned voff) {
  asm volatile("s_cmp_lg_u32 %3, 7\n\ts_cbranch_scc1 .Lskip%=\n\tbuffer_load_dword %0, %1, %2, 0 offen offset:%4 sc1\n.Lskip%=:"
               : "+v"(pv) : "v"(voff), "s"(c.sw), "s"(c.w), "i"(EV * 128) : "scc");
}
template <int EV, int N> DI void check_poll(Ctx& c, unsigned& pv, bool dest) {
  if (c.w == 7) {
    asm volatile("s_waitcnt vmcnt(%1)" : "+v"(pv) : "i"(N) : "memory");
    if (__builtin_expect(__builtin_amdgcn_ballot_w64(pv != c.tag) != 0, 0)) {
      const unsigned voff = poll_off(c, dest);
      for (unsigned spin = 0; spin < (1u << 16); ++spin) {
        unsigned v;
        asm volatile("s_sleep 1\n\tbuffer_load_dword %0, %1, %2, 0 offen offset:%3 sc1\n\ts_waitcnt vmcnt(0)" : "=&v"(v) : "v"(voff), "s"(c.sw), "i"(EV * 128) : "memory");
        if (__builtin_amdgcn_ballot_w64(v != c.tag) == 0) break;
      }
    }
  }
}
template <int P> DI void store_h(Ctx& c, const unsigned (&hv)[8], unsigned vo, unsigned so) {
  const u32x4 v = {hv[4 * P], hv[4 * P + 1], hv[4 * P + 2], hv[4 * P + 3]};
  asm volatile("buffer_store_dwordx4 %0, %1, %2, %3 offen sc1\n\ts_nop 1" ::"v"(v), "v"(vo), "s"(c.sw), "s"(so));
}
template <int N0, int N1> DI void wait_vm(const Ctx&) {
  static_assert(N1 <= N0 && N1 >= 0 && N0 < 64, "vmcnt");
  asm volatile("s_waitcnt vmcnt(%0)" ::"i"(N1) : "memory");
}

template <int H> DI void set_bases(Ctx& c) {
  constexpr unsigned d = (H & 3) ? SLOT : 0u - 3u * SLOT;
  if constexpr (H == 6) {
    const unsigned l = lane_id();
    c.wb = opq(2u * SLOT + (l >> 2) * 288u + 8u * (l & 3u) + c.hf * 18432u);
  } else if constexpr (H > 0 && H < 14) c.wb = opq(c.wb + d);
  if constexpr (H < 6) c.wb2 = opq(c.wb ^ 32u);
}
template <int H, int S> DI void rd(Ctx& c) {
  constexpr int tau = 18 * (H - 13) + S;
  constexpr unsigned o = H < 6 ? 4096u * (S >> 1) : H < 13 ? 9216u * (S / 9) + 32u * (S % 9) : ((tau >> 1) & 1) * SLOT + (tau & 1) * 9216u + 32u * (tau >> 2);
  constexpr unsigned e = H < 6 ? 512u : 4608u;
  const unsigned b = (H < 6 && (S & 1)) ? c.wb2 : c.wb;
  c.F[(H * 18 + S) & 3] = cat(trr(b + o), trr(b + o + e));
}
template <int H, int S> DI void mm(Ctx& c) {
  const bf16x8 a = c.F[(H * 18 + S) & 3];
  if constexpr (H < 6) {
    constexpr int B = ((H >> 1) + 1) & 1;
    constexpr int P = 9 * (H & 1) + (S >> 1);
    mfma16(c.acc[B][S & 1], a, c.X[2 * P]);
    mfma16(c.acc[B][2 + (S & 1)], a, c.X[2 * P + 1]);
  } else {
    constexpr int tau = 18 * (H - 13) + S;
    constexpr int N = H < 13 ? 2 * (H - 6) + S / 9 : 14 + (tau & 3), dt = H < 13 ? S % 9 : tau >> 2;
    u32x4 b0 = c.Hb[N & 7][0], b1 = c.Hb[N & 7][1];
    if constexpr (own_unit(N)) {
      const unsigned(&o)[8] = c.own[pair_of(6 + N / 2)];
      b0 = (u32x4){o[0], o[1], o[2], o[3]}; b1 = (u32x4){o[4], o[5], o[6], o[7]};
    }
    if constexpr (N == 0) {
      mfma16z(c.acc2[0][dt], a, __builtin_bit_cast(bf16x8, b0));
      mfma16z(c.acc2[1][dt], a, __builtin_bit_cast(bf16x8, b1));
    } else {
      mfma16(c.acc2[0][dt], a, __builtin_bit_cast(bf16x8, b0));
      mfma16(c.acc2[1][dt], a, __builtin_bit_cast(bf16x8, b1));
    }
  }
}
template <int Q> DI void bias_quad(f32x4 (&acc)[4], unsigned a) { acc[Q] = *LDSP(volatile f32x4, a + (Q & 1) * 64u); }
template <int E, int ST> DI void gelu_stage(float s, float& z, float& m0, unsigned (&hv)[8]) {
  if constexpr (ST == 0) { z = s * __builtin_fmaf(s, s, GAM); PIN(z); }
  if constexpr (ST == 1) { z = __builtin_amdgcn_exp2f(-z); PIN(z); }
  if constexpr (ST == 2) { z = __builtin_amdgcn_rcpf(1.0f + z); PIN(z); }
  if constexpr (ST == 3) {
    if constexpr ((E & 1) == 0) { m0 = s * z; PIN(m0); }
    else { float m = s * z, mm0 = m0; PIN(m); PIN(mm0); hv[E >> 1] = cvtpk(mm0, m); PIN(hv[E >> 1]); }
  }
}
constexpr int gstart(int e) { return 1 + (e * 29) / 15; }
constexpr int gelem(int g, int st) {
  for (int e = 0; e < 16; ++e) if (gstart(e) + st == g) return e;
  return -1;
}
template <int B, int G, int ST> DI void gelu_part(Ctx& c) {
  constexpr int e = gelem(G, ST);
  if constexpr (e >= 0) gelu_stage<e, ST>(c.acc[B & 1][e >> 2][e & 3], c.gz[e & 1], c.gm, c.own[B]);
}
template <int B, int G> DI void gelu_gap(Ctx& c) {
  gelu_part<B, G, 3>(c); gelu_part<B, G, 2>(c); gelu_part<B, G, 1>(c); gelu_part<B, G, 0>(c);
  if constexpr (B < 2) {
    if constexpr (G == 27) { bias_quad<0>(c.acc[B & 1], c.tb + (B + 2) * 256u); bias_quad<1>(c.acc[B & 1], c.tb + (B + 2) * 256u); }
    if constexpr (G == 31) bias_quad<2>(c.acc[B & 1], c.tb + (B + 2) * 256u);
    if constexpr (G == 33) bias_quad<3>(c.acc[B & 1], c.tb + (B + 2) * 256u);
  }
}

template <int H0, int K0, int H1, int K1> DI void wait_after(const Ctx& c) {
  wait_vm<between(H0, K0, H1, K1, 5), between(H0, K0, H1, K1, 4)>(c);
}
DI unsigned nosent(unsigned v) { return v < 0xfffffffeu ? v : 0xfffffffeu; }
template <int M, int ST> DI void comb(Ctx& c) {
  constexpr int NT = M < 4 ? 2 : 1;
  if constexpr (ST == 0) {
#pragma unroll
    for (int i = 0; i < NT; ++i) *LDSP(f32x4, c.xw + (2 * M + i) * 1024u) = c.acc2[1][2 * M + i];
    asm volatile("s_mov_b64 exec, 1\n\tds_write_b32 %0, %1\n\ts_mov_b64 exec, -1" ::"v"(L_PROG + c.w * 4u), "v"(M + 1u) : "memory");
  }
  if constexpr (ST == 1) c.pg = *LDSP(volatile unsigned, L_PROG + (c.w ^ 4u) * 4u);
  if constexpr (ST == 2) {
    if (__builtin_expect(rfl(c.pg) <= (unsigned)M, 0)) {
      for (unsigned spin = 0; spin < (1u << 20); ++spin)
        if (rfl(*LDSP(volatile unsigned, L_PROG + (c.w ^ 4u) * 4u)) > (unsigned)M) break;
    }
    asm volatile("" ::: "memory");
#pragma unroll
    for (int i = 0; i < NT; ++i) c.xg[i] = ldsf4(c.xr + (2 * M + i) * 1024u);
  }
  if constexpr (ST == 3) {
    f32x4 r[2];
#pragma unroll
    for (int i = 0; i < NT; ++i)
#pragma unroll
      for (int k = 0; k < 4; ++k) { r[i][k] = c.acc2[0][2 * M + i][k] + c.xg[i][k]; PIN(r[i][k]); }
    if (c.t != c.j) {
      if constexpr (NT == 2) {
        const u32x4 v = {nosent(cvtpk(r[0][0], r[0][1])), nosent(cvtpk(r[0][2], r[0][3])), nosent(cvtpk(r[1][0], r[1][1])), nosent(cvtpk(r[1][2], r[1][3]))};
        asm volatile("buffer_store_dwordx4 %0, %1, %2, %3 offen offset:%4 sc1\n\ts_nop 1" ::"v"(v), "v"(c.vhld[0] & 1023u), "s"(c.sw), "s"(c.sp), "i"(M * 1024));
      } else {
        const u32x2 v = {nosent(cvtpk(r[0][0], r[0][1])), nosent(cvtpk(r[0][2], r[0][3]))};
        asm volatile("buffer_store_dwordx2 %0, %1, %2, %3 offen sc1" ::"v"(v), "v"((c.vhld[0] & 1023u) >> 1), "s"(c.sw), "s"(c.sp + 4096u));
      }
    } else {
#pragma unroll
      for (int i = 0; i < NT; ++i) *LDSP(f32x4, c.xr + (2 * M + i) * 1024u) = r[i];
    }
  }
}
template <int H, int K> DI void side(Ctx& c) {
  if constexpr (poll_at(H, K)) {
    if constexpr (H == 11) issue_poll<4>(c, c.pollv, poll_off(c, true));
    else issue_poll<poll_ev(H)>(c, c.pollv, c.pref);
  }
  if constexpr (K == -1 && flag_at(H)) store_flag<flag_at(H)>(c);
  if constexpr (hload_at(H, K) >= 0) issue_hload<(hload_at(H, K) >> 1), (hload_at(H, K) & 1)>(c);
  if constexpr (mark_at(H, K)) {
    constexpr unsigned k = H == 4 ? 2u : K == 4 ? 0u : 1u;
    const u32x4 ones = {~0u, ~0u, ~0u, ~0u};
    const unsigned so = WS_P + (((c.r * 4u + c.q) * 4u + c.j) * 4u + ((c.j + 1u + k) & 3u)) * 9216u, vo = lane_id() * 16u;
    asm volatile("buffer_store_dwordx4 %0, %1, %2, %3 offen sc1\n\ts_cmp_lg_u32 %4, %5\n\ts_cbranch_scc1 .Lskip%=\n\t"
                 "buffer_store_dwordx4 %0, %1, %2, %6 offen sc1\n.Lskip%=:\n\ts_nop 1"
                 ::"v"(ones), "v"(vo), "s"(c.sw), "s"(so + c.w * 1024u), "s"(c.w), "i"(k), "s"(so + 8192u) : "scc");
  }
  if constexpr (dma_at(H, K) >= 0) issue_piece<(dma_at(H, K) >> 3), (dma_at(H, K) & 7)>(c);
  if constexpr ((H == 2 && K == 14) || ((H == 4 || H == 5) && K == 15)) {
    const unsigned a = L_PAT + (H == 4 ? 2304u : 0u) + lane_id() * 4u;
#pragma unroll
    for (int i = 0; i < 4; ++i) c.vpat[i] = *LDSP(unsigned, a + (c.hf * 5u + i) * 256u);
    c.vpat4 = *LDSP(unsigned, a + 1024u);
  }
  if constexpr (b2_at(H, K)) {
    const unsigned l = lane_id();
#pragma unroll
    for (int i = 0; i < 3; ++i)
      asm volatile("buffer_load_dwordx2 %0, %1, %2, %3 offen" : "=v"(c.b2v[i]) : "v"((l >> 4) * 8u), "s"(c.sb2), "s"(288u * c.q + (i < 2 ? 64u * c.t + 32u * i : 256u)));
    c.xw = c.hf * 3u * SLOT + c.t * 9216u + l * 16u;
    c.xr = (c.hf ^ 1u) * 3u * SLOT + c.t * 9216u + l * 16u;
    c.sp = WS_P + (((c.r * 4u + c.q) * 4u + c.t) * 4u + c.j) * 9216u + c.hf * 4608u;
  }
  if constexpr (H >= 13) {
    constexpr int tau = 18 * (H - 13) + K;
    if constexpr (tau >= 9 && (tau - 9) % 8 == 0) comb<(tau - 9) / 8, 0>(c);
    if constexpr (tau >= 12 && (tau - 12) % 8 == 0) comb<(tau - 12) / 8, 1>(c);
    if constexpr (tau >= 14 && (tau - 14) % 8 == 0) comb<(tau - 14) / 8, 2>(c);
    if constexpr (tau >= 16 && (tau - 16) % 8 == 0) comb<(tau - 16) / 8, 3>(c);
  }
  if constexpr (H < 8) gelu_gap<(H >> 1), K + 18 * (H & 1)>(c);
  if constexpr (H < 2) {
    constexpr int G = K + 18 * H - 1;
    if constexpr (G >= 0 && G < 28) gelu_stage<G / 4, (G & 3)>(c.acc8[G / 16][(G / 4) & 3], c.gz8, c.gm8, c.hv8);
    if constexpr (G == 28) { gelu_stage<7, 0>(c.acc8[1][3], c.gz8, c.gm8, c.hv8); gelu_stage<7, 1>(c.acc8[1][3], c.gz8, c.gm8, c.hv8); }
    if constexpr (G == 29) { gelu_stage<7, 2>(c.acc8[1][3], c.gz8, c.gm8, c.hv8); gelu_stage<7, 3>(c.acc8[1][3], c.gz8, c.gm8, c.hv8); }
  }
  if constexpr (hst_at(H, K)) {
    if constexpr (K == 12) {
      const unsigned l = lane_id();
#pragma unroll
      for (int i = 0; i < 2; ++i) {
        auto rr = __builtin_amdgcn_permlane16_swap(c.hv8[i], c.hv8[2 + i], false, false);
        c.hv8[i] = rr[0]; c.hv8[2 + i] = rr[1];
      }
      store_h<0>(c, c.hv8, (((l >> 4) & 1u) ^ c.hf) * 1024u + ((l & 15u) + 16u * (l >> 5)) * 16u, c.hq[0] + 65536u - c.hf * 7680u);
    } else store_h<(K == 0 ? 0 : 1)>(c, c.own[H >> 1], c.vhld[K == 0 ? 0 : 1], c.hq[0] + (H >> 1) * 16384u);
  }
  if constexpr (pref_at(H, K))
    asm volatile("s_mov_b32 m0, %0\n\ts_nop 0\n\tbuffer_load_dword %1, %2, %3 offen lds" ::"s"(L_JUNK), "v"(c.vtch), "s"(c.s2), "s"((c.j * 1152u + H * 48u) * W2ROW));
}
constexpr int hpos(int n) {
  for (int h = 4; h < 14; ++h) for (int k = -2; k < 16; ++k) if (hload_at(h, k) == 2 * n + 1) return h * 32 + k + 2;
  return 0;
}
constexpr int cmax(int a, int b) { return a > b ? a : b; }
template <int H> DI void end_hs(Ctx& c) {
  if constexpr (H < 13) {
    constexpr int p0 = H == 0 ? -1 : H == 1 ? 8 : H == 12 ? 12 * 32 + 8 : (H - 2) * 32 + 17;
    constexpr int p1 = (H >= 5 && !own_unit(2 * (H - 5))) ? hpos(2 * (H - 5)) : 0, p2 = 0, p3 = flag_at(H + 1) ? (H - 1) * 32 + 17 : 0;
    constexpr int p = H == 0 ? -1 : cmax(cmax(p0, p1), cmax(p2, p3));
    if constexpr (H == 0) wait_vm<between(0, -3, 0, 16, 4) + ((W1PREF & 8) ? 1 : 0), between(0, -3, 0, 16, 4) + ((W1PREF & 8) ? 1 : 0)>(c);
    else wait_after<p / 32, p % 32 - 2, H, 16>(c);
    if constexpr (poll_k(H) > -9) check_poll<poll_ev(H), between(H, poll_k(H), H, 16, 4)>(c, c.pollv, H == 11);
    BARRIER_LGKM();
  }
  if constexpr (H == 12) {
#pragma unroll
    for (int n = 15; n < 18; ++n) { PIN(c.Hb[n & 7][0]); PIN(c.Hb[n & 7][1]); }
    PIN(c.b2v[0]); PIN(c.b2v[1]); PIN(c.b2v[2]);
  }
  if constexpr (H >= 5 && H < 12 && !own_unit(2 * (H - 5))) { PIN(c.Hb[(2 * (H - 5)) & 7][0]); PIN(c.Hb[(2 * (H - 5)) & 7][1]); }
  GAP();
}
template <int H> DI void mid_wait(Ctx& c) {
  if constexpr (H >= 6 && H < 13) {
    constexpr int p = hpos(2 * (H - 6) + 1);
    wait_after<p / 32, p % 32 - 2, H, 9>(c);
    PIN(c.Hb[(2 * (H - 6) + 1) & 7][0]); PIN(c.Hb[(2 * (H - 6) + 1) & 7][1]);
  }
}
#define GST(H, i) do {} while (0)
template <int H, int K> DI void step(Ctx& c) {
  GST(H, K + 2);
  if constexpr (K == 9) mid_wait<H>(c);
  if constexpr (K == KSW) asm volatile("s_setprio 0");
  mm<H, K>(c);
  if constexpr (K + 4 < 18) rd<H, K + 4>(c);
  side<H, K>(c);
  GAP();
}
template <int H, int... Ks> DI void steps(Ctx& c, std::integer_sequence<int, Ks...>) { (step<H, Ks>(c), ...); }
DI void prio_young(const Ctx& c) {
  asm volatile("s_cmp_eq_u32 %0, 0\n\ts_cbranch_scc1 .Lskip%=\n\ts_setprio 1\n.Lskip%=:" ::"s"(c.hf) : "scc");
}
template <int H> DI void half_stage(Ctx& c) {
  prio_young(c);
  set_bases<H>(c);
  rd<H, 0>(c); rd<H, 1>(c);
  GAP();
  GST(H, 0);
  if constexpr (H > 0) mm<H - 1, 16>(c); else { mfma16(c.acc[0][0], c.F[2], c.X[34]); mfma16(c.acc[0][2], c.F[2], c.X[35]); }
  rd<H, 2>(c);
  side<H, -2>(c);
  GAP();
  GST(H, 1);
  if constexpr (H > 0) mm<H - 1, 17>(c); else { mfma16(c.acc[0][1], c.F[3], c.X[34]); mfma16(c.acc[0][3], c.F[3], c.X[35]); }
  rd<H, 3>(c);
  side<H, -1>(c);
  GAP();
  steps<H>(c, std::make_integer_sequence<int, 16>{});
  if constexpr (H == 14) { mm<14, 16>(c); mm<14, 17>(c); GAP(); asm volatile("s_nop 15" ::: "memory"); GAP(); }
  GST(H, 18);
  end_hs<H>(c);
  GST(H, 20);
}

struct B0 {
  unsigned vy, vw1, vc8, xa[4], l8, l80, lx, lw;
  bf16x8 G[2];
};
template <int S> DI void issue_y(Ctx& c, B0& b) {
  const unsigned dst = (S % 5) * SLICE + c.w * 1024u, so = WS_Y + c.r * 128u * XROW + S * 128u;
  asm volatile("s_mov_b32 m0, %0\n\ts_nop 0\n\tbuffer_load_dwordx4 %1, %2, %3 offen sc1 lds" ::"s"(dst), "v"(b.vy), "s"(c.sw), "s"(so));
  asm volatile("s_mov_b32 m0, %0\n\ts_nop 0\n\tbuffer_load_dwordx4 %1, %2, %3 offen sc1 lds" ::"s"(dst + 8192u), "v"(b.vy), "s"(c.sw), "s"(so + 64u * XROW));
}
template <int S> DI void issue_w(Ctx& c, B0& b) {
  const unsigned dst = (S % 5) * SLICE, kk = S * 64u;
  dma16(dst + c.w * 1024u + 16384u, b.vw1, c.s1, (kk + 8u * c.w) * W1ROW + c.freg2);
  asm volatile("s_cmp_lg_u32 %4, 0\n\ts_cbranch_scc1 .Lskip%=\n\ts_mov_b32 m0, %0\n\ts_nop 0\n\tbuffer_load_dwordx4 %1, %2, %3 offen lds\n.Lskip%=:"
               ::"s"(dst + c.t * 1024u + 24576u), "v"(b.vc8), "s"(c.s1), "s"((kk + 16u * c.t) * W1ROW + c.f82), "s"(c.hf) : "scc");
}
template <int S, int P> DI void rdx(Ctx& c, B0& b) {
  c.X[4 * S + 2 * P] = *LDSP(bf16x8, b.xa[2 * P]);
  c.X[4 * S + 2 * P + 1] = *LDSP(bf16x8, b.xa[2 * P + 1]);
}
template <int K> DI void rdf(Ctx& c) {
  const unsigned a = ((K & 1) ? c.wb2 : c.wb) + 4096u * (K >> 1);
  c.F[K] = cat(trr(a), trr(a + 512u));
}
template <int P> DI void rdg(B0& b) { b.G[P] = cat(trr(b.l8 + 2048u * P), trr(b.l8 + 2048u * P + 256u)); }
template <int S, int K> DI void mmA(Ctx& c) {
  mfma16(c.acc[0][K & 1], c.F[K], c.X[4 * S + (K & 2)]);
  mfma16(c.acc[0][2 + (K & 1)], c.F[K], c.X[4 * S + (K & 2) + 1]);
}
template <int S, int P> DI void mmB(Ctx& c, B0& b) {
  mfma16(c.acc8[0], b.G[P], c.X[4 * S + 2 * P]);
  mfma16(c.acc8[1], b.G[P], c.X[4 * S + 2 * P + 1]);
}
template <int S> DI void bases0(Ctx& c, B0& b) {
  constexpr unsigned base = (S % 5) * SLICE;
#pragma unroll
  for (int k = 0; k < 4; ++k) b.xa[k] = opq(base + (b.lx ^ (unsigned)((k >> 1) * 64 + (k & 1) * 2048)));
  c.wb = opq(base + 16384u + b.lw);
  c.wb2 = opq(c.wb ^ 32u);
  b.l8 = opq(base + 24576u + b.l80);
}
template <int S> DI void slice(Ctx& c, B0& b) {
  constexpr int n0 = 3, n = 4;
  constexpr int N = S == 0 ? 2 : S == 1 ? 4 : S == 2 ? 4 + n0 : S == 3 ? 2 + 2 * n0 : S <= 5 ? 3 * n0 : S == 6 ? 2 * n0 : S == 7 ? n0 : n;
  asm volatile("s_waitcnt vmcnt(%0) lgkmcnt(0)\n\ts_barrier" ::"i"(N) : "memory");
  STAMP(3 + S);
  GAP();
  if constexpr (S > 0) mmA<S - 1, 2>(c);
  rdx<S, 0>(c, b); rdf<0>(c);
  GAP();
  if constexpr (S > 0) mmA<S - 1, 3>(c);
  rdf<1>(c); rdg<0>(b);
  GAP();
  if constexpr (S > 0) mmB<S - 1, 1>(c, b);
  rdx<S, 1>(c, b); rdf<2>(c);
  if constexpr (S <= 1) issue_y<2 * S + 2>(c, b);
  if constexpr (S >= 2 && S <= 4) issue_y<S + 4>(c, b);
  if constexpr (S == 7) issue_slot<0>(c);
  if constexpr (S == 8) issue_slot<1>(c);
  GAP();
  mmA<S, 0>(c);
  rdf<3>(c); rdg<1>(b);
  GAP();
  mmA<S, 1>(c);
  if constexpr (S <= 1) issue_y<2 * S + 3>(c, b);
  if constexpr (S >= 1 && S <= 4) issue_w<S + 4>(c, b);
  if constexpr (S == 5) {
    const unsigned m = c.w * 7u + (lane_id() & 7u), ln = ((c.r & 7u) * 4u + c.q) * 54u + (m < 54u ? m : 53u);
    const unsigned mem = ln / 432u, rem = ln - mem * 432u, row = rem / 9u;
    c.vtch = (288u * mem + row) * W2ROW + (rem - row * 9u) * 128u;
    c.pref = poll_off(c, false);
  }
  if constexpr (S == 6) {
    const unsigned v0 = b.vw1 ^ (c.hf << 5);
#pragma unroll
    for (int i = 0; i < 4; ++i) c.vpat[i] = (v0 ^ ((i & 1) << 5)) + i * (8u * W1ROW - 1024u);
    c.vpat4 = v0 + 32u * W1ROW;
    bias_quad<0>(c.acc[1], c.tb + 256u); bias_quad<1>(c.acc[1], c.tb + 256u); bias_quad<2>(c.acc[1], c.tb + 256u); bias_quad<3>(c.acc[1], c.tb + 256u);
  }
  if constexpr (S < 8) bases0<S + 1>(c, b);
  GAP();
  mmB<S, 0>(c, b);
  GAP();
}
DI float half_sum(float v) {
  v = dpp_add<0x121>(v); v = dpp_add<0x122>(v); v = dpp_add<0x124>(v); v = dpp_add<0x128>(v);
  auto a = __builtin_amdgcn_permlane16_swap(__float_as_uint(v), __float_as_uint(v), false, false);
  return __uint_as_float(a[0]) + __uint_as_float(a[1]);
}
template <int HF> DI void prologue(Ctx& c, B0& b, const srd_t sx, const srd_t sb1) {
  const unsigned l = threadIdx.x & 63u, g = l >> 4, qq = (l >> 2) & 3u, pp = l & 3u, h = l >> 5;
  unsigned epoch;
  b.vw1 = (l >> 3) * W1ROW + (((l & 7u) ^ ((g & 1u) << 2) ^ ((c.w & 1u) << 1)) << 4);
  b.vc8 = (l >> 2) * W1ROW + ((pp ^ (h << 1)) << 4);
  if constexpr (HF == 0) {
    const unsigned row = (c.r * 128u + (c.j * 4u + c.q) * 8u + 2u * c.t) * XROW, va = h * XROW + (l & 31u) * 16u, vb = h * XROW + 1024u + (l & 31u) * 4u;
    u32x4 xa, xc; unsigned xb;
    asm volatile("buffer_load_dwordx4 %0, %1, %2, %3 offen" : "=v"(xa) : "v"(va), "s"(sx), "s"(row));
    asm volatile("buffer_load_dwordx4 %0, %1, %2, %3 offen offset:512" : "=v"(xc) : "v"(va), "s"(sx), "s"(row));
    asm volatile("buffer_load_dword %0, %1, %2, %3 offen" : "=v"(xb) : "v"(vb), "s"(sx), "s"(row));
    asm volatile("buffer_load_dword %0, off, %1, %2 sc1" : "=v"(epoch) : "s"(c.sw), "s"(WS_EPOCH + c.r * 128u));
    GAP();
    asm volatile("s_waitcnt vmcnt(1)" : "+v"(xa), "+v"(xb), "+v"(xc)::"memory");
    float v[18];
#pragma unroll
    for (int i = 0; i < 4; ++i) {
      v[2 * i] = __uint_as_float(xa[i] << 16); v[2 * i + 1] = __uint_as_float(xa[i] & 0xffff0000u);
      v[8 + 2 * i] = __uint_as_float(xc[i] << 16); v[9 + 2 * i] = __uint_as_float(xc[i] & 0xffff0000u);
    }
    v[16] = __uint_as_float(xb << 16); v[17] = __uint_as_float(xb & 0xffff0000u);
    float s0 = v[16], s1 = v[17];
#pragma unroll
    for (int i = 0; i < 8; ++i) { s0 += v[2 * i]; s1 += v[2 * i + 1]; }
    const float mean = half_sum(s0 + s1) * (1.0f / 576.0f);
#pragma unroll
    for (int i = 0; i < 18; ++i) v[i] -= mean;
    s0 = v[16] * v[16]; s1 = v[17] * v[17];
#pragma unroll
    for (int i = 0; i < 8; ++i) { s0 = __builtin_fmaf(v[2 * i], v[2 * i], s0); s1 = __builtin_fmaf(v[2 * i + 1], v[2 * i + 1], s1); }
    const float rs = KAP * __builtin_amdgcn_rsqf(half_sum(s0 + s1) * (1.0f / 576.0f) + 1e-5f);
    u32x4 ya, yc;
#pragma unroll
    for (int i = 0; i < 4; ++i) { ya[i] = cvtpk(v[2 * i] * rs, v[2 * i + 1] * rs); yc[i] = cvtpk(v[8 + 2 * i] * rs, v[9 + 2 * i] * rs); }
    const unsigned yb = cvtpk(v[16] * rs, v[17] * rs);
    asm volatile("buffer_store_dwordx4 %0, %1, %2, %3 offen sc1" ::"v"(ya), "v"(va), "s"(c.sw), "s"(WS_Y + row));
    asm volatile("buffer_store_dwordx4 %0, %1, %2, %3 offen offset:512 sc1" ::"v"(yc), "v"(va), "s"(c.sw), "s"(WS_Y + row));
    asm volatile("buffer_store_dword %0, %1, %2, %3 offen sc1\n\ts_nop 1" ::"v"(yb), "v"(vb), "s"(c.sw), "s"(WS_Y + row));
    GAP();
    STAMP(1);
  b.vy = ((l >> 3) + 8u * c.w) * XROW + (((l & 7u) ^ ((g + 4u * c.w) & 7u)) << 4);
  b.lw = (8u * g + qq) * 128u + (((4u * HF + 2u * (g & 1u) + (pp >> 1)) ^ ((qq >> 1) << 2)) << 4) + 8u * (pp & 1u);
  const unsigned xrow = 32u * c.t + 16u * HF + (l & 15u);
  b.lx = xrow * 128u + ((g ^ ((xrow >> 1) & 7u)) << 4);
  b.l80 = (8u * g + qq) * 64u + ((HF ^ (g & 1u)) << 5) + 8u * pp;
  c.tb = L_B1 + HF * 128u + g * 16u;
  c.vhld[0] = l * 16u + HF * 1024u; c.vhld[1] = c.vhld[0] ^ 1024u;
  bases0<0>(c, b);
    GAP();
    asm volatile("s_waitcnt vmcnt(0)\n\ts_barrier" : "+v"(epoch)::"memory");
    c.tag = rfl(epoch) + 1u;
    STAMP(31);
    store_flag<1>(c);
    issue_w<0>(c, b); issue_w<1>(c, b); issue_w<2>(c, b); issue_w<3>(c, b); issue_w<4>(c, b);
  } else {
    unsigned b1a, b1b;
    {
      unsigned len;
      asm volatile("s_mov_b32 %0, mixer_code_end-mixer_code_begin-4" : "=s"(len));
      const unsigned off = (c.t * 64u + l) * 128u;
      asm volatile("s_mov_b32 m0, %0\n\ts_nop 0\n\tglobal_load_lds_dword %1, %2" ::"s"(L_JUNK), "v"(off < len ? off : len), "s"(c.pc));
    }
    asm volatile("buffer_load_dword %0, off, %1, %2 sc1" : "=v"(epoch) : "s"(c.sw), "s"(WS_EPOCH + c.r * 128u));
    asm volatile("buffer_load_ushort %0, %1, %2, %3 offen" : "=v"(b1a) : "v"((c.t * 64u + l) * 2u), "s"(sb1), "s"(c.freg2));
    asm volatile("buffer_load_ushort %0, %1, %2, %3 offen" : "=v"(b1b) : "v"((l & 31u) * 2u), "s"(sb1), "s"(c.f82));
    issue_w<0>(c, b); issue_w<1>(c, b); issue_w<2>(c, b); issue_w<3>(c, b); issue_w<4>(c, b);
    const unsigned uln = (c.r * 4u + c.q) + 64u * (c.t * 54u + (l < 54u ? l : 53u));
    if (c.t < 3) {
      const unsigned rw = uln / 18u;
      asm volatile("s_mov_b32 m0, %0\n\ts_nop 0\n\tbuffer_load_dword %1, %2, %3 offen lds" ::"s"(L_JUNK),
                   "v"(rw * W1ROW + (uln - rw * 18u) * 128u), "s"(c.s1), "s"(c.j * 2304u));
    }
    STAMP(1);
  b.vy = ((l >> 3) + 8u * c.w) * XROW + (((l & 7u) ^ ((g + 4u * c.w) & 7u)) << 4);
  b.lw = (8u * g + qq) * 128u + (((4u * HF + 2u * (g & 1u) + (pp >> 1)) ^ ((qq >> 1) << 2)) << 4) + 8u * (pp & 1u);
  const unsigned xrow = 32u * c.t + 16u * HF + (l & 15u);
  b.lx = xrow * 128u + ((g ^ ((xrow >> 1) & 7u)) << 4);
  b.l80 = (8u * g + qq) * 64u + ((HF ^ (g & 1u)) << 5) + 8u * pp;
  c.tb = L_B1 + HF * 128u + g * 16u;
  c.vhld[0] = l * 16u + HF * 1024u; c.vhld[1] = c.vhld[0] ^ 1024u;
  bases0<0>(c, b);
#pragma unroll
    for (int k = 0; k < 3; ++k) {
      const unsigned i = k < 2 ? 4u * k + c.t : 8u, nn = 64u * i + l, rho = nn / 18u, chn = nn - rho * 18u;
      const unsigned rel = i < 5u ? i : i - 5u, o = chn * 16u - (rel < 4u ? rel * 1024u : 0u);
      *LDSP(unsigned, L_PAT + i * 256u + l * 4u) = rho * W2ROW + o;
      *LDSP(unsigned, L_PAT + 2304u + i * 256u + l * 4u) = ((rho & 3u) | ((rho & 12u) << 1) | ((rho & 16u) >> 2)) * W2ROW + o;
    }
    asm volatile("s_waitcnt vmcnt(%3)" : "+v"(b1a), "+v"(b1b), "+v"(epoch) : "i"(5) : "memory");
    c.tag = rfl(epoch) + 1u;
    *LDSP(float, L_B1 + (c.t * 64u + l) * 4u) = __uint_as_float(b1a << 16) * KAP;
    *LDSP(float, L_B1 + 1024u + (l & 31u) * 4u) = __uint_as_float(b1b << 16) * KAP;
    *LDSP(unsigned, L_PROG + (l & 7u) * 4u) = 0u;
    asm volatile("s_barrier" ::: "memory");
    STAMP(31);
    if (c.t < 3)
      asm volatile("s_mov_b32 m0, %0\n\ts_nop 0\n\tbuffer_load_dword %1, %2, %3 offen lds" ::"s"(L_JUNK), "v"(uln * 128u), "s"(c.s2), "s"(c.j * 1152u * W2ROW));
    if (c.w == 7) {
      const unsigned voff = WS_FLAG + 128u + ((c.r * 16u + (l & 15u)) << 10);
      __builtin_amdgcn_s_sleep(PDELAY);
      for (unsigned spin = 0; spin < (1u << 16); ++spin) {
        unsigned pv;
        asm volatile("s_sleep %3\n\tbuffer_load_dword %0, %1, %2, 0 offen sc1\n\ts_waitcnt vmcnt(0)" : "=&v"(pv) : "v"(voff), "s"(c.sw), "i"(PSLEEP) : "memory");
        if (__builtin_amdgcn_ballot_w64(pv != c.tag) == 0) break;
      }
    }
  }
}
DI void block0(Ctx& c, B0& b) {
  BARRIER_LGKM();
  STAMP(2);
  issue_y<0>(c, b); issue_y<1>(c, b);
  bias_quad<0>(c.acc[0], c.tb); bias_quad<1>(c.acc[0], c.tb); bias_quad<2>(c.acc[0], c.tb); bias_quad<3>(c.acc[0], c.tb);
  c.acc8[0] = *LDSP(volatile f32x4, c.tb + 1024u - c.hf * 64u); c.acc8[1] = *LDSP(volatile f32x4, c.tb + 1024u - c.hf * 64u);
  slice<0>(c, b); slice<1>(c, b); slice<2>(c, b); slice<3>(c, b); slice<4>(c, b);
  slice<5>(c, b); slice<6>(c, b); slice<7>(c, b); slice<8>(c, b);
  mmB<8, 1>(c, b);
  c.wb = opq(b.lw);
  asm volatile("s_waitcnt vmcnt(%0) lgkmcnt(0)\n\ts_barrier" ::"i"(4 + ((W1PREF & 8) ? 1 : 0)) : "memory");
}

__global__ __launch_bounds__(512, 2) void mixer_mlp_kernel(const void* __restrict__ x, const void* __restrict__ w1, const void* __restrict__ b1,
                                                           const void* __restrict__ w2, const void* __restrict__ b2, void* __restrict__ out,
                                                           void* __restrict__ ws) {
  Ctx c;
  asm volatile("s_getpc_b64 %0\nmixer_code_begin:" : "=s"(c.pc));
  const unsigned blk = blockIdx.x, xg = blk & 7u, idx = blk >> 3;
  c.r = 8u * (xg >> 2) + (idx >> 2); c.j = xg & 3u; c.q = idx & 3u;
  c.w = rfl(threadIdx.x >> 6); c.t = c.w & 3u; c.hf = c.w >> 2;
  STAMP(0);
  const srd_t sx = __builtin_amdgcn_make_buffer_rsrc((void*)x, 0, S_ * D_ * 2, 0x00020000);
  c.s1 = __builtin_amdgcn_make_buffer_rsrc((void*)w1, 0, D_ * F_ * 2, 0x00020000);
  c.s2 = __builtin_amdgcn_make_buffer_rsrc((void*)w2, 0, D_ * F_ * 2, 0x00020000);
  c.sw = __builtin_amdgcn_make_buffer_rsrc(ws, 0, WS_USED, 0x00020000);
  c.sb2 = __builtin_amdgcn_make_buffer_rsrc((void*)b2, 0, D_ * 2, 0x00020000);
  const srd_t sb1 = __builtin_amdgcn_make_buffer_rsrc((void*)b1, 0, F_ * 2, 0x00020000);
  c.freg2 = (1152u * c.j + fstart(c.q, 0)) * 2u;
  c.f82 = (1152u * c.j + fstart(c.q, 8)) * 2u;
  c.fown = WS_FLAG + (((c.r * 4u + c.j) * 4u + c.q) << 10);
  c.pollv = 0u;
  c.sw1 = (72u * c.t + 40u * c.hf) * W1ROW + c.freg2;
  c.ldst = c.t * 9216u + c.hf * 5120u;
  B0 b0;
  if (c.hf == 0) prologue<0>(c, b0, sx, sb1); else prologue<1>(c, b0, sx, sb1);
  asm volatile("" : "+s"(c.sw1), "+s"(c.ldst), "+s"(c.freg2), "+s"(c.f82), "+s"(c.r), "+s"(c.w), "+s"(c.t));
  block0(c, b0);
  {
    const unsigned hb = WS_H + ((c.r * 4u + c.j) * 144u + c.t) * 2048u;
#pragma unroll
    for (int d = 0; d < 4; ++d) c.hq[d] = hb + ((c.q + d) & 3u) * 73728u + c.hf * 8192u;
#pragma unroll
    for (int e = 0; e < 2; ++e) {
      c.h8[e] = hb + ((c.q + 2u * c.hf + e) & 3u) * 73728u + 65536u;
      c.sw2[e] = (1152u * c.j + fstart((c.q + 2u * e + (c.t & 1u)) & 3u, c.t >> 1)) * W2ROW + 288u * c.q;
    }
    c.sw28 = (1152u * c.j + fstart((c.q + c.t) & 3u, 8)) * W2ROW + 288u * c.q;
  }
  asm volatile("" : "+s"(c.sw1), "+s"(c.ldst), "+s"(c.hq[0]), "+s"(c.hq[1]), "+s"(c.hq[2]), "+s"(c.hq[3]), "+s"(c.h8[0]), "+s"(c.h8[1]),
               "+s"(c.sw2[0]), "+s"(c.sw2[1]), "+s"(c.sw28));
  STAMP(12);
  half_stage<0>(c); STAMP(13);
  half_stage<1>(c); STAMP(14);
  half_stage<2>(c); STAMP(15);
  half_stage<3>(c); STAMP(16);
  half_stage<4>(c); STAMP(17);
  half_stage<5>(c); STAMP(18);
  half_stage<6>(c); STAMP(19);
  half_stage<7>(c); STAMP(20);
  half_stage<8>(c); STAMP(21);
  half_stage<9>(c); STAMP(22);
  half_stage<10>(c); STAMP(23);
  half_stage<11>(c); STAMP(24);
  half_stage<12>(c); STAMP(25);
  half_stage<13>(c); STAMP(26);
  half_stage<14>(c); STAMP(27);

  const unsigned l = lane_id();
  comb<4, 0>(c);
  comb<3, 1>(c); comb<3, 2>(c); comb<3, 3>(c);
  comb<4, 1>(c); comb<4, 2>(c); comb<4, 3>(c);
  BARRIER_LGKM();
  STAMP(28);
  {
    const u32x2 bA = c.b2v[0], bB = c.b2v[1], bC = c.b2v[2];
    const unsigned sp = WS_P + ((c.r * 4u + c.q) * 4u + c.j) * 4u * 9216u + c.hf * 4608u;
    const unsigned s0 = sp + ((c.j + 1u) & 3u) * 9216u, s1 = sp + ((c.j + 2u) & 3u) * 9216u, s2 = sp + ((c.j + 3u) & 3u) * 9216u;
    const unsigned va = l * 16u + c.t * 1024u, vb = 4096u + l * 8u, oa = (c.hf ^ 1u) * 3u * SLOT + c.j * 9216u + l * 16u;
    u32x4 pa[3]; u32x2 pb[3];
    f32x4 o0 = ldsf4(oa + c.t * 2048u), o1 = ldsf4(oa + c.t * 2048u + 1024u), o2 = ldsf4(oa + 8192u);
    if (c.t == 3) __builtin_amdgcn_s_sleep(TDELAY);
    for (unsigned spin = 0; spin < (1u << 16); ++spin) {
      asm volatile("buffer_load_dwordx4 %0, %6, %8, %9 offen sc1\n\tbuffer_load_dwordx4 %1, %6, %8, %10 offen sc1\n\t"
                   "buffer_load_dwordx4 %2, %6, %8, %11 offen sc1\n\t"
                   "s_cmp_lg_u32 %12, 3\n\ts_cbranch_scc1 .Lskip%=\n\t"
                   "buffer_load_dwordx2 %3, %7, %8, %9 offen sc1\n\tbuffer_load_dwordx2 %4, %7, %8, %10 offen sc1\n\t"
                   "buffer_load_dwordx2 %5, %7, %8, %11 offen sc1\n.Lskip%=:\n\ts_waitcnt vmcnt(0)"
                   : "=&v"(pa[0]), "=&v"(pa[1]), "=&v"(pa[2]), "=&v"(pb[0]), "=&v"(pb[1]), "=&v"(pb[2])
                   : "v"(va), "v"(vb), "s"(c.sw), "s"(s0), "s"(s1), "s"(s2), "s"(c.t) : "memory", "scc");
      unsigned mx = 0;
#pragma unroll
      for (int k = 0; k < 3; ++k) {
#pragma unroll
        for (int i = 0; i < 4; ++i) mx = mx > pa[k][i] ? mx : pa[k][i];
        if (c.t == 3) { mx = mx > pb[k][0] ? mx : pb[k][0]; mx = mx > pb[k][1] ? mx : pb[k][1]; }
      }
      if (!__any(mx == ~0u)) break;
      __builtin_amdgcn_s_sleep(2);
    }
    STAMP(29);
#pragma unroll
    for (int k = 0; k < 3; ++k)
#pragma unroll
      for (int i = 0; i < 2; ++i) {
        o0[2 * i] += __uint_as_float(pa[k][i] << 16); o0[2 * i + 1] += __uint_as_float(pa[k][i] & 0xffff0000u);
        o1[2 * i] += __uint_as_float(pa[k][2 + i] << 16); o1[2 * i + 1] += __uint_as_float(pa[k][2 + i] & 0xffff0000u);
      }
    unsigned short* op = (unsigned short*)out + (size_t)(128u * c.r + 32u * c.j + 16u * c.hf + (l & 15u)) * D_ + 144u * c.q;
    const unsigned e0 = cvtpk(__builtin_fmaf(o0[0], RKAP, __uint_as_float(bA[0] << 16)), __builtin_fmaf(o0[1], RKAP, __uint_as_float(bA[0] & 0xffff0000u)));
    const unsigned e1 = cvtpk(__builtin_fmaf(o0[2], RKAP, __uint_as_float(bA[1] << 16)), __builtin_fmaf(o0[3], RKAP, __uint_as_float(bA[1] & 0xffff0000u)));
    const unsigned f0 = cvtpk(__builtin_fmaf(o1[0], RKAP, __uint_as_float(bB[0] << 16)), __builtin_fmaf(o1[1], RKAP, __uint_as_float(bB[0] & 0xffff0000u)));
    const unsigned f1 = cvtpk(__builtin_fmaf(o1[2], RKAP, __uint_as_float(bB[1] << 16)), __builtin_fmaf(o1[3], RKAP, __uint_as_float(bB[1] & 0xffff0000u)));
    auto r0 = __builtin_amdgcn_permlane16_swap(e0, f0, false, false);
    auto r1 = __builtin_amdgcn_permlane16_swap(e1, f1, false, false);
    *(u32x4*)(op + 32u * c.t + 16u * ((l >> 4) & 1u) + 8u * (l >> 5)) = (u32x4){r0[0], r1[0], r0[1], r1[1]};
    if (c.t == 3) {
#pragma unroll
      for (int k = 0; k < 3; ++k)
#pragma unroll
        for (int i = 0; i < 2; ++i) { o2[2 * i] += __uint_as_float(pb[k][i] << 16); o2[2 * i + 1] += __uint_as_float(pb[k][i] & 0xffff0000u); }
      *(u32x2*)(op + 128u + 4u * (l >> 4)) =
          (u32x2){cvtpk(__builtin_fmaf(o2[0], RKAP, __uint_as_float(bC[0] << 16)), __builtin_fmaf(o2[1], RKAP, __uint_as_float(bC[0] & 0xffff0000u))),
                  cvtpk(__builtin_fmaf(o2[2], RKAP, __uint_as_float(bC[1] << 16)), __builtin_fmaf(o2[3], RKAP, __uint_as_float(bC[1] & 0xffff0000u)))};
    }
    asm volatile("mixer_code_end:");
    if (c.w == 0 && c.j == 0 && c.q == 0 && l == 0)
      asm volatile("buffer_store_dword %0, off, %1, %2 sc1" ::"v"(c.tag), "s"(c.sw), "s"(WS_EPOCH + c.r * 128u));
  }
}

extern "C" void kernel_launch(void* const* d_in, const int* in_sizes, int n_in, void* d_out, int out_size, void* d_ws, size_t ws_size,
                              hipStream_t stream) {
  (void)in_sizes; (void)n_in; (void)out_size; (void)ws_size;
  (void)hipFuncSetAttribute((const void*)mixer_mlp_kernel, hipFuncAttributeMaxDynamicSharedMemorySize, LDS_BYTES);
  mixer_mlp_kernel<<<256, 512, LDS_BYTES, stream>>>(d_in[0], d_in[1], d_in[2], d_in[3], d_in[4], d_out, d_ws);
}
```

```cpp
#include <hip/hip_runtime.h>
#include <stdint.h>
#include <utility>

typedef __bf16 bf16x8 __attribute__((ext_vector_type(8)));
typedef __bf16 bf16x2 __attribute__((ext_vector_type(2)));
typedef short s16x4 __attribute__((ext_vector_type(4)));
typedef float f32x16 __attribute__((ext_vector_type(16)));
typedef float f32x4 __attribute__((ext_vector_type(4)));
typedef float f32x2 __attribute__((ext_vector_type(2)));
typedef unsigned u32x4 __attribute__((ext_vector_type(4)));
typedef unsigned u32x2 __attribute__((ext_vector_type(2)));
typedef __amdgpu_buffer_rsrc_t srd_t;
#define DI __device__ __forceinline__
#define LAS __attribute__((address_space(3)))
#define LDSP(T, a) ((LAS T*)(size_t)(a))

constexpr unsigned S_ = 2048, D_ = 576, F_ = 4608;
constexpr unsigned XROW = D_ * 2, W1ROW = F_ * 2, W2ROW = D_ * 2;
constexpr unsigned SLOT = 36864, RING = 4 * SLOT, SLICE = 28672;
constexpr unsigned L_B1 = RING, L_PAT = L_B1 + 1152, L_JUNK = L_PAT + 4608, L_PROG = L_JUNK + 256, LDS_BYTES = L_PROG + 64;
static_assert(LDS_BYTES <= 163840, "LDS");
constexpr unsigned WS_EPOCH = 0, WS_FLAG = 4096, WS_Y = WS_FLAG + 256 * 8 * 128, WS_H = WS_Y + S_ * D_ * 2, H_BYTES = S_ * F_ * 2;
constexpr unsigned WS_P = WS_H + H_BYTES, P_BYTES = 16 * 4 * 4 * 4 * 9216;
constexpr unsigned WS_USED = WS_P + P_BYTES;
constexpr float KAP = 0.4686686930532063f, GAM = 4.91222953926424f, RKAP = 2.1337034344781243f;
#define KSW 10
#define PSLEEP 1
#define TOUCH 1
#define PREFH 1
#define HK2 2
#define CPREF 1
#define W1PREF 24
#define PDELAY 10
#define TDELAY 8
#define EARLYMARK 0
#define BPRIO 0
#define PREF 1
#define STAMP(i) do {} while (0)

DI unsigned rfl(unsigned x) { return __builtin_amdgcn_readfirstlane(x); }
DI s16x4 trr(unsigned a) { return __builtin_amdgcn_ds_read_tr16_b64_v4i16(LDSP(s16x4, a)); }
DI bf16x8 cat(s16x4 lo, s16x4 hi) { return __builtin_bit_cast(bf16x8, __builtin_shufflevector(lo, hi, 0, 1, 2, 3, 4, 5, 6, 7)); }
DI f32x4 ldsf4(unsigned a) { return *LDSP(f32x4, a); }
DI unsigned cvtpk(float lo, float hi) { f32x2 v = {lo, hi}; return __builtin_bit_cast(unsigned, __builtin_convertvector(v, bf16x2)); }
template <int CTRL> DI float dpp_add(float v) {
  return v + __builtin_bit_cast(float, __builtin_amdgcn_update_dpp(0, __builtin_bit_cast(int, v), CTRL, 0xf, 0xf, false));
}
DI void mfma16(f32x4& acc, bf16x8 a, bf16x8 b) { asm volatile("v_mfma_f32_16x16x32_bf16 %0, %1, %2, %0" : "+v"(acc) : "v"(a), "v"(b)); }
DI void mfma16z(f32x4& acc, bf16x8 a, bf16x8 b) { asm volatile("v_mfma_f32_16x16x32_bf16 %0, %1, %2, 0" : "=&v"(acc) : "v"(a), "v"(b)); }
#define BARRIER_LGKM() asm volatile("s_waitcnt lgkmcnt(0)\n\ts_barrier" ::: "memory")
#define GAP() __builtin_amdgcn_sched_barrier(0)
#define PIN(x) asm volatile("" : "+v"(x))

struct Ctx {
  srd_t s1, s2, sw;
  unsigned long long pc;
  unsigned w, t, hf, r, j, q;
  unsigned freg2, f82;
  unsigned tag, sdma, sw1, ldst, hq[4], h8[2], sw2[2], sw28, fown;
  u32x4 vpat;
  unsigned vpat4;
  unsigned wb, wb2;
  unsigned vhld[2], tb, pref, vtch, xw, xr, pg, sp, vpg;
  u32x4 pk;
  u32x2 b2v[3];
  f32x4 xg[2];
  srd_t sb2;
  float gz[2], gm, gz8, gm8;
  unsigned own[4][8], hv8[8];
  bf16x8 X[36];
  bf16x8 F[4];
  f32x4 acc[2][4];
  f32x4 acc8[2];
  f32x4 acc2[2][9];
  u32x4 Hb[8][2];
  unsigned pollv;
};
DI unsigned lane_id() {
  unsigned l;
  asm volatile("v_mbcnt_lo_u32_b32 %0, -1, 0\n\tv_mbcnt_hi_u32_b32 %0, -1, %0" : "=v"(l));
  return l;
}
DI unsigned opq(unsigned v) { PIN(v); return v; }

constexpr int pair_of(int h) { return h < 8 ? 0 : (h - 7) / 2; }
constexpr int ph_of(int h) { return h < 8 ? h - 6 : (h - 7) % 2; }
constexpr bool own_unit(int n) { return n == 0 || n == 6 || n == 10 || n == 14; }
constexpr int hload_at(int h, int k) {
  if (h == 8 || h == 10) return k == 0 ? 4 * (h - 8) + 14 : k == 2 ? 4 * (h - 8) + 15 : -1;
  if (h == 11 && (k == 1 || k == 3)) return 30 + (k == 3);
  if (h == 12) return k == -2 ? 32 : k == 0 ? 33 : k == 2 ? 34 : k == 4 ? 35 : -1;
  const int slot = k == 0 ? 0 : k == 2 ? 1 : k == 10 ? 2 : k == 12 ? 3 : k == 1 ? 4 : k == 3 ? 5 : -1;
  if (slot < 0) return -1;
  if (h == 5) return slot < 4 ? 2 + slot : -1;
  if (h == 6) return k < 4 ? 6 + k : 8 + slot;
  if (h < 8 || h > 11 || slot > 3) return -1;
  return 12 + 4 * (h - 8) + slot;
}
constexpr bool b2_at(int h, int k) { return h == 12 && k == 6; }
constexpr int dma_at(int h, int k) {
  if (h == 0) return (k < 16 && (k & 1) == 0) ? (k < 8 ? 2 * 8 + (k + 2) / 2 : 3 * 8 + (k - 8) / 2) : (k == 15 ? 3 * 8 + 4 : -1);
  if (h + 3 > 14) return -1;
  const int i = h < 4 ? (k == -2 ? 0 : k == 2 ? 1 : k == 6 ? 2 : k == 10 ? 3 : k == 13 ? 4 : -1)
                      : (k == -2 ? 0 : k == 4 ? 1 : k == 6 ? 2 : k == 8 ? 3 : k == 14 ? 4 : -1);
  return i < 0 ? -1 : (h + 3) * 8 + i;
}
constexpr int poll_k(int h) { return h == 4 ? 5 : h == 10 ? 5 : (h == 7 || h == 9 || h == 11) ? -1 : -9; }
constexpr int poll_ev(int h) { return h == 4 ? 2 : h == 7 ? 3 : h == 10 ? 5 : 4; }
constexpr bool poll_at(int h, int k) { return k == poll_k(h); }
constexpr int flag_at(int h) { return (h == 3 || h == 5 || h == 7 || h == 9) ? (h + 1) / 2 : 0; }
constexpr bool hst_at(int h, int k) { return (h < 8 && (h & 1) && (k == 0 || k == 15)) || (h == 1 && k == 12); }
constexpr bool pref_at(int h, int k) { return PREF && h < 6 && k == 15; }
constexpr bool touch_at(int h, int k) { return TOUCH && (h == 2 || h == 4 || h == 6 || h == 8) && k == 13; }
constexpr bool mark_at(int h, int k) { return !EARLYMARK && ((h == 2 && (k == 4 || k == 12)) || (h == 4 && k == 2)); }
constexpr int nops(int h, int k, int n) {
  return (b2_at(h, k) ? 3 : 0) + (hload_at(h, k) >= 0 ? 1 : 0) + (hst_at(h, k) ? 1 : 0) + (mark_at(h, k) ? 1 : 0) +
         (dma_at(h, k) >= 0 && (dma_at(h, k) & 7) < n ? 1 : 0);
}
constexpr int between(int h0, int k0, int h1, int k1, int n) {
  int s = 0;
  for (int h = h0; h <= h1; ++h)
    for (int k = -2; k < 16; ++k)
      if ((h > h0 || k > k0) && (h < h1 || k < k1)) s += nops(h, k, n);
  for (int h = h0; h <= h1; ++h)
    if (pref_at(h, 15) && (h < h1 || k1 == 16)) s += 1;
  return s;
}
constexpr int cmin(int a, int b) { return a < b ? a : b; }

DI unsigned fstart(unsigned qq, unsigned ch) { return 32u * (9u * qq + ch + (qq & 1u) * (1u - 9u * (ch >> 3))); }

DI void dma16(unsigned m0v, unsigned voff, srd_t srd, unsigned soff) {
  asm volatile("s_mov_b32 m0, %0\n\ts_nop 0\n\tbuffer_load_dwordx4 %1, %2, %3 offen lds" ::"s"(m0v), "v"(voff), "s"(srd), "s"(soff));
}
template <int HS, int I> DI void issue_piece(Ctx& c) {
  const srd_t sd = HS < 6 ? c.s1 : c.s2;
  if constexpr (I == 0) {
    if constexpr (HS < 6) c.sdma = c.sw1 + ((HS & 1) * 288u * W1ROW + ((HS >> 1) + 1) * 128u);
    else c.sdma = HS == 8 ? c.sw28 : c.sw2[ph_of(HS)] + pair_of(HS) * 64u * W2ROW;
    dma16((HS & 3) * SLOT + c.ldst, c.vpat[0], sd, c.sdma);
  } else if constexpr (I < 4) {
    asm volatile("buffer_load_dwordx4 %0, %1, %2 offen offset:%3 lds" ::"v"(c.vpat[I]), "s"(sd), "s"(c.sdma), "i"(1024 * I));
  } else {
    asm volatile("s_cmp_lg_u32 %4, 0\n\ts_cbranch_scc1 .Lskip%=\n\ts_mov_b32 m0, %0\n\ts_nop 0\n\tbuffer_load_dwordx4 %1, %2, %3 offen lds\n.Lskip%=:"
                 ::"s"((HS & 3) * SLOT + 4096u + c.ldst), "v"(c.vpat4), "s"(sd), "s"(c.sdma), "s"(c.hf) : "scc");
  }
}
template <int HS> DI void issue_slot(Ctx& c) {
  issue_piece<HS, 0>(c); issue_piece<HS, 1>(c); issue_piece<HS, 2>(c); issue_piece<HS, 3>(c); issue_piece<HS, 4>(c);
}
template <int N, int U> DI void issue_hload(Ctx& c) {
  constexpr int h = 6 + N / 2;
  const unsigned so = h == 8 ? c.h8[N & 1] : c.hq[2 * ph_of(h) + (N & 1)] + pair_of(h) * 16384u;
  asm volatile("buffer_load_dwordx4 %0, %1, %2, %3 offen sc1" : "=v"(c.Hb[N & 7][U]) : "v"(c.vhld[U]), "s"(c.sw), "s"(so));
}
DI unsigned poll_off(const Ctx& c, bool dest) {
  const unsigned l = lane_id(), dp = (l & 3u) ? (l & 3u) : 1u;
  return WS_FLAG + ((dest ? (c.r * 4u + ((c.j + dp) & 3u)) * 4u + c.q : (c.r * 4u + c.j) * 4u + ((c.q + dp) & 3u)) << 10);
}
template <int EV> DI void store_flag(Ctx& c) {
  const u32x4 v = {c.tag, c.tag, c.tag, c.tag};
  asm volatile("s_cmp_lg_u32 %4, 0\n\ts_cbranch_scc1 .Lskip%=\n\ts_mov_b64 exec, 0xff\n\t"
               "buffer_store_dwordx4 %0, %1, %2, %3 offen offset:%5 sc1\n\ts_mov_b64 exec, -1\n.Lskip%=:\n\ts_nop 0"
               ::"v"(v), "v"(c.vhld[0]), "s"(c.sw), "s"(c.fown), "s"(c.w), "i"(EV * 128) : "scc");
}
template <int EV> DI void issue_poll(Ctx& c, unsigned& pv, unsigned voff) {
  asm volatile("s_cmp_lg_u32 %3, 7\n\ts_cbranch_scc1 .Lskip%=\n\tbuffer_load_dword %0, %1, %2, 0 offen offset:%4 sc1\n.Lskip%=:"
               : "+v"(pv) : "v"(voff), "s"(c.sw), "s"(c.w), "i"(EV * 128) : "scc");
}
template <int EV, int N> DI void check_poll(Ctx& c, unsigned& pv, bool dest) {
  if (c.w == 7) {
    asm volatile("s_waitcnt vmcnt(%1)" : "+v"(pv) : "i"(N) : "memory");
    if (__builtin_expect(__builtin_amdgcn_ballot_w64(pv != c.tag) != 0, 0)) {
      const unsigned voff = poll_off(c, dest);
      for (unsigned spin = 0; spin < (1u << 16); ++spin) {
        unsigned v;
        asm volatile("s_sleep 1\n\tbuffer_load_dword %0, %1, %2, 0 offen offset:%3 sc1\n\ts_waitcnt vmcnt(0)" : "=&v"(v) : "v"(voff), "s"(c.sw), "i"(EV * 128) : "memory");
        if (__builtin_amdgcn_ballot_w64(v != c.tag) == 0) break;
      }
    }
  }
}
template <int P> DI void store_h(Ctx& c, const unsigned (&hv)[8], unsigned vo, unsigned so) {
  const u32x4 v = {hv[4 * P], hv[4 * P + 1], hv[4 * P + 2], hv[4 * P + 3]};
  asm volatile("buffer_store_dwordx4 %0, %1, %2, %3 offen sc1\n\ts_nop 1" ::"v"(v), "v"(vo), "s"(c.sw), "s"(so));
}
template <int N0, int N1> DI void wait_vm(const Ctx&) {
  static_assert(N1 <= N0 && N1 >= 0 && N0 < 64, "vmcnt");
  asm volatile("s_waitcnt vmcnt(%0)" ::"i"(N1) : "memory");
}

template <int H> DI void set_bases(Ctx& c) {
  constexpr unsigned d = (H & 3) ? SLOT : 0u - 3u * SLOT;
  if constexpr (H == 6) {
    const unsigned l = lane_id();
    c.wb = opq(2u * SLOT + (l >> 2) * 288u + 8u * (l & 3u) + c.hf * 18432u);
  } else if constexpr (H > 0 && H < 14) c.wb = opq(c.wb + d);
  if constexpr (H < 6) c.wb2 = opq(c.wb ^ 32u);
}
template <int H, int S> DI void rd(Ctx& c) {
  constexpr int tau = 18 * (H - 13) + S;
  constexpr unsigned o = H < 6 ? 4096u * (S >> 1) : H < 13 ? 9216u * (S / 9) + 32u * (S % 9) : ((tau >> 1) & 1) * SLOT + (tau & 1) * 9216u + 32u * (tau >> 2);
  constexpr unsigned e = H < 6 ? 512u : 4608u;
  const unsigned b = (H < 6 && (S & 1)) ? c.wb2 : c.wb;
  c.F[(H * 18 + S) & 3] = cat(trr(b + o), trr(b + o + e));
}
template <int H, int S> DI void mm(Ctx& c) {
  const bf16x8 a = c.F[(H * 18 + S) & 3];
  if constexpr (H < 6) {
    constexpr int B = ((H >> 1) + 1) & 1;
    constexpr int P = 9 * (H & 1) + (S >> 1);
    mfma16(c.acc[B][S & 1], a, c.X[2 * P]);
    mfma16(c.acc[B][2 + (S & 1)], a, c.X[2 * P + 1]);
  } else {
    constexpr int tau = 18 * (H - 13) + S;
    constexpr int N = H < 13 ? 2 * (H - 6) + S / 9 : 14 + (tau & 3), dt = H < 13 ? S % 9 : tau >> 2;
    u32x4 b0 = c.Hb[N & 7][0], b1 = c.Hb[N & 7][1];
    if constexpr (own_unit(N)) {
      const unsigned(&o)[8] = c.own[pair_of(6 + N / 2)];
      b0 = (u32x4){o[0], o[1], o[2], o[3]}; b1 = (u32x4){o[4], o[5], o[6], o[7]};
    }
    if constexpr (N == 0) {
      mfma16z(c.acc2[0][dt], a, __builtin_bit_cast(bf16x8, b0));
      mfma16z(c.acc2[1][dt], a, __builtin_bit_cast(bf16x8, b1));
    } else {
      mfma16(c.acc2[0][dt], a, __builtin_bit_cast(bf16x8, b0));
      mfma16(c.acc2[1][dt], a, __builtin_bit_cast(bf16x8, b1));
    }
  }
}
template <int Q> DI void bias_quad(f32x4 (&acc)[4], unsigned a) { acc[Q] = *LDSP(volatile f32x4, a + (Q & 1) * 64u); }
template <int E, int ST> DI void gelu_stage(float s, float& z, float& m0, unsigned (&hv)[8]) {
  if constexpr (ST == 0) { z = s * __builtin_fmaf(s, s, GAM); PIN(z); }
  if constexpr (ST == 1) { z = __builtin_amdgcn_exp2f(-z); PIN(z); }
  if constexpr (ST == 2) { z = __builtin_amdgcn_rcpf(1.0f + z); PIN(z); }
  if constexpr (ST == 3) {
    if constexpr ((E & 1) == 0) { m0 = s * z; PIN(m0); }
    else { float m = s * z, mm0 = m0; PIN(m); PIN(mm0); hv[E >> 1] = cvtpk(mm0, m); PIN(hv[E >> 1]); }
  }
}
constexpr int gstart(int e) { return 1 + (e * 29) / 15; }
constexpr int gelem(int g, int st) {
  for (int e = 0; e < 16; ++e) if (gstart(e) + st == g) return e;
  return -1;
}
template <int B, int G, int ST> DI void gelu_part(Ctx& c) {
  constexpr int e = gelem(G, ST);
  if constexpr (e >= 0) gelu_stage<e, ST>(c.acc[B & 1][e >> 2][e & 3], c.gz[e & 1], c.gm, c.own[B]);
}
template <int B, int G> DI void gelu_gap(Ctx& c) {
  gelu_part<B, G, 3>(c); gelu_part<B, G, 2>(c); gelu_part<B, G, 1>(c); gelu_part<B, G, 0>(c);
  if constexpr (B < 2) {
    if constexpr (G == 27) { bias_quad<0>(c.acc[B & 1], c.tb + (B + 2) * 256u); bias_quad<1>(c.acc[B & 1], c.tb + (B + 2) * 256u); }
    if constexpr (G == 31) bias_quad<2>(c.acc[B & 1], c.tb + (B + 2) * 256u);
    if constexpr (G == 33) bias_quad<3>(c.acc[B & 1], c.tb + (B + 2) * 256u);
  }
}

template <int H0, int K0, int H1, int K1> DI void wait_after(const Ctx& c) {
  wait_vm<between(H0, K0, H1, K1, 5), between(H0, K0, H1, K1, 4)>(c);
}
DI unsigned nosent(unsigned v) { return v < 0xfffffffeu ? v : 0xfffffffeu; }
template <int M, int ST> DI void comb(Ctx& c) {
  constexpr int NT = M < 4 ? 2 : 1;
  if constexpr (ST == 0) *LDSP(f32x4, c.xw + 2 * M * 1024u) = c.acc2[1][2 * M];
  if constexpr (ST == 1) {
    if constexpr (NT == 2) *LDSP(f32x4, c.xw + (2 * M + 1) * 1024u) = c.acc2[1][2 * M + 1];
    asm volatile("s_mov_b64 exec, 1\n\tds_write_b32 %0, %1\n\ts_mov_b64 exec, -1" ::"v"(c.vpg), "v"(M + 1u) : "memory");
  }
  if constexpr (ST == 2) c.pg = *LDSP(volatile unsigned, c.vpg ^ 16u);
  if constexpr (ST == 3) {
    if (__builtin_expect(rfl(c.pg) <= (unsigned)M, 0)) {
      for (unsigned spin = 0; spin < (1u << 20); ++spin)
        if (rfl(*LDSP(volatile unsigned, c.vpg ^ 16u)) > (unsigned)M) break;
    }
    asm volatile("" ::: "memory");
#pragma unroll
    for (int i = 0; i < NT; ++i) c.xg[i] = ldsf4(c.xr + (2 * M + i) * 1024u);
  }
  if constexpr (ST == 4) {
#pragma unroll
    for (int i = 0; i < NT; ++i)
#pragma unroll
      for (int k = 0; k < 4; ++k) { c.xg[i][k] += c.acc2[0][2 * M + i][k]; PIN(c.xg[i][k]); }
  }
  if constexpr (ST == 5) {
#pragma unroll
    for (int i = 0; i < 2 * NT; ++i) { c.pk[i] = nosent(cvtpk(c.xg[i >> 1][2 * (i & 1)], c.xg[i >> 1][2 * (i & 1) + 1])); }
    PIN(c.pk);
  }
  if constexpr (ST == 6) {
    if (c.t != c.j) {
      if constexpr (NT == 2)
        asm volatile("buffer_store_dwordx4 %0, %1, %2, %3 offen offset:%4 sc1\n\ts_nop 1" ::"v"(c.pk), "v"(c.vtch), "s"(c.sw), "s"(c.sp), "i"(M * 1024));
      else
        asm volatile("buffer_store_dwordx2 %0, %1, %2, %3 offen sc1" ::"v"((u32x2){c.pk[0], c.pk[1]}), "v"(c.vtch >> 1), "s"(c.sw), "s"(c.sp + 4096u));
    } else {
#pragma unroll
      for (int i = 0; i < NT; ++i) *LDSP(f32x4, c.xr + (2 * M + i) * 1024u) = c.xg[i];
    }
  }
}
template <int TAU, int ST, int OFF> DI void comb_at(Ctx& c) {
  if constexpr (TAU >= OFF && (TAU - OFF) % 8 == 0 && (TAU - OFF) / 8 < 4) comb<(TAU - OFF) / 8, ST>(c);
}
template <int H, int K> DI void side(Ctx& c) {
  if constexpr (poll_at(H, K)) {
    if constexpr (H == 11) issue_poll<4>(c, c.pollv, poll_off(c, true));
    else issue_poll<poll_ev(H)>(c, c.pollv, c.pref);
  }
  if constexpr (K == -1 && flag_at(H)) store_flag<flag_at(H)>(c);
  if constexpr (hload_at(H, K) >= 0) issue_hload<(hload_at(H, K) >> 1), (hload_at(H, K) & 1)>(c);
  if constexpr (mark_at(H, K)) {
    constexpr unsigned k = H == 4 ? 2u : K == 4 ? 0u : 1u;
    const u32x4 ones = {~0u, ~0u, ~0u, ~0u};
    const unsigned so = WS_P + (((c.r * 4u + c.q) * 4u + c.j) * 4u + ((c.j + 1u + k) & 3u)) * 9216u, vo = lane_id() * 16u;
    asm volatile("buffer_store_dwordx4 %0, %1, %2, %3 offen sc1\n\ts_cmp_lg_u32 %4, %5\n\ts_cbranch_scc1 .Lskip%=\n\t"
                 "buffer_store_dwordx4 %0, %1, %2, %6 offen sc1\n.Lskip%=:\n\ts_nop 1"
                 ::"v"(ones), "v"(vo), "s"(c.sw), "s"(so + c.w * 1024u), "s"(c.w), "i"(k), "s"(so + 8192u) : "scc");
  }
  if constexpr (dma_at(H, K) >= 0) issue_piece<(dma_at(H, K) >> 3), (dma_at(H, K) & 7)>(c);
  if constexpr ((H == 2 && K == 14) || ((H == 4 || H == 5) && K == 15)) {
    const unsigned a = L_PAT + (H == 4 ? 2304u : 0u) + lane_id() * 4u;
#pragma unroll
    for (int i = 0; i < 4; ++i) c.vpat[i] = *LDSP(unsigned, a + (c.hf * 5u + i) * 256u);
    c.vpat4 = *LDSP(unsigned, a + 1024u);
  }
  if constexpr (b2_at(H, K)) {
    const unsigned l = lane_id();
#pragma unroll
    for (int i = 0; i < 3; ++i)
      asm volatile("buffer_load_dwordx2 %0, %1, %2, %3 offen" : "=v"(c.b2v[i]) : "v"((l >> 4) * 8u), "s"(c.sb2), "s"(288u * c.q + (i < 2 ? 64u * c.t + 32u * i : 256u)));
    c.xw = c.hf * 3u * SLOT + c.t * 9216u + l * 16u;
    c.xr = (c.hf ^ 1u) * 3u * SLOT + c.t * 9216u + l * 16u;
    c.sp = WS_P + (((c.r * 4u + c.q) * 4u + c.t) * 4u + c.j) * 9216u + c.hf * 4608u;
    c.vpg = L_PROG + c.w * 4u; c.vtch = l * 16u;
    asm volatile("" : "+v"(c.xw), "+v"(c.xr), "+v"(c.vpg), "+v"(c.vtch), "+s"(c.sp));
  }
  if constexpr (H >= 13) {
    constexpr int tau = 18 * (H - 13) + K;
    comb_at<tau, 6, 18>(c); comb_at<tau, 5, 17>(c); comb_at<tau, 4, 16>(c); comb_at<tau, 3, 14>(c);
    comb_at<tau, 2, 12>(c); comb_at<tau, 1, 9>(c); comb_at<tau, 0, 6>(c);
  }
  if constexpr (H < 8) gelu_gap<(H >> 1), K + 18 * (H & 1)>(c);
  if constexpr (H < 2) {
    constexpr int G = K + 18 * H - 1;
    if constexpr (G >= 0 && G < 28) gelu_stage<G / 4, (G & 3)>(c.acc8[G / 16][(G / 4) & 3], c.gz8, c.gm8, c.hv8);
    if constexpr (G == 28) { gelu_stage<7, 0>(c.acc8[1][3], c.gz8, c.gm8, c.hv8); gelu_stage<7, 1>(c.acc8[1][3], c.gz8, c.gm8, c.hv8); }
    if constexpr (G == 29) { gelu_stage<7, 2>(c.acc8[1][3], c.gz8, c.gm8, c.hv8); gelu_stage<7, 3>(c.acc8[1][3], c.gz8, c.gm8, c.hv8); }
  }
  if constexpr (hst_at(H, K)) {
    if constexpr (K == 12) {
      const unsigned l = lane_id();
#pragma unroll
      for (int i = 0; i < 2; ++i) {
        auto rr = __builtin_amdgcn_permlane16_swap(c.hv8[i], c.hv8[2 + i], false, false);
        c.hv8[i] = rr[0]; c.hv8[2 + i] = rr[1];
      }
      store_h<0>(c, c.hv8, (((l >> 4) & 1u) ^ c.hf) * 1024u + ((l & 15u) + 16u * (l >> 5)) * 16u, c.hq[0] + 65536u - c.hf * 7680u);
    } else store_h<(K == 0 ? 0 : 1)>(c, c.own[H >> 1], c.vhld[K == 0 ? 0 : 1], c.hq[0] + (H >> 1) * 16384u);
  }
  if constexpr (pref_at(H, K))
    asm volatile("s_mov_b32 m0, %0\n\ts_nop 0\n\tbuffer_load_dword %1, %2, %3 offen lds" ::"s"(L_JUNK), "v"(c.vtch), "s"(c.s2), "s"((c.j * 1152u + H * 48u) * W2ROW));
}
constexpr int hpos(int n) {
  for (int h = 4; h < 14; ++h) for (int k = -2; k < 16; ++k) if (hload_at(h, k) == 2 * n + 1) return h * 32 + k + 2;
  return 0;
}
constexpr int cmax(int a, int b) { return a > b ? a : b; }
template <int H> DI void end_hs(Ctx& c) {
  if constexpr (H < 13) {
    constexpr int p0 = H == 0 ? -1 : H == 1 ? 8 : H == 12 ? 12 * 32 + 8 : (H - 2) * 32 + 17;
    constexpr int p1 = (H >= 5 && !own_unit(2 * (H - 5))) ? hpos(2 * (H - 5)) : 0, p2 = 0, p3 = flag_at(H + 1) ? (H - 1) * 32 + 17 : 0;
    constexpr int p = H == 0 ? -1 : cmax(cmax(p0, p1), cmax(p2, p3));
    if constexpr (H == 0) wait_vm<between(0, -3, 0, 16, 4) + ((W1PREF & 8) ? 1 : 0), between(0, -3, 0, 16, 4) + ((W1PREF & 8) ? 1 : 0)>(c);
    else wait_after<p / 32, p % 32 - 2, H, 16>(c);
    if constexpr (poll_k(H) > -9) check_poll<poll_ev(H), between(H, poll_k(H), H, 16, 4)>(c, c.pollv, H == 11);
    BARRIER_LGKM();
  }
  if constexpr (H == 12) {
#pragma unroll
    for (int n = 15; n < 18; ++n) { PIN(c.Hb[n & 7][0]); PIN(c.Hb[n & 7][1]); }
    PIN(c.b2v[0]); PIN(c.b2v[1]); PIN(c.b2v[2]);
  }
  if constexpr (H >= 5 && H < 12 && !own_unit(2 * (H - 5))) { PIN(c.Hb[(2 * (H - 5)) & 7][0]); PIN(c.Hb[(2 * (H - 5)) & 7][1]); }
  GAP();
}
template <int H> DI void mid_wait(Ctx& c) {
  if constexpr (H >= 6 && H < 13) {
    constexpr int p = hpos(2 * (H - 6) + 1);
    wait_after<p / 32, p % 32 - 2, H, 9>(c);
    PIN(c.Hb[(2 * (H - 6) + 1) & 7][0]); PIN(c.Hb[(2 * (H - 6) + 1) & 7][1]);
  }
}
#define GST(H, i) do {} while (0)
template <int H, int K> DI void step(Ctx& c) {
  GST(H, K + 2);
  if constexpr (K == 9) mid_wait<H>(c);
  if constexpr (K == KSW) asm volatile("s_setprio 0");
  mm<H, K>(c);
  if constexpr (K + 4 < 18) rd<H, K + 4>(c);
  else if constexpr (H == 13) rd<14, K - 14>(c);
  side<H, K>(c);
  GAP();
}
template <int H, int... Ks> DI void steps(Ctx& c, std::integer_sequence<int, Ks...>) { (step<H, Ks>(c), ...); }
DI void prio_young(const Ctx& c) {
  asm volatile("s_cmp_eq_u32 %0, 0\n\ts_cbranch_scc1 .Lskip%=\n\ts_setprio 1\n.Lskip%=:" ::"s"(c.hf) : "scc");
}
template <int H> DI void half_stage(Ctx& c) {
  if constexpr (H < 13) prio_young(c);
  set_bases<H>(c);
  if constexpr (H < 14) { rd<H, 0>(c); rd<H, 1>(c); }
  GAP();
  GST(H, 0);
  if constexpr (H > 0) mm<H - 1, 16>(c); else { mfma16(c.acc[0][0], c.F[2], c.X[34]); mfma16(c.acc[0][2], c.F[2], c.X[35]); }
  rd<H, 2>(c);
  side<H, -2>(c);
  GAP();
  GST(H, 1);
  if constexpr (H > 0) mm<H - 1, 17>(c); else { mfma16(c.acc[0][1], c.F[3], c.X[34]); mfma16(c.acc[0][3], c.F[3], c.X[35]); }
  rd<H, 3>(c);
  side<H, -1>(c);
  GAP();
  steps<H>(c, std::make_integer_sequence<int, 16>{});
  if constexpr (H == 14) { mm<14, 16>(c); mm<14, 17>(c); GAP(); asm volatile("s_nop 15" ::: "memory"); GAP(); }
  GST(H, 18);
  end_hs<H>(c);
  GST(H, 20);
}

struct B0 {
  unsigned vy, vw1, vc8, xa[4], l8, l80, lx, lw;
  bf16x8 G[2];
};
template <int S> DI void issue_y(Ctx& c, B0& b) {
  const unsigned dst = (S % 5) * SLICE + c.w * 1024u, so = WS_Y + c.r * 128u * XROW + S * 128u;
  asm volatile("s_mov_b32 m0, %0\n\ts_nop 0\n\tbuffer_load_dwordx4 %1, %2, %3 offen sc1 lds" ::"s"(dst), "v"(b.vy), "s"(c.sw), "s"(so));
  asm volatile("s_mov_b32 m0, %0\n\ts_nop 0\n\tbuffer_load_dwordx4 %1, %2, %3 offen sc1 lds" ::"s"(dst + 8192u), "v"(b.vy), "s"(c.sw), "s"(so + 64u * XROW));
}
template <int S> DI void issue_w(Ctx& c, B0& b) {
  const unsigned dst = (S % 5) * SLICE, kk = S * 64u;
  dma16(dst + c.w * 1024u + 16384u, b.vw1, c.s1, (kk + 8u * c.w) * W1ROW + c.freg2);
  asm volatile("s_cmp_lg_u32 %4, 0\n\ts_cbranch_scc1 .Lskip%=\n\ts_mov_b32 m0, %0\n\ts_nop 0\n\tbuffer_load_dwordx4 %1, %2, %3 offen lds\n.Lskip%=:"
               ::"s"(dst + c.t * 1024u + 24576u), "v"(b.vc8), "s"(c.s1), "s"((kk + 16u * c.t) * W1ROW + c.f82), "s"(c.hf) : "scc");
}
template <int S, int P> DI void rdx(Ctx& c, B0& b) {
  c.X[4 * S + 2 * P] = *LDSP(bf16x8, b.xa[2 * P]);
  c.X[4 * S + 2 * P + 1] = *LDSP(bf16x8, b.xa[2 * P + 1]);
}
template <int K> DI void rdf(Ctx& c) {
  const unsigned a = ((K & 1) ? c.wb2 : c.wb) + 4096u * (K >> 1);
  c.F[K] = cat(trr(a), trr(a + 512u));
}
template <int P> DI void rdg(B0& b) { b.G[P] = cat(trr(b.l8 + 2048u * P), trr(b.l8 + 2048u * P + 256u)); }
template <int S, int K> DI void mmA(Ctx& c) {
  mfma16(c.acc[0][K & 1], c.F[K], c.X[4 * S + (K & 2)]);
  mfma16(c.acc[0][2 + (K & 1)], c.F[K], c.X[4 * S + (K & 2) + 1]);
}
template <int S, int P> DI void mmB(Ctx& c, B0& b) {
  mfma16(c.acc8[0], b.G[P], c.X[4 * S + 2 * P]);
  mfma16(c.acc8[1], b.G[P], c.X[4 * S + 2 * P + 1]);
}
template <int S> DI void bases0(Ctx& c, B0& b) {
  constexpr unsigned base = (S % 5) * SLICE;
#pragma unroll
  for (int k = 0; k < 4; ++k) b.xa[k] = opq(base + (b.lx ^ (unsigned)((k >> 1) * 64 + (k & 1) * 2048)));
  c.wb = opq(base + 16384u + b.lw);
  c.wb2 = opq(c.wb ^ 32u);
  b.l8 = opq(base + 24576u + b.l80);
}
template <int S> DI void slice(Ctx& c, B0& b) {
  constexpr int n0 = 3, n = 4;
  constexpr int N = S == 0 ? 2 : S == 1 ? 4 : S == 2 ? 4 + n0 : S == 3 ? 2 + 2 * n0 : S <= 5 ? 3 * n0 : S == 6 ? 2 * n0 : S == 7 ? n0 : n;
  asm volatile("s_waitcnt vmcnt(%0) lgkmcnt(0)\n\ts_barrier" ::"i"(N) : "memory");
  STAMP(3 + S);
  GAP();
  if constexpr (S > 0) mmA<S - 1, 2>(c);
  rdx<S, 0>(c, b); rdf<0>(c);
  GAP();
  if constexpr (S > 0) mmA<S - 1, 3>(c);
  rdf<1>(c); rdg<0>(b);
  GAP();
  if constexpr (S > 0) mmB<S - 1, 1>(c, b);
  rdx<S, 1>(c, b); rdf<2>(c);
  if constexpr (S <= 1) issue_y<2 * S + 2>(c, b);
  if constexpr (S >= 2 && S <= 4) issue_y<S + 4>(c, b);
  if constexpr (S == 7) issue_slot<0>(c);
  if constexpr (S == 8) issue_slot<1>(c);
  GAP();
  mmA<S, 0>(c);
  rdf<3>(c); rdg<1>(b);
  GAP();
  mmA<S, 1>(c);
  if constexpr (S <= 1) issue_y<2 * S + 3>(c, b);
  if constexpr (S >= 1 && S <= 4) issue_w<S + 4>(c, b);
  if constexpr (S == 5) {
    const unsigned m = c.w * 7u + (lane_id() & 7u), ln = ((c.r & 7u) * 4u + c.q) * 54u + (m < 54u ? m : 53u);
    const unsigned mem = ln / 432u, rem = ln - mem * 432u, row = rem / 9u;
    c.vtch = (288u * mem + row) * W2ROW + (rem - row * 9u) * 128u;
    c.pref = poll_off(c, false);
  }
  if constexpr (S == 6) {
    const unsigned v0 = b.vw1 ^ (c.hf << 5);
#pragma unroll
    for (int i = 0; i < 4; ++i) c.vpat[i] = (v0 ^ ((i & 1) << 5)) + i * (8u * W1ROW - 1024u);
    c.vpat4 = v0 + 32u * W1ROW;
    bias_quad<0>(c.acc[1], c.tb + 256u); bias_quad<1>(c.acc[1], c.tb + 256u); bias_quad<2>(c.acc[1], c.tb + 256u); bias_quad<3>(c.acc[1], c.tb + 256u);
  }
  if constexpr (S < 8) bases0<S + 1>(c, b);
  GAP();
  mmB<S, 0>(c, b);
  GAP();
}
DI float half_sum(float v) {
  v = dpp_add<0x121>(v); v = dpp_add<0x122>(v); v = dpp_add<0x124>(v); v = dpp_add<0x128>(v);
  auto a = __builtin_amdgcn_permlane16_swap(__float_as_uint(v), __float_as_uint(v), false, false);
  return __uint_as_float(a[0]) + __uint_as_float(a[1]);
}
template <int HF> DI void prologue(Ctx& c, B0& b, const srd_t sx, const srd_t sb1) {
  const unsigned l = threadIdx.x & 63u, g = l >> 4, qq = (l >> 2) & 3u, pp = l & 3u, h = l >> 5;
  unsigned epoch;
  b.vw1 = (l >> 3) * W1ROW + (((l & 7u) ^ ((g & 1u) << 2) ^ ((c.w & 1u) << 1)) << 4);
  b.vc8 = (l >> 2) * W1ROW + ((pp ^ (h << 1)) << 4);
  if constexpr (HF == 0) {
    const unsigned row = (c.r * 128u + (c.j * 4u + c.q) * 8u + 2u * c.t) * XROW, va = h * XROW + (l & 31u) * 16u, vb = h * XROW + 1024u + (l & 31u) * 4u;
    u32x4 xa, xc; unsigned xb;
    asm volatile("buffer_load_dwordx4 %0, %1, %2, %3 offen" : "=v"(xa) : "v"(va), "s"(sx), "s"(row));
    asm volatile("buffer_load_dwordx4 %0, %1, %2, %3 offen offset:512" : "=v"(xc) : "v"(va), "s"(sx), "s"(row));
    asm volatile("buffer_load_dword %0, %1, %2, %3 offen" : "=v"(xb) : "v"(vb), "s"(sx), "s"(row));
    asm volatile("buffer_load_dword %0, off, %1, %2 sc1" : "=v"(epoch) : "s"(c.sw), "s"(WS_EPOCH + c.r * 128u));
    GAP();
    asm volatile("s_waitcnt vmcnt(1)" : "+v"(xa), "+v"(xb), "+v"(xc)::"memory");
    float v[18];
#pragma unroll
    for (int i = 0; i < 4; ++i) {
      v[2 * i] = __uint_as_float(xa[i] << 16); v[2 * i + 1] = __uint_as_float(xa[i] & 0xffff0000u);
      v[8 + 2 * i] = __uint_as_float(xc[i] << 16); v[9 + 2 * i] = __uint_as_float(xc[i] & 0xffff0000u);
    }
    v[16] = __uint_as_float(xb << 16); v[17] = __uint_as_float(xb & 0xffff0000u);
    float s0 = v[16], s1 = v[17];
#pragma unroll
    for (int i = 0; i < 8; ++i) { s0 += v[2 * i]; s1 += v[2 * i + 1]; }
    const float mean = half_sum(s0 + s1) * (1.0f / 576.0f);
#pragma unroll
    for (int i = 0; i < 18; ++i) v[i] -= mean;
    s0 = v[16] * v[16]; s1 = v[17] * v[17];
#pragma unroll
    for (int i = 0; i < 8; ++i) { s0 = __builtin_fmaf(v[2 * i], v[2 * i], s0); s1 = __builtin_fmaf(v[2 * i + 1], v[2 * i + 1], s1); }
    const float rs = KAP * __builtin_amdgcn_rsqf(half_sum(s0 + s1) * (1.0f / 576.0f) + 1e-5f);
    u32x4 ya, yc;
#pragma unroll
    for (int i = 0; i < 4; ++i) { ya[i] = cvtpk(v[2 * i] * rs, v[2 * i + 1] * rs); yc[i] = cvtpk(v[8 + 2 * i] * rs, v[9 + 2 * i] * rs); }
    const unsigned yb = cvtpk(v[16] * rs, v[17] * rs);
    asm volatile("buffer_store_dwordx4 %0, %1, %2, %3 offen sc1" ::"v"(ya), "v"(va), "s"(c.sw), "s"(WS_Y + row));
    asm volatile("buffer_store_dwordx4 %0, %1, %2, %3 offen offset:512 sc1" ::"v"(yc), "v"(va), "s"(c.sw), "s"(WS_Y + row));
    asm volatile("buffer_store_dword %0, %1, %2, %3 offen sc1\n\ts_nop 1" ::"v"(yb), "v"(vb), "s"(c.sw), "s"(WS_Y + row));
    GAP();
    STAMP(1);
  b.vy = ((l >> 3) + 8u * c.w) * XROW + (((l & 7u) ^ ((g + 4u * c.w) & 7u)) << 4);
  b.lw = (8u * g + qq) * 128u + (((4u * HF + 2u * (g & 1u) + (pp >> 1)) ^ ((qq >> 1) << 2)) << 4) + 8u * (pp & 1u);
  const unsigned xrow = 32u * c.t + 16u * HF + (l & 15u);
  b.lx = xrow * 128u + ((g ^ ((xrow >> 1) & 7u)) << 4);
  b.l80 = (8u * g + qq) * 64u + ((HF ^ (g & 1u)) << 5) + 8u * pp;
  c.tb = L_B1 + HF * 128u + g * 16u;
  c.vhld[0] = l * 16u + HF * 1024u; c.vhld[1] = c.vhld[0] ^ 1024u;
  bases0<0>(c, b);
    GAP();
    asm volatile("s_waitcnt vmcnt(0)\n\ts_barrier" : "+v"(epoch)::"memory");
    c.tag = rfl(epoch) + 1u;
    STAMP(31);
    store_flag<1>(c);
    issue_w<0>(c, b); issue_w<1>(c, b); issue_w<2>(c, b); issue_w<3>(c, b); issue_w<4>(c, b);
  } else {
    unsigned b1a, b1b;
    {
      unsigned len;
      asm volatile("s_mov_b32 %0, mixer_code_end-mixer_code_begin-4" : "=s"(len));
      const unsigned off = (c.t * 64u + l) * 128u;
      asm volatile("s_mov_b32 m0, %0\n\ts_nop 0\n\tglobal_load_lds_dword %1, %2" ::"s"(L_JUNK), "v"(off < len ? off : len), "s"(c.pc));
    }
    asm volatile("buffer_load_dword %0, off, %1, %2 sc1" : "=v"(epoch) : "s"(c.sw), "s"(WS_EPOCH + c.r * 128u));
    asm volatile("buffer_load_ushort %0, %1, %2, %3 offen" : "=v"(b1a) : "v"((c.t * 64u + l) * 2u), "s"(sb1), "s"(c.freg2));
    asm volatile("buffer_load_ushort %0, %1, %2, %3 offen" : "=v"(b1b) : "v"((l & 31u) * 2u), "s"(sb1), "s"(c.f82));
    issue_w<0>(c, b); issue_w<1>(c, b); issue_w<2>(c, b); issue_w<3>(c, b); issue_w<4>(c, b);
    const unsigned uln = (c.r * 4u + c.q) + 64u * (c.t * 54u + (l < 54u ? l : 53u));
    STAMP(1);
  b.vy = ((l >> 3) + 8u * c.w) * XROW + (((l & 7u) ^ ((g + 4u * c.w) & 7u)) << 4);
  b.lw = (8u * g + qq) * 128u + (((4u * HF + 2u * (g & 1u) + (pp >> 1)) ^ ((qq >> 1) << 2)) << 4) + 8u * (pp & 1u);
  const unsigned xrow = 32u * c.t + 16u * HF + (l & 15u);
  b.lx = xrow * 128u + ((g ^ ((xrow >> 1) & 7u)) << 4);
  b.l80 = (8u * g + qq) * 64u + ((HF ^ (g & 1u)) << 5) + 8u * pp;
  c.tb = L_B1 + HF * 128u + g * 16u;
  c.vhld[0] = l * 16u + HF * 1024u; c.vhld[1] = c.vhld[0] ^ 1024u;
  bases0<0>(c, b);
#pragma unroll
    for (int k = 0; k < 3; ++k) {
      const unsigned i = k < 2 ? 4u * k + c.t : 8u, nn = 64u * i + l, rho = nn / 18u, chn = nn - rho * 18u;
      const unsigned rel = i < 5u ? i : i - 5u, o = chn * 16u - (rel < 4u ? rel * 1024u : 0u);
      *LDSP(unsigned, L_PAT + i * 256u + l * 4u) = rho * W2ROW + o;
      *LDSP(unsigned, L_PAT + 2304u + i * 256u + l * 4u) = ((rho & 3u) | ((rho & 12u) << 1) | ((rho & 16u) >> 2)) * W2ROW + o;
    }
    asm volatile("s_waitcnt vmcnt(%3)" : "+v"(b1a), "+v"(b1b), "+v"(epoch) : "i"(5) : "memory");
    c.tag = rfl(epoch) + 1u;
    *LDSP(float, L_B1 + (c.t * 64u + l) * 4u) = __uint_as_float(b1a << 16) * KAP;
    *LDSP(float, L_B1 + 1024u + (l & 31u) * 4u) = __uint_as_float(b1b << 16) * KAP;
    *LDSP(unsigned, L_PROG + (l & 7u) * 4u) = 0u;
    asm volatile("s_barrier" ::: "memory");
    STAMP(31);
    if (c.t < 3) {
      const unsigned rw = uln / 18u;
      asm volatile("s_mov_b32 m0, %0\n\ts_nop 0\n\tbuffer_load_dword %1, %2, %3 offen lds" ::"s"(L_JUNK),
                   "v"(rw * W1ROW + (uln - rw * 18u) * 128u), "s"(c.s1), "s"(c.j * 2304u));
    }
    if (c.w == 7) {
      const unsigned voff = WS_FLAG + 128u + ((c.r * 16u + (l & 15u)) << 10);
      __builtin_amdgcn_s_sleep(PDELAY);
      for (unsigned spin = 0; spin < (1u << 16); ++spin) {
        unsigned pv;
        asm volatile("s_sleep %3\n\tbuffer_load_dword %0, %1, %2, 0 offen sc1\n\ts_waitcnt vmcnt(0)" : "=&v"(pv) : "v"(voff), "s"(c.sw), "i"(PSLEEP) : "memory");
        if (__builtin_amdgcn_ballot_w64(pv != c.tag) == 0) break;
      }
    }
  }
}
DI void block0(Ctx& c, B0& b) {
  BARRIER_LGKM();
  STAMP(2);
  issue_y<0>(c, b); issue_y<1>(c, b);
  bias_quad<0>(c.acc[0], c.tb); bias_quad<1>(c.acc[0], c.tb); bias_quad<2>(c.acc[0], c.tb); bias_quad<3>(c.acc[0], c.tb);
  c.acc8[0] = *LDSP(volatile f32x4, c.tb + 1024u - c.hf * 64u); c.acc8[1] = *LDSP(volatile f32x4, c.tb + 1024u - c.hf * 64u);
  slice<0>(c, b); slice<1>(c, b); slice<2>(c, b); slice<3>(c, b); slice<4>(c, b);
  slice<5>(c, b); slice<6>(c, b); slice<7>(c, b); slice<8>(c, b);
  {
    const unsigned l = lane_id(), m = c.w * 21u + (l < 21u ? l : 20u);
    asm volatile("s_mov_b32 m0, %0\n\ts_nop 0\n\tbuffer_load_dword %1, %2, %3 offen lds" ::"s"(L_JUNK),
                 "v"(((c.r * 4u + c.q) + 64u * (m < 162u ? m : 161u)) * 128u), "s"(c.s2), "s"(c.j * 1152u * W2ROW));
  }
  mmB<8, 1>(c, b);
  c.wb = opq(b.lw);
  asm volatile("s_waitcnt vmcnt(%0) lgkmcnt(0)\n\ts_barrier" ::"i"(4 + ((W1PREF & 8) ? 1 : 0)) : "memory");
}

__global__ __launch_bounds__(512, 2) void mixer_mlp_kernel(const void* __restrict__ x, const void* __restrict__ w1, const void* __restrict__ b1,
                                                           const void* __restrict__ w2, const void* __restrict__ b2, void* __restrict__ out,
                                                           void* __restrict__ ws) {
  Ctx c;
  asm volatile("s_getpc_b64 %0\nmixer_code_begin:" : "=s"(c.pc));
  const unsigned blk = blockIdx.x, xg = blk & 7u, idx = blk >> 3;
  c.r = 8u * (xg >> 2) + (idx >> 2); c.j = xg & 3u; c.q = idx & 3u;
  c.w = rfl(threadIdx.x >> 6); c.t = c.w & 3u; c.hf = c.w >> 2;
  STAMP(0);
  const srd_t sx = __builtin_amdgcn_make_buffer_rsrc((void*)x, 0, S_ * D_ * 2, 0x00020000);
  c.s1 = __builtin_amdgcn_make_buffer_rsrc((void*)w1, 0, D_ * F_ * 2, 0x00020000);
  c.s2 = __builtin_amdgcn_make_buffer_rsrc((void*)w2, 0, D_ * F_ * 2, 0x00020000);
  c.sw = __builtin_amdgcn_make_buffer_rsrc(ws, 0, WS_USED, 0x00020000);
  c.sb2 = __builtin_amdgcn_make_buffer_rsrc((void*)b2, 0, D_ * 2, 0x00020000);
  const srd_t sb1 = __builtin_amdgcn_make_buffer_rsrc((void*)b1, 0, F_ * 2, 0x00020000);
  c.freg2 = (1152u * c.j + fstart(c.q, 0)) * 2u;
  c.f82 = (1152u * c.j + fstart(c.q, 8)) * 2u;
  c.fown = WS_FLAG + (((c.r * 4u + c.j) * 4u + c.q) << 10);
  c.pollv = 0u;
  c.sw1 = (72u * c.t + 40u * c.hf) * W1ROW + c.freg2;
  c.ldst = c.t * 9216u + c.hf * 5120u;
  B0 b0;
  if (c.hf == 0) prologue<0>(c, b0, sx, sb1); else prologue<1>(c, b0, sx, sb1);
  asm volatile("" : "+s"(c.sw1), "+s"(c.ldst), "+s"(c.freg2), "+s"(c.f82), "+s"(c.r), "+s"(c.w), "+s"(c.t));
  block0(c, b0);
  {
    const unsigned hb = WS_H + ((c.r * 4u + c.j) * 144u + c.t) * 2048u;
#pragma unroll
    for (int d = 0; d < 4; ++d) c.hq[d] = hb + ((c.q + d) & 3u) * 73728u + c.hf * 8192u;
#pragma unroll
    for (int e = 0; e < 2; ++e) {
      c.h8[e] = hb + ((c.q + 2u * c.hf + e) & 3u) * 73728u + 65536u;
      c.sw2[e] = (1152u * c.j + fstart((c.q + 2u * e + (c.t & 1u)) & 3u, c.t >> 1)) * W2ROW + 288u * c.q;
    }
    c.sw28 = (1152u * c.j + fstart((c.q + c.t) & 3u, 8)) * W2ROW + 288u * c.q;
  }
  asm volatile("" : "+s"(c.sw1), "+s"(c.ldst), "+s"(c.hq[0]), "+s"(c.hq[1]), "+s"(c.hq[2]), "+s"(c.hq[3]), "+s"(c.h8[0]), "+s"(c.h8[1]),
               "+s"(c.sw2[0]), "+s"(c.sw2[1]), "+s"(c.sw28));
  STAMP(12);
  half_stage<0>(c); STAMP(13);
  half_stage<1>(c); STAMP(14);
  half_stage<2>(c); STAMP(15);
  half_stage<3>(c); STAMP(16);
  half_stage<4>(c); STAMP(17);
  half_stage<5>(c); STAMP(18);
  half_stage<6>(c); STAMP(19);
  half_stage<7>(c); STAMP(20);
  half_stage<8>(c); STAMP(21);
  half_stage<9>(c); STAMP(22);
  half_stage<10>(c); STAMP(23);
  half_stage<11>(c); STAMP(24);
  half_stage<12>(c); STAMP(25);
  half_stage<13>(c); STAMP(26);
  half_stage<14>(c); STAMP(27);

  const unsigned l = lane_id();
  comb<2, 6>(c);
  comb<4, 0>(c); comb<4, 1>(c);
  comb<3, 2>(c); comb<3, 3>(c); comb<3, 4>(c); comb<3, 5>(c); comb<3, 6>(c);
  comb<4, 2>(c); comb<4, 3>(c); comb<4, 4>(c); comb<4, 5>(c); comb<4, 6>(c);
  BARRIER_LGKM();
  STAMP(28);
  {
    const u32x2 bA = c.b2v[0], bB = c.b2v[1], bC = c.b2v[2];
    const unsigned sp = WS_P + ((c.r * 4u + c.q) * 4u + c.j) * 4u * 9216u + c.hf * 4608u;
    const unsigned s0 = sp + ((c.j + 1u) & 3u) * 9216u, s1 = sp + ((c.j + 2u) & 3u) * 9216u, s2 = sp + ((c.j + 3u) & 3u) * 9216u;
    const unsigned va = l * 16u + c.t * 1024u, vb = 4096u + l * 8u, oa = (c.hf ^ 1u) * 3u * SLOT + c.j * 9216u + l * 16u;
    u32x4 pa[3]; u32x2 pb[3];
    f32x4 o0 = ldsf4(oa + c.t * 2048u), o1 = ldsf4(oa + c.t * 2048u + 1024u), o2 = ldsf4(oa + 8192u);
    if (c.t == 3) __builtin_amdgcn_s_sleep(TDELAY);
    for (unsigned spin = 0; spin < (1u << 16); ++spin) {
      asm volatile("buffer_load_dwordx4 %0, %6, %8, %9 offen sc1\n\tbuffer_load_dwordx4 %1, %6, %8, %10 offen sc1\n\t"
                   "buffer_load_dwordx4 %2, %6, %8, %11 offen sc1\n\t"
                   "s_cmp_lg_u32 %12, 3\n\ts_cbranch_scc1 .Lskip%=\n\t"
                   "buffer_load_dwordx2 %3, %7, %8, %9 offen sc1\n\tbuffer_load_dwordx2 %4, %7, %8, %10 offen sc1\n\t"
                   "buffer_load_dwordx2 %5, %7, %8, %11 offen sc1\n.Lskip%=:\n\ts_waitcnt vmcnt(0)"
                   : "=&v"(pa[0]), "=&v"(pa[1]), "=&v"(pa[2]), "=&v"(pb[0]), "=&v"(pb[1]), "=&v"(pb[2])
                   : "v"(va), "v"(vb), "s"(c.sw), "s"(s0), "s"(s1), "s"(s2), "s"(c.t) : "memory", "scc");
      unsigned mx = 0;
#pragma unroll
      for (int k = 0; k < 3; ++k) {
#pragma unroll
        for (int i = 0; i < 4; ++i) mx = mx > pa[k][i] ? mx : pa[k][i];
        if (c.t == 3) { mx = mx > pb[k][0] ? mx : pb[k][0]; mx = mx > pb[k][1] ? mx : pb[k][1]; }
      }
      if (!__any(mx == ~0u)) break;
      __builtin_amdgcn_s_sleep(2);
    }
    STAMP(29);
#pragma unroll
    for (int k = 0; k < 3; ++k)
#pragma unroll
      for (int i = 0; i < 2; ++i) {
        o0[2 * i] += __uint_as_float(pa[k][i] << 16); o0[2 * i + 1] += __uint_as_float(pa[k][i] & 0xffff0000u);
        o1[2 * i] += __uint_as_float(pa[k][2 + i] << 16); o1[2 * i + 1] += __uint_as_float(pa[k][2 + i] & 0xffff0000u);
      }
    unsigned short* op = (unsigned short*)out + (size_t)(128u * c.r + 32u * c.j + 16u * c.hf + (l & 15u)) * D_ + 144u * c.q;
    const unsigned e0 = cvtpk(__builtin_fmaf(o0[0], RKAP, __uint_as_float(bA[0] << 16)), __builtin_fmaf(o0[1], RKAP, __uint_as_float(bA[0] & 0xffff0000u)));
    const unsigned e1 = cvtpk(__builtin_fmaf(o0[2], RKAP, __uint_as_float(bA[1] << 16)), __builtin_fmaf(o0[3], RKAP, __uint_as_float(bA[1] & 0xffff0000u)));
    const unsigned f0 = cvtpk(__builtin_fmaf(o1[0], RKAP, __uint_as_float(bB[0] << 16)), __builtin_fmaf(o1[1], RKAP, __uint_as_float(bB[0] & 0xffff0000u)));
    const unsigned f1 = cvtpk(__builtin_fmaf(o1[2], RKAP, __uint_as_float(bB[1] << 16)), __builtin_fmaf(o1[3], RKAP, __uint_as_float(bB[1] & 0xffff0000u)));
    auto r0 = __builtin_amdgcn_permlane16_swap(e0, f0, false, false);
    auto r1 = __builtin_amdgcn_permlane16_swap(e1, f1, false, false);
    *(u32x4*)(op + 32u * c.t + 16u * ((l >> 4) & 1u) + 8u * (l >> 5)) = (u32x4){r0[0], r1[0], r0[1], r1[1]};
    if (c.t == 3) {
#pragma unroll
      for (int k = 0; k < 3; ++k)
#pragma unroll
        for (int i = 0; i < 2; ++i) { o2[2 * i] += __uint_as_float(pb[k][i] << 16); o2[2 * i + 1] += __uint_as_float(pb[k][i] & 0xffff0000u); }
      *(u32x2*)(op + 128u + 4u * (l >> 4)) =
          (u32x2){cvtpk(__builtin_fmaf(o2[0], RKAP, __uint_as_float(bC[0] << 16)), __builtin_fmaf(o2[1], RKAP, __uint_as_float(bC[0] & 0xffff0000u))),
                  cvtpk(__builtin_fmaf(o2[2], RKAP, __uint_as_float(bC[1] << 16)), __builtin_fmaf(o2[3], RKAP, __uint_as_float(bC[1] & 0xffff0000u)))};
    }
    asm volatile("mixer_code_end:");
    if (c.w == 0 && c.j == 0 && c.q == 0 && l == 0)
      asm volatile("buffer_store_dword %0, off, %1, %2 sc1" ::"v"(c.tag), "s"(c.sw), "s"(WS_EPOCH + c.r * 128u));
  }
}

extern "C" void kernel_launch(void* const* d_in, const int* in_sizes, int n_in, void* d_out, int out_size, void* d_ws, size_t ws_size,
                              hipStream_t stream) {
  (void)in_sizes; (void)n_in; (void)out_size; (void)ws_size;
  (void)hipFuncSetAttribute((const void*)mixer_mlp_kernel, hipFuncAttributeMaxDynamicSharedMemorySize, LDS_BYTES);
  mixer_mlp_kernel<<<256, 512, LDS_BYTES, stream>>>(d_in[0], d_in[1], d_in[2], d_in[3], d_in[4], d_out, d_ws);
}
```

```cpp
#include <hip/hip_runtime.h>
#include <stdint.h>
#include <utility>

typedef __bf16 bf16x8 __attribute__((ext_vector_type(8)));
typedef __bf16 bf16x2 __attribute__((ext_vector_type(2)));
typedef short s16x4 __attribute__((ext_vector_type(4)));
typedef float f32x16 __attribute__((ext_vector_type(16)));
typedef float f32x4 __attribute__((ext_vector_type(4)));
typedef float f32x2 __attribute__((ext_vector_type(2)));
typedef unsigned u32x4 __attribute__((ext_vector_type(4)));
typedef unsigned u32x2 __attribute__((ext_vector_type(2)));
typedef __amdgpu_buffer_rsrc_t srd_t;
#define DI __device__ __forceinline__
#define LAS __attribute__((address_space(3)))
#define LDSP(T, a) ((LAS T*)(size_t)(a))

constexpr unsigned S_ = 2048, D_ = 576, F_ = 4608;
constexpr unsigned XROW = D_ * 2, W1ROW = F_ * 2, W2ROW = D_ * 2;
constexpr unsigned SLOT = 36864, RING = 4 * SLOT, SLICE = 28672;
constexpr unsigned L_B1 = RING, L_PAT = L_B1 + 1152, L_JUNK = L_PAT + 4608, L_PROG = L_JUNK + 256, LDS_BYTES = L_PROG + 64;
static_assert(LDS_BYTES <= 163840, "LDS");
constexpr unsigned WS_EPOCH = 0, WS_FLAG = 4096, WS_Y = WS_FLAG + 256 * 8 * 128, WS_H = WS_Y + S_ * D_ * 2, H_BYTES = S_ * F_ * 2;
constexpr unsigned WS_P = WS_H + H_BYTES, P_BYTES = 16 * 4 * 4 * 4 * 9216;
constexpr unsigned WS_USED = WS_P + P_BYTES;
constexpr float KAP = 0.4686686930532063f, GAM = 4.91222953926424f, RKAP = 2.1337034344781243f;
#define KSW 10
#define PSLEEP 1
#define TOUCH 1
#define PREFH 1
#define HK2 2
#define CPREF 1
#define W1PREF 24
#define PDELAY 10
#define TDELAY 8
#define EARLYMARK 0
#define BPRIO 0
#define HSCHED 1
#define HDELAY 0
#define PREF 1
#define STAMP(i) do {} while (0)

DI unsigned rfl(unsigned x) { return __builtin_amdgcn_readfirstlane(x); }
DI s16x4 trr(unsigned a) { return __builtin_amdgcn_ds_read_tr16_b64_v4i16(LDSP(s16x4, a)); }
DI bf16x8 cat(s16x4 lo, s16x4 hi) { return __builtin_bit_cast(bf16x8, __builtin_shufflevector(lo, hi, 0, 1, 2, 3, 4, 5, 6, 7)); }
DI f32x4 ldsf4(unsigned a) { return *LDSP(f32x4, a); }
DI unsigned cvtpk(float lo, float hi) { f32x2 v = {lo, hi}; return __builtin_bit_cast(unsigned, __builtin_convertvector(v, bf16x2)); }
template <int CTRL> DI float dpp_add(float v) {
  return v + __builtin_bit_cast(float, __builtin_amdgcn_update_dpp(0, __builtin_bit_cast(int, v), CTRL, 0xf, 0xf, false));
}
DI void mfma16(f32x4& acc, bf16x8 a, bf16x8 b) { asm volatile("v_mfma_f32_16x16x32_bf16 %0, %1, %2, %0" : "+v"(acc) : "v"(a), "v"(b)); }
DI void mfma16z(f32x4& acc, bf16x8 a, bf16x8 b) { asm volatile("v_mfma_f32_16x16x32_bf16 %0, %1, %2, 0" : "=&v"(acc) : "v"(a), "v"(b)); }
#define BARRIER_LGKM() asm volatile("s_waitcnt lgkmcnt(0)\n\ts_barrier" ::: "memory")
#define GAP() __builtin_amdgcn_sched_barrier(0)
#define PIN(x) asm volatile("" : "+v"(x))

struct Ctx {
  srd_t s1, s2, sw;
  unsigned long long pc;
  unsigned w, t, hf, r, j, q;
  unsigned freg2, f82;
  unsigned tag, sdma, sw1, ldst, hq[4], h8[2], sw2[2], sw28, fown;
  u32x4 vpat;
  unsigned vpat4;
  unsigned wb, wb2;
  unsigned vhld[2], tb, pref, vtch, xw, xr, pg, sp, vpg;
  u32x4 pk;
  u32x2 b2v[3];
  f32x4 xg[2];
  srd_t sb2;
  float gz[2], gm, gz8, gm8;
  unsigned own[4][8], hv8[8];
  bf16x8 X[36];
  bf16x8 F[4];
  f32x4 acc[2][4];
  f32x4 acc8[2];
  f32x4 acc2[2][9];
  u32x4 Hb[8][2];
  unsigned pollv;
};
DI unsigned lane_id() {
  unsigned l;
  asm volatile("v_mbcnt_lo_u32_b32 %0, -1, 0\n\tv_mbcnt_hi_u32_b32 %0, -1, %0" : "=v"(l));
  return l;
}
DI unsigned opq(unsigned v) { PIN(v); return v; }

constexpr int pair_of(int h) { return h < 8 ? 0 : (h - 7) / 2; }
constexpr int ph_of(int h) { return h < 8 ? h - 6 : (h - 7) % 2; }
constexpr bool own_unit(int n) { return n == 0 || n == 6 || n == 10 || n == 14; }
constexpr int hload_at(int h, int k) {
  if (h == 5) return k == 2 ? 2 : k == 5 ? 3 : k == 10 ? 4 : k == 12 ? 5 : -1;
  if (h == 6) return k == 0 ? 6 : k == 2 ? 7 : k == 10 ? 8 : k == 12 ? 9 : -1;
  if (h == 7) return k == 2 ? 10 : k == 10 ? 11 : -1;
  if (h == 8) return k == 0 ? 14 : k == 2 ? 15 : k == 12 ? 16 : -1;
  if (h == 9) return k == 0 ? 17 : k == 2 ? 18 : k == 10 ? 19 : -1;
  if (h == 10) return k == 0 ? 22 : k == 2 ? 23 : k == 10 ? 24 : k == 12 ? 25 : -1;
  if (h == 11) return k == 0 ? 30 : k == 2 ? 31 : k == 10 ? 26 : k == 12 ? 27 : -1;
  if (h == 12) return k == -2 ? 32 : k == 0 ? 33 : k == 2 ? 34 : k == 4 ? 35 : -1;
  return -1;
}
constexpr bool b2_at(int h, int k) { return h == 12 && k == 6; }
constexpr int dma_at(int h, int k) {
  if (h == 0) return (k < 16 && (k & 1) == 0) ? (k < 8 ? 2 * 8 + (k + 2) / 2 : 3 * 8 + (k - 8) / 2) : (k == 15 ? 3 * 8 + 4 : -1);
  if (h + 3 > 14) return -1;
  const int i = h < 4 ? (k == -2 ? 0 : k == 2 ? 1 : k == 6 ? 2 : k == 10 ? 3 : k == 13 ? 4 : -1)
                      : (k == -2 ? 0 : k == 4 ? 1 : k == 6 ? 2 : k == 8 ? 3 : k == 14 ? 4 : -1);
  return i < 0 ? -1 : (h + 3) * 8 + i;
}
constexpr int poll_k(int h) { return h == 4 ? 5 : h == 10 ? 5 : (h == 7 || h == 9 || h == 11) ? -1 : -9; }
constexpr int poll_ev(int h) { return h == 4 ? 2 : h == 7 ? 3 : h == 10 ? 5 : 4; }
constexpr bool poll_at(int h, int k) { return k == poll_k(h); }
constexpr int flag_at(int h) { return (h == 3 || h == 5 || h == 7 || h == 9) ? (h + 1) / 2 : 0; }
constexpr bool hst_at(int h, int k) { return (h < 8 && (h & 1) && (k == 0 || k == 15)) || (h == 1 && k == 12); }
constexpr bool pref_at(int h, int k) { return PREF && h < 6 && k == 15; }
constexpr bool touch_at(int h, int k) { return TOUCH && (h == 2 || h == 4 || h == 6 || h == 8) && k == 13; }
constexpr bool mark_at(int h, int k) { return !EARLYMARK && ((h == 2 && (k == 4 || k == 12)) || (h == 4 && k == 2)); }
constexpr int nops(int h, int k, int n) {
  return (b2_at(h, k) ? 3 : 0) + (hload_at(h, k) >= 0 ? 1 : 0) + (hst_at(h, k) ? 1 : 0) + (mark_at(h, k) ? 1 : 0) +
         (dma_at(h, k) >= 0 && (dma_at(h, k) & 7) < n ? 1 : 0);
}
constexpr int between(int h0, int k0, int h1, int k1, int n) {
  int s = 0;
  for (int h = h0; h <= h1; ++h)
    for (int k = -2; k < 16; ++k)
      if ((h > h0 || k > k0) && (h < h1 || k < k1)) s += nops(h, k, n);
  for (int h = h0; h <= h1; ++h)
    if (pref_at(h, 15) && (h < h1 || k1 == 16)) s += 1;
  return s;
}
constexpr int cmin(int a, int b) { return a < b ? a : b; }

DI unsigned fstart(unsigned qq, unsigned ch) { return 32u * (9u * qq + ch + (qq & 1u) * (1u - 9u * (ch >> 3))); }

DI void dma16(unsigned m0v, unsigned voff, srd_t srd, unsigned soff) {
  asm volatile("s_mov_b32 m0, %0\n\ts_nop 0\n\tbuffer_load_dwordx4 %1, %2, %3 offen lds" ::"s"(m0v), "v"(voff), "s"(srd), "s"(soff));
}
template <int HS, int I> DI void issue_piece(Ctx& c) {
  const srd_t sd = HS < 6 ? c.s1 : c.s2;
  if constexpr (I == 0) {
    if constexpr (HS < 6) c.sdma = c.sw1 + ((HS & 1) * 288u * W1ROW + ((HS >> 1) + 1) * 128u);
    else c.sdma = HS == 8 ? c.sw28 : c.sw2[ph_of(HS)] + pair_of(HS) * 64u * W2ROW;
    dma16((HS & 3) * SLOT + c.ldst, c.vpat[0], sd, c.sdma);
  } else if constexpr (I < 4) {
    asm volatile("buffer_load_dwordx4 %0, %1, %2 offen offset:%3 lds" ::"v"(c.vpat[I]), "s"(sd), "s"(c.sdma), "i"(1024 * I));
  } else {
    asm volatile("s_cmp_lg_u32 %4, 0\n\ts_cbranch_scc1 .Lskip%=\n\ts_mov_b32 m0, %0\n\ts_nop 0\n\tbuffer_load_dwordx4 %1, %2, %3 offen lds\n.Lskip%=:"
                 ::"s"((HS & 3) * SLOT + 4096u + c.ldst), "v"(c.vpat4), "s"(sd), "s"(c.sdma), "s"(c.hf) : "scc");
  }
}
template <int HS> DI void issue_slot(Ctx& c) {
  issue_piece<HS, 0>(c); issue_piece<HS, 1>(c); issue_piece<HS, 2>(c); issue_piece<HS, 3>(c); issue_piece<HS, 4>(c);
}
template <int N, int U> DI void issue_hload(Ctx& c) {
  constexpr int h = 6 + N / 2;
  const unsigned so = h == 8 ? c.h8[N & 1] : c.hq[2 * ph_of(h) + (N & 1)] + pair_of(h) * 16384u;
  asm volatile("buffer_load_dwordx4 %0, %1, %2, %3 offen sc1" : "=v"(c.Hb[N & 7][U]) : "v"(c.vhld[U]), "s"(c.sw), "s"(so));
}
DI unsigned poll_off(const Ctx& c, bool dest) {
  const unsigned l = lane_id(), dp = (l & 3u) ? (l & 3u) : 1u;
  return WS_FLAG + ((dest ? (c.r * 4u + ((c.j + dp) & 3u)) * 4u + c.q : (c.r * 4u + c.j) * 4u + ((c.q + dp) & 3u)) << 10);
}
template <int EV> DI void store_flag(Ctx& c) {
  const u32x4 v = {c.tag, c.tag, c.tag, c.tag};
  asm volatile("s_cmp_lg_u32 %4, 0\n\ts_cbranch_scc1 .Lskip%=\n\ts_mov_b64 exec, 0xff\n\t"
               "buffer_store_dwordx4 %0, %1, %2, %3 offen offset:%5 sc1\n\ts_mov_b64 exec, -1\n.Lskip%=:\n\ts_nop 0"
               ::"v"(v), "v"(c.vhld[0]), "s"(c.sw), "s"(c.fown), "s"(c.w), "i"(EV * 128) : "scc");
}
template <int EV> DI void issue_poll(Ctx& c, unsigned& pv, unsigned voff) {
  asm volatile("s_cmp_lg_u32 %3, 7\n\ts_cbranch_scc1 .Lskip%=\n\tbuffer_load_dword %0, %1, %2, 0 offen offset:%4 sc1\n.Lskip%=:"
               : "+v"(pv) : "v"(voff), "s"(c.sw), "s"(c.w), "i"(EV * 128) : "scc");
}
template <int EV, int N> DI void check_poll(Ctx& c, unsigned& pv, bool dest) {
  if (c.w == 7) {
    asm volatile("s_waitcnt vmcnt(%1)" : "+v"(pv) : "i"(N) : "memory");
    if (__builtin_expect(__builtin_amdgcn_ballot_w64(pv != c.tag) != 0, 0)) {
      const unsigned voff = poll_off(c, dest);
      for (unsigned spin = 0; spin < (1u << 16); ++spin) {
        unsigned v;
        asm volatile("s_sleep 1\n\tbuffer_load_dword %0, %1, %2, 0 offen offset:%3 sc1\n\ts_waitcnt vmcnt(0)" : "=&v"(v) : "v"(voff), "s"(c.sw), "i"(EV * 128) : "memory");
        if (__builtin_amdgcn_ballot_w64(v != c.tag) == 0) break;
      }
    }
  }
}
template <int P> DI void store_h(Ctx& c, const unsigned (&hv)[8], unsigned vo, unsigned so) {
  const u32x4 v = {hv[4 * P], hv[4 * P + 1], hv[4 * P + 2], hv[4 * P + 3]};
  asm volatile("buffer_store_dwordx4 %0, %1, %2, %3 offen sc1\n\ts_nop 1" ::"v"(v), "v"(vo), "s"(c.sw), "s"(so));
}
template <int N0, int N1> DI void wait_vm(const Ctx&) {
  static_assert(N1 <= N0 && N1 >= 0 && N0 < 64, "vmcnt");
  asm volatile("s_waitcnt vmcnt(%0)" ::"i"(N1) : "memory");
}

template <int H> DI void set_bases(Ctx& c) {
  constexpr unsigned d = (H & 3) ? SLOT : 0u - 3u * SLOT;
  if constexpr (H == 6) {
    const unsigned l = lane_id();
    c.wb = opq(2u * SLOT + (l >> 2) * 288u + 8u * (l & 3u) + c.hf * 18432u);
  } else if constexpr (H > 0 && H < 14) c.wb = opq(c.wb + d);
  if constexpr (H < 6) c.wb2 = opq(c.wb ^ 32u);
}
template <int H, int S> DI void rd(Ctx& c) {
  constexpr int tau = 18 * (H - 13) + S;
  constexpr unsigned o = H < 6 ? 4096u * (S >> 1) : H < 13 ? 9216u * (S / 9) + 32u * (S % 9) : ((tau >> 1) & 1) * SLOT + (tau & 1) * 9216u + 32u * (tau >> 2);
  constexpr unsigned e = H < 6 ? 512u : 4608u;
  const unsigned b = (H < 6 && (S & 1)) ? c.wb2 : c.wb;
  c.F[(H * 18 + S) & 3] = cat(trr(b + o), trr(b + o + e));
}
template <int H, int S> DI void mm(Ctx& c) {
  const bf16x8 a = c.F[(H * 18 + S) & 3];
  if constexpr (H < 6) {
    constexpr int B = ((H >> 1) + 1) & 1;
    constexpr int P = 9 * (H & 1) + (S >> 1);
    mfma16(c.acc[B][S & 1], a, c.X[2 * P]);
    mfma16(c.acc[B][2 + (S & 1)], a, c.X[2 * P + 1]);
  } else {
    constexpr int tau = 18 * (H - 13) + S;
    constexpr int N = H < 13 ? 2 * (H - 6) + S / 9 : 14 + (tau & 3), dt = H < 13 ? S % 9 : tau >> 2;
    u32x4 b0 = c.Hb[N & 7][0], b1 = c.Hb[N & 7][1];
    if constexpr (own_unit(N)) {
      const unsigned(&o)[8] = c.own[pair_of(6 + N / 2)];
      b0 = (u32x4){o[0], o[1], o[2], o[3]}; b1 = (u32x4){o[4], o[5], o[6], o[7]};
    }
    if constexpr (N == 0) {
      mfma16z(c.acc2[0][dt], a, __builtin_bit_cast(bf16x8, b0));
      mfma16z(c.acc2[1][dt], a, __builtin_bit_cast(bf16x8, b1));
    } else {
      mfma16(c.acc2[0][dt], a, __builtin_bit_cast(bf16x8, b0));
      mfma16(c.acc2[1][dt], a, __builtin_bit_cast(bf16x8, b1));
    }
  }
}
template <int Q> DI void bias_quad(f32x4 (&acc)[4], unsigned a) { acc[Q] = *LDSP(volatile f32x4, a + (Q & 1) * 64u); }
template <int E, int ST> DI void gelu_stage(float s, float& z, float& m0, unsigned (&hv)[8]) {
  if constexpr (ST == 0) { z = s * __builtin_fmaf(s, s, GAM); PIN(z); }
  if constexpr (ST == 1) { z = __builtin_amdgcn_exp2f(-z); PIN(z); }
  if constexpr (ST == 2) { z = __builtin_amdgcn_rcpf(1.0f + z); PIN(z); }
  if constexpr (ST == 3) {
    if constexpr ((E & 1) == 0) { m0 = s * z; PIN(m0); }
    else { float m = s * z, mm0 = m0; PIN(m); PIN(mm0); hv[E >> 1] = cvtpk(mm0, m); PIN(hv[E >> 1]); }
  }
}
constexpr int gstart(int e) { return 1 + (e * 29) / 15; }
constexpr int gelem(int g, int st) {
  for (int e = 0; e < 16; ++e) if (gstart(e) + st == g) return e;
  return -1;
}
template <int B, int G, int ST> DI void gelu_part(Ctx& c) {
  constexpr int e = gelem(G, ST);
  if constexpr (e >= 0) gelu_stage<e, ST>(c.acc[B & 1][e >> 2][e & 3], c.gz[e & 1], c.gm, c.own[B]);
}
template <int B, int G> DI void gelu_gap(Ctx& c) {
  gelu_part<B, G, 3>(c); gelu_part<B, G, 2>(c); gelu_part<B, G, 1>(c); gelu_part<B, G, 0>(c);
  if constexpr (B < 2) {
    if constexpr (G == 27) { bias_quad<0>(c.acc[B & 1], c.tb + (B + 2) * 256u); bias_quad<1>(c.acc[B & 1], c.tb + (B + 2) * 256u); }
    if constexpr (G == 31) bias_quad<2>(c.acc[B & 1], c.tb + (B + 2) * 256u);
    if constexpr (G == 33) bias_quad<3>(c.acc[B & 1], c.tb + (B + 2) * 256u);
  }
}

template <int H0, int K0, int H1, int K1> DI void wait_after(const Ctx& c) {
  wait_vm<between(H0, K0, H1, K1, 5), between(H0, K0, H1, K1, 4)>(c);
}
DI unsigned nosent(unsigned v) { return v < 0xfffffffeu ? v : 0xfffffffeu; }
template <int M, int ST> DI void comb(Ctx& c) {
  constexpr int NT = M < 4 ? 2 : 1;
  if constexpr (ST == 0) *LDSP(f32x4, c.xw + 2 * M * 1024u) = c.acc2[1][2 * M];
  if constexpr (ST == 1) {
    if constexpr (NT == 2) *LDSP(f32x4, c.xw + (2 * M + 1) * 1024u) = c.acc2[1][2 * M + 1];
    asm volatile("s_mov_b64 exec, 1\n\tds_write_b32 %0, %1\n\ts_mov_b64 exec, -1" ::"v"(c.vpg), "v"(M + 1u) : "memory");
  }
  if constexpr (ST == 2) c.pg = *LDSP(volatile unsigned, c.vpg ^ 16u);
  if constexpr (ST == 3) {
    if (__builtin_expect(rfl(c.pg) <= (unsigned)M, 0)) {
      for (unsigned spin = 0; spin < (1u << 20); ++spin)
        if (rfl(*LDSP(volatile unsigned, c.vpg ^ 16u)) > (unsigned)M) break;
    }
    asm volatile("" ::: "memory");
#pragma unroll
    for (int i = 0; i < NT; ++i) c.xg[i] = ldsf4(c.xr + (2 * M + i) * 1024u);
  }
  if constexpr (ST == 4) {
#pragma unroll
    for (int i = 0; i < NT; ++i)
#pragma unroll
      for (int k = 0; k < 4; ++k) { c.xg[i][k] += c.acc2[0][2 * M + i][k]; PIN(c.xg[i][k]); }
  }
  if constexpr (ST == 5) {
#pragma unroll
    for (int i = 0; i < 2 * NT; ++i) { c.pk[i] = nosent(cvtpk(c.xg[i >> 1][2 * (i & 1)], c.xg[i >> 1][2 * (i & 1) + 1])); }
    PIN(c.pk);
  }
  if constexpr (ST == 6) {
    if (c.t != c.j) {
      if constexpr (NT == 2)
        asm volatile("buffer_store_dwordx4 %0, %1, %2, %3 offen offset:%4 sc1\n\ts_nop 1" ::"v"(c.pk), "v"(c.vtch), "s"(c.sw), "s"(c.sp), "i"(M * 1024));
      else
        asm volatile("buffer_store_dwordx2 %0, %1, %2, %3 offen sc1" ::"v"((u32x2){c.pk[0], c.pk[1]}), "v"(c.vtch >> 1), "s"(c.sw), "s"(c.sp + 4096u));
    } else {
#pragma unroll
      for (int i = 0; i < NT; ++i) *LDSP(f32x4, c.xr + (2 * M + i) * 1024u) = c.xg[i];
    }
  }
}
template <int TAU, int ST, int OFF> DI void comb_at(Ctx& c) {
  if constexpr (TAU >= OFF && (TAU - OFF) % 8 == 0 && (TAU - OFF) / 8 < 4) comb<(TAU - OFF) / 8, ST>(c);
}
template <int H, int K> DI void side(Ctx& c) {
  if constexpr (poll_at(H, K)) {
    if constexpr (H == 11) issue_poll<4>(c, c.pollv, poll_off(c, true));
    else issue_poll<poll_ev(H)>(c, c.pollv, c.pref);
  }
  if constexpr (K == -1 && flag_at(H)) store_flag<flag_at(H)>(c);
  if constexpr (hload_at(H, K) >= 0) issue_hload<(hload_at(H, K) >> 1), (hload_at(H, K) & 1)>(c);
  if constexpr (mark_at(H, K)) {
    constexpr unsigned k = H == 4 ? 2u : K == 4 ? 0u : 1u;
    const u32x4 ones = {~0u, ~0u, ~0u, ~0u};
    const unsigned so = WS_P + (((c.r * 4u + c.q) * 4u + c.j) * 4u + ((c.j + 1u + k) & 3u)) * 9216u, vo = lane_id() * 16u;
    asm volatile("buffer_store_dwordx4 %0, %1, %2, %3 offen sc1\n\ts_cmp_lg_u32 %4, %5\n\ts_cbranch_scc1 .Lskip%=\n\t"
                 "buffer_store_dwordx4 %0, %1, %2, %6 offen sc1\n.Lskip%=:\n\ts_nop 1"
                 ::"v"(ones), "v"(vo), "s"(c.sw), "s"(so + c.w * 1024u), "s"(c.w), "i"(k), "s"(so + 8192u) : "scc");
  }
  if constexpr (dma_at(H, K) >= 0) issue_piece<(dma_at(H, K) >> 3), (dma_at(H, K) & 7)>(c);
  if constexpr ((H == 2 && K == 14) || ((H == 4 || H == 5) && K == 15)) {
    const unsigned a = L_PAT + (H == 4 ? 2304u : 0u) + lane_id() * 4u;
#pragma unroll
    for (int i = 0; i < 4; ++i) c.vpat[i] = *LDSP(unsigned, a + (c.hf * 5u + i) * 256u);
    c.vpat4 = *LDSP(unsigned, a + 1024u);
  }
  if constexpr (b2_at(H, K)) {
    const unsigned l = lane_id();
#pragma unroll
    for (int i = 0; i < 3; ++i)
      asm volatile("buffer_load_dwordx2 %0, %1, %2, %3 offen" : "=v"(c.b2v[i]) : "v"((l >> 4) * 8u), "s"(c.sb2), "s"(288u * c.q + (i < 2 ? 64u * c.t + 32u * i : 256u)));
    c.xw = c.hf * 3u * SLOT + c.t * 9216u + l * 16u;
    c.xr = (c.hf ^ 1u) * 3u * SLOT + c.t * 9216u + l * 16u;
    c.sp = WS_P + (((c.r * 4u + c.q) * 4u + c.t) * 4u + c.j) * 9216u + c.hf * 4608u;
    c.vpg = L_PROG + c.w * 4u; c.vtch = l * 16u;
    asm volatile("" : "+v"(c.xw), "+v"(c.xr), "+v"(c.vpg), "+v"(c.vtch), "+s"(c.sp));
  }
  if constexpr (H >= 13) {
    constexpr int tau = 18 * (H - 13) + K;
    comb_at<tau, 6, 17>(c); comb_at<tau, 5, 16>(c); comb_at<tau, 4, 15>(c); comb_at<tau, 3, 13>(c);
    comb_at<tau, 2, 12>(c); comb_at<tau, 1, 9>(c); comb_at<tau, 0, 6>(c);
  }
  if constexpr (H < 8) gelu_gap<(H >> 1), K + 18 * (H & 1)>(c);
  if constexpr (H < 2) {
    constexpr int G = K + 18 * H - 1;
    if constexpr (G >= 0 && G < 28) gelu_stage<G / 4, (G & 3)>(c.acc8[G / 16][(G / 4) & 3], c.gz8, c.gm8, c.hv8);
    if constexpr (G == 28) { gelu_stage<7, 0>(c.acc8[1][3], c.gz8, c.gm8, c.hv8); gelu_stage<7, 1>(c.acc8[1][3], c.gz8, c.gm8, c.hv8); }
    if constexpr (G == 29) { gelu_stage<7, 2>(c.acc8[1][3], c.gz8, c.gm8, c.hv8); gelu_stage<7, 3>(c.acc8[1][3], c.gz8, c.gm8, c.hv8); }
  }
  if constexpr (hst_at(H, K)) {
    if constexpr (K == 12) {
      const unsigned l = lane_id();
#pragma unroll
      for (int i = 0; i < 2; ++i) {
        auto rr = __builtin_amdgcn_permlane16_swap(c.hv8[i], c.hv8[2 + i], false, false);
        c.hv8[i] = rr[0]; c.hv8[2 + i] = rr[1];
      }
      store_h<0>(c, c.hv8, (((l >> 4) & 1u) ^ c.hf) * 1024u + ((l & 15u) + 16u * (l >> 5)) * 16u, c.hq[0] + 65536u - c.hf * 7680u);
    } else store_h<(K == 0 ? 0 : 1)>(c, c.own[H >> 1], c.vhld[K == 0 ? 0 : 1], c.hq[0] + (H >> 1) * 16384u);
  }
  if constexpr (pref_at(H, K))
    asm volatile("s_mov_b32 m0, %0\n\ts_nop 0\n\tbuffer_load_dword %1, %2, %3 offen lds" ::"s"(L_JUNK), "v"(c.vtch), "s"(c.s2), "s"((c.j * 1152u + H * 48u) * W2ROW));
}
constexpr int hpos(int n) {
  for (int h = 4; h < 14; ++h) for (int k = -2; k < 16; ++k) if (hload_at(h, k) == 2 * n + 1) return h * 32 + k + 2;
  return 0;
}
constexpr int cmax(int a, int b) { return a > b ? a : b; }
template <int H> DI void end_hs(Ctx& c) {
  if constexpr (H < 13) {
    constexpr int p0 = H == 0 ? -1 : H == 1 ? 8 : H == 12 ? 12 * 32 + 8 : (H - 2) * 32 + 17;
    constexpr int p1 = (H >= 5 && !own_unit(2 * (H - 5))) ? hpos(2 * (H - 5)) : 0, p2 = 0, p3 = flag_at(H + 1) ? (H - 1) * 32 + 17 : 0;
    constexpr int p = H == 0 ? -1 : cmax(cmax(p0, p1), cmax(p2, p3));
    if constexpr (H == 0) wait_vm<between(0, -3, 0, 16, 4) + ((W1PREF & 8) ? 1 : 0), between(0, -3, 0, 16, 4) + ((W1PREF & 8) ? 1 : 0)>(c);
    else wait_after<p / 32, p % 32 - 2, H, 16>(c);
    if constexpr (poll_k(H) > -9) check_poll<poll_ev(H), between(H, poll_k(H), H, 16, 4)>(c, c.pollv, H == 11);
    BARRIER_LGKM();
  }
  if constexpr (H == 12) {
#pragma unroll
    for (int n = 15; n < 18; ++n) { PIN(c.Hb[n & 7][0]); PIN(c.Hb[n & 7][1]); }
    PIN(c.b2v[0]); PIN(c.b2v[1]); PIN(c.b2v[2]);
  }
  if constexpr (H >= 5 && H < 12 && !own_unit(2 * (H - 5))) { PIN(c.Hb[(2 * (H - 5)) & 7][0]); PIN(c.Hb[(2 * (H - 5)) & 7][1]); }
  GAP();
}
template <int H> DI void mid_wait(Ctx& c) {
  if constexpr (H >= 6 && H < 13) {
    constexpr int p = hpos(2 * (H - 6) + 1);
    wait_after<p / 32, p % 32 - 2, H, 9>(c);
    PIN(c.Hb[(2 * (H - 6) + 1) & 7][0]); PIN(c.Hb[(2 * (H - 6) + 1) & 7][1]);
  }
}
#define GST(H, i) do {} while (0)
template <int H, int K> DI void step(Ctx& c) {
  GST(H, K + 2);
  if constexpr (K == 9) mid_wait<H>(c);
  if constexpr (K == KSW) asm volatile("s_setprio 0");
  mm<H, K>(c);
  if constexpr (K + 4 < 18) rd<H, K + 4>(c);
  else if constexpr (H == 13) rd<14, K - 14>(c);
  side<H, K>(c);
  GAP();
}
template <int H, int... Ks> DI void steps(Ctx& c, std::integer_sequence<int, Ks...>) { (step<H, Ks>(c), ...); }
DI void prio_young(const Ctx& c) {
  asm volatile("s_cmp_eq_u32 %0, 0\n\ts_cbranch_scc1 .Lskip%=\n\ts_setprio 1\n.Lskip%=:" ::"s"(c.hf) : "scc");
}
template <int H> DI void half_stage(Ctx& c) {
  if constexpr (H < 13) prio_young(c);
  set_bases<H>(c);
  if constexpr (H < 14) { rd<H, 0>(c); rd<H, 1>(c); }
  GAP();
  GST(H, 0);
  if constexpr (H > 0) mm<H - 1, 16>(c); else { mfma16(c.acc[0][0], c.F[2], c.X[34]); mfma16(c.acc[0][2], c.F[2], c.X[35]); }
  rd<H, 2>(c);
  side<H, -2>(c);
  GAP();
  GST(H, 1);
  if constexpr (H > 0) mm<H - 1, 17>(c); else { mfma16(c.acc[0][1], c.F[3], c.X[34]); mfma16(c.acc[0][3], c.F[3], c.X[35]); }
  rd<H, 3>(c);
  side<H, -1>(c);
  GAP();
  steps<H>(c, std::make_integer_sequence<int, 16>{});
  if constexpr (H == 14) { mm<14, 16>(c); mm<14, 17>(c); GAP(); asm volatile("s_nop 15" ::: "memory"); GAP(); }
  GST(H, 18);
  end_hs<H>(c);
  GST(H, 20);
}

struct B0 {
  unsigned vy, vw1, vc8, xa[4], l8, l80, lx, lw;
  bf16x8 G[2];
};
template <int S> DI void issue_y(Ctx& c, B0& b) {
  const unsigned dst = (S % 5) * SLICE + c.w * 1024u, so = WS_Y + c.r * 128u * XROW + S * 128u;
  asm volatile("s_mov_b32 m0, %0\n\ts_nop 0\n\tbuffer_load_dwordx4 %1, %2, %3 offen sc1 lds" ::"s"(dst), "v"(b.vy), "s"(c.sw), "s"(so));
  asm volatile("s_mov_b32 m0, %0\n\ts_nop 0\n\tbuffer_load_dwordx4 %1, %2, %3 offen sc1 lds" ::"s"(dst + 8192u), "v"(b.vy), "s"(c.sw), "s"(so + 64u * XROW));
}
template <int S> DI void issue_w(Ctx& c, B0& b) {
  const unsigned dst = (S % 5) * SLICE, kk = S * 64u;
  dma16(dst + c.w * 1024u + 16384u, b.vw1, c.s1, (kk + 8u * c.w) * W1ROW + c.freg2);
  asm volatile("s_cmp_lg_u32 %4, 0\n\ts_cbranch_scc1 .Lskip%=\n\ts_mov_b32 m0, %0\n\ts_nop 0\n\tbuffer_load_dwordx4 %1, %2, %3 offen lds\n.Lskip%=:"
               ::"s"(dst + c.t * 1024u + 24576u), "v"(b.vc8), "s"(c.s1), "s"((kk + 16u * c.t) * W1ROW + c.f82), "s"(c.hf) : "scc");
}
template <int S, int P> DI void rdx(Ctx& c, B0& b) {
  c.X[4 * S + 2 * P] = *LDSP(bf16x8, b.xa[2 * P]);
  c.X[4 * S + 2 * P + 1] = *LDSP(bf16x8, b.xa[2 * P + 1]);
}
template <int K> DI void rdf(Ctx& c) {
  const unsigned a = ((K & 1) ? c.wb2 : c.wb) + 4096u * (K >> 1);
  c.F[K] = cat(trr(a), trr(a + 512u));
}
template <int P> DI void rdg(B0& b) { b.G[P] = cat(trr(b.l8 + 2048u * P), trr(b.l8 + 2048u * P + 256u)); }
template <int S, int K> DI void mmA(Ctx& c) {
  mfma16(c.acc[0][K & 1], c.F[K], c.X[4 * S + (K & 2)]);
  mfma16(c.acc[0][2 + (K & 1)], c.F[K], c.X[4 * S + (K & 2) + 1]);
}
template <int S, int P> DI void mmB(Ctx& c, B0& b) {
  mfma16(c.acc8[0], b.G[P], c.X[4 * S + 2 * P]);
  mfma16(c.acc8[1], b.G[P], c.X[4 * S + 2 * P + 1]);
}
template <int S> DI void bases0(Ctx& c, B0& b) {
  constexpr unsigned base = (S % 5) * SLICE;
#pragma unroll
  for (int k = 0; k < 4; ++k) b.xa[k] = opq(base + (b.lx ^ (unsigned)((k >> 1) * 64 + (k & 1) * 2048)));
  c.wb = opq(base + 16384u + b.lw);
  c.wb2 = opq(c.wb ^ 32u);
  b.l8 = opq(base + 24576u + b.l80);
}
template <int S> DI void slice(Ctx& c, B0& b) {
  constexpr int n0 = 3, n = 4;
  constexpr int N = S == 0 ? 2 : S == 1 ? 4 : S == 2 ? 4 + n0 : S == 3 ? 2 + 2 * n0 : S <= 5 ? 3 * n0 : S == 6 ? 2 * n0 : S == 7 ? n0 : n;
  asm volatile("s_waitcnt vmcnt(%0) lgkmcnt(0)\n\ts_barrier" ::"i"(N) : "memory");
  STAMP(3 + S);
  GAP();
  if constexpr (S > 0) mmA<S - 1, 2>(c);
  rdx<S, 0>(c, b); rdf<0>(c);
  GAP();
  if constexpr (S > 0) mmA<S - 1, 3>(c);
  rdf<1>(c); rdg<0>(b);
  GAP();
  if constexpr (S > 0) mmB<S - 1, 1>(c, b);
  rdx<S, 1>(c, b); rdf<2>(c);
  if constexpr (S <= 1) issue_y<2 * S + 2>(c, b);
  if constexpr (S >= 2 && S <= 4) issue_y<S + 4>(c, b);
  if constexpr (S == 7) issue_slot<0>(c);
  if constexpr (S == 8) issue_slot<1>(c);
  GAP();
  mmA<S, 0>(c);
  rdf<3>(c); rdg<1>(b);
  GAP();
  mmA<S, 1>(c);
  if constexpr (S <= 1) issue_y<2 * S + 3>(c, b);
  if constexpr (S >= 1 && S <= 4) issue_w<S + 4>(c, b);
  if constexpr (S == 5) {
    const unsigned m = c.w * 7u + (lane_id() & 7u), ln = ((c.r & 7u) * 4u + c.q) * 54u + (m < 54u ? m : 53u);
    const unsigned mem = ln / 432u, rem = ln - mem * 432u, row = rem / 9u;
    c.vtch = (288u * mem + row) * W2ROW + (rem - row * 9u) * 128u;
    c.pref = poll_off(c, false);
  }
  if constexpr (S == 6) {
    const unsigned v0 = b.vw1 ^ (c.hf << 5);
#pragma unroll
    for (int i = 0; i < 4; ++i) c.vpat[i] = (v0 ^ ((i & 1) << 5)) + i * (8u * W1ROW - 1024u);
    c.vpat4 = v0 + 32u * W1ROW;
    bias_quad<0>(c.acc[1], c.tb + 256u); bias_quad<1>(c.acc[1], c.tb + 256u); bias_quad<2>(c.acc[1], c.tb + 256u); bias_quad<3>(c.acc[1], c.tb + 256u);
  }
  if constexpr (S < 8) bases0<S + 1>(c, b);
  GAP();
  mmB<S, 0>(c, b);
  GAP();
}
DI float half_sum(float v) {
  v = dpp_add<0x121>(v); v = dpp_add<0x122>(v); v = dpp_add<0x124>(v); v = dpp_add<0x128>(v);
  auto a = __builtin_amdgcn_permlane16_swap(__float_as_uint(v), __float_as_uint(v), false, false);
  return __uint_as_float(a[0]) + __uint_as_float(a[1]);
}
template <int HF> DI void prologue(Ctx& c, B0& b, const srd_t sx, const srd_t sb1) {
  const unsigned l = threadIdx.x & 63u, g = l >> 4, qq = (l >> 2) & 3u, pp = l & 3u, h = l >> 5;
  unsigned epoch;
  b.vw1 = (l >> 3) * W1ROW + (((l & 7u) ^ ((g & 1u) << 2) ^ ((c.w & 1u) << 1)) << 4);
  b.vc8 = (l >> 2) * W1ROW + ((pp ^ (h << 1)) << 4);
  if constexpr (HF == 0) {
    const unsigned row = (c.r * 128u + (c.j * 4u + c.q) * 8u + 2u * c.t) * XROW, va = h * XROW + (l & 31u) * 16u, vb = h * XROW + 1024u + (l & 31u) * 4u;
    u32x4 xa, xc; unsigned xb;
    asm volatile("buffer_load_dwordx4 %0, %1, %2, %3 offen" : "=v"(xa) : "v"(va), "s"(sx), "s"(row));
    asm volatile("buffer_load_dwordx4 %0, %1, %2, %3 offen offset:512" : "=v"(xc) : "v"(va), "s"(sx), "s"(row));
    asm volatile("buffer_load_dword %0, %1, %2, %3 offen" : "=v"(xb) : "v"(vb), "s"(sx), "s"(row));
    asm volatile("buffer_load_dword %0, off, %1, %2 sc1" : "=v"(epoch) : "s"(c.sw), "s"(WS_EPOCH + c.r * 128u));
    GAP();
    asm volatile("s_waitcnt vmcnt(1)" : "+v"(xa), "+v"(xb), "+v"(xc)::"memory");
    float v[18];
#pragma unroll
    for (int i = 0; i < 4; ++i) {
      v[2 * i] = __uint_as_float(xa[i] << 16); v[2 * i + 1] = __uint_as_float(xa[i] & 0xffff0000u);
      v[8 + 2 * i] = __uint_as_float(xc[i] << 16); v[9 + 2 * i] = __uint_as_float(xc[i] & 0xffff0000u);
    }
    v[16] = __uint_as_float(xb << 16); v[17] = __uint_as_float(xb & 0xffff0000u);
    float s0 = v[16], s1 = v[17];
#pragma unroll
    for (int i = 0; i < 8; ++i) { s0 += v[2 * i]; s1 += v[2 * i + 1]; }
    const float mean = half_sum(s0 + s1) * (1.0f / 576.0f);
#pragma unroll
    for (int i = 0; i < 18; ++i) v[i] -= mean;
    s0 = v[16] * v[16]; s1 = v[17] * v[17];
#pragma unroll
    for (int i = 0; i < 8; ++i) { s0 = __builtin_fmaf(v[2 * i], v[2 * i], s0); s1 = __builtin_fmaf(v[2 * i + 1], v[2 * i + 1], s1); }
    const float rs = KAP * __builtin_amdgcn_rsqf(half_sum(s0 + s1) * (1.0f / 576.0f) + 1e-5f);
    u32x4 ya, yc;
#pragma unroll
    for (int i = 0; i < 4; ++i) { ya[i] = cvtpk(v[2 * i] * rs, v[2 * i + 1] * rs); yc[i] = cvtpk(v[8 + 2 * i] * rs, v[9 + 2 * i] * rs); }
    const unsigned yb = cvtpk(v[16] * rs, v[17] * rs);
    asm volatile("buffer_store_dwordx4 %0, %1, %2, %3 offen sc1" ::"v"(ya), "v"(va), "s"(c.sw), "s"(WS_Y + row));
    asm volatile("buffer_store_dwordx4 %0, %1, %2, %3 offen offset:512 sc1" ::"v"(yc), "v"(va), "s"(c.sw), "s"(WS_Y + row));
    asm volatile("buffer_store_dword %0, %1, %2, %3 offen sc1\n\ts_nop 1" ::"v"(yb), "v"(vb), "s"(c.sw), "s"(WS_Y + row));
    GAP();
    STAMP(1);
  b.vy = ((l >> 3) + 8u * c.w) * XROW + (((l & 7u) ^ ((g + 4u * c.w) & 7u)) << 4);
  b.lw = (8u * g + qq) * 128u + (((4u * HF + 2u * (g & 1u) + (pp >> 1)) ^ ((qq >> 1) << 2)) << 4) + 8u * (pp & 1u);
  const unsigned xrow = 32u * c.t + 16u * HF + (l & 15u);
  b.lx = xrow * 128u + ((g ^ ((xrow >> 1) & 7u)) << 4);
  b.l80 = (8u * g + qq) * 64u + ((HF ^ (g & 1u)) << 5) + 8u * pp;
  c.tb = L_B1 + HF * 128u + g * 16u;
  c.vhld[0] = l * 16u + HF * 1024u; c.vhld[1] = c.vhld[0] ^ 1024u;
  bases0<0>(c, b);
    GAP();
    asm volatile("s_waitcnt vmcnt(0)\n\ts_barrier" : "+v"(epoch)::"memory");
    c.tag = rfl(epoch) + 1u;
    STAMP(31);
    store_flag<1>(c);
    issue_w<0>(c, b); issue_w<1>(c, b); issue_w<2>(c, b); issue_w<3>(c, b); issue_w<4>(c, b);
  } else {
    unsigned b1a, b1b;
    if (HDELAY) __builtin_amdgcn_s_sleep(HDELAY);
    {
      unsigned len;
      asm volatile("s_mov_b32 %0, mixer_code_end-mixer_code_begin-4" : "=s"(len));
      const unsigned off = (c.t * 64u + l) * 128u;
      asm volatile("s_mov_b32 m0, %0\n\ts_nop 0\n\tglobal_load_lds_dword %1, %2" ::"s"(L_JUNK), "v"(off < len ? off : len), "s"(c.pc));
    }
    asm volatile("buffer_load_dword %0, off, %1, %2 sc1" : "=v"(epoch) : "s"(c.sw), "s"(WS_EPOCH + c.r * 128u));
    asm volatile("buffer_load_ushort %0, %1, %2, %3 offen" : "=v"(b1a) : "v"((c.t * 64u + l) * 2u), "s"(sb1), "s"(c.freg2));
    asm volatile("buffer_load_ushort %0, %1, %2, %3 offen" : "=v"(b1b) : "v"((l & 31u) * 2u), "s"(sb1), "s"(c.f82));
    issue_w<0>(c, b); issue_w<1>(c, b); issue_w<2>(c, b); issue_w<3>(c, b); issue_w<4>(c, b);
    const unsigned uln = (c.r * 4u + c.q) + 64u * (c.t * 54u + (l < 54u ? l : 53u));
    STAMP(1);
  b.vy = ((l >> 3) + 8u * c.w) * XROW + (((l & 7u) ^ ((g + 4u * c.w) & 7u)) << 4);
  b.lw = (8u * g + qq) * 128u + (((4u * HF + 2u * (g & 1u) + (pp >> 1)) ^ ((qq >> 1) << 2)) << 4) + 8u * (pp & 1u);
  const unsigned xrow = 32u * c.t + 16u * HF + (l & 15u);
  b.lx = xrow * 128u + ((g ^ ((xrow >> 1) & 7u)) << 4);
  b.l80 = (8u * g + qq) * 64u + ((HF ^ (g & 1u)) << 5) + 8u * pp;
  c.tb = L_B1 + HF * 128u + g * 16u;
  c.vhld[0] = l * 16u + HF * 1024u; c.vhld[1] = c.vhld[0] ^ 1024u;
  bases0<0>(c, b);
#pragma unroll
    for (int k = 0; k < 3; ++k) {
      const unsigned i = k < 2 ? 4u * k + c.t : 8u, nn = 64u * i + l, rho = nn / 18u, chn = nn - rho * 18u;
      const unsigned rel = i < 5u ? i : i - 5u, o = chn * 16u - (rel < 4u ? rel * 1024u : 0u);
      *LDSP(unsigned, L_PAT + i * 256u + l * 4u) = rho * W2ROW + o;
      *LDSP(unsigned, L_PAT + 2304u + i * 256u + l * 4u) = ((rho & 3u) | ((rho & 12u) << 1) | ((rho & 16u) >> 2)) * W2ROW + o;
    }
    asm volatile("s_waitcnt vmcnt(%3)" : "+v"(b1a), "+v"(b1b), "+v"(epoch) : "i"(5) : "memory");
    c.tag = rfl(epoch) + 1u;
    *LDSP(float, L_B1 + (c.t * 64u + l) * 4u) = __uint_as_float(b1a << 16) * KAP;
    *LDSP(float, L_B1 + 1024u + (l & 31u) * 4u) = __uint_as_float(b1b << 16) * KAP;
    *LDSP(unsigned, L_PROG + (l & 7u) * 4u) = 0u;
    asm volatile("s_barrier" ::: "memory");
    STAMP(31);
    if (c.t < 3) {
      const unsigned rw = uln / 18u;
      asm volatile("s_mov_b32 m0, %0\n\ts_nop 0\n\tbuffer_load_dword %1, %2, %3 offen lds" ::"s"(L_JUNK),
                   "v"(rw * W1ROW + (uln - rw * 18u) * 128u), "s"(c.s1), "s"(c.j * 2304u));
    }
    if (c.w == 7) {
      const unsigned voff = WS_FLAG + 128u + ((c.r * 16u + (l & 15u)) << 10);
      __builtin_amdgcn_s_sleep(PDELAY);
      for (unsigned spin = 0; spin < (1u << 16); ++spin) {
        unsigned pv;
        asm volatile("s_sleep %3\n\tbuffer_load_dword %0, %1, %2, 0 offen sc1\n\ts_waitcnt vmcnt(0)" : "=&v"(pv) : "v"(voff), "s"(c.sw), "i"(PSLEEP) : "memory");
        if (__builtin_amdgcn_ballot_w64(pv != c.tag) == 0) break;
      }
    }
  }
}
DI void block0(Ctx& c, B0& b) {
  BARRIER_LGKM();
  STAMP(2);
  issue_y<0>(c, b); issue_y<1>(c, b);
  bias_quad<0>(c.acc[0], c.tb); bias_quad<1>(c.acc[0], c.tb); bias_quad<2>(c.acc[0], c.tb); bias_quad<3>(c.acc[0], c.tb);
  c.acc8[0] = *LDSP(volatile f32x4, c.tb + 1024u - c.hf * 64u); c.acc8[1] = *LDSP(volatile f32x4, c.tb + 1024u - c.hf * 64u);
  slice<0>(c, b); slice<1>(c, b); slice<2>(c, b); slice<3>(c, b); slice<4>(c, b);
  slice<5>(c, b); slice<6>(c, b); slice<7>(c, b); slice<8>(c, b);
  {
    const unsigned l = lane_id(), m = c.w * 21u + (l < 21u ? l : 20u);
    asm volatile("s_mov_b32 m0, %0\n\ts_nop 0\n\tbuffer_load_dword %1, %2, %3 offen lds" ::"s"(L_JUNK),
                 "v"(((c.r * 4u + c.q) + 64u * (m < 162u ? m : 161u)) * 128u), "s"(c.s2), "s"(c.j * 1152u * W2ROW));
  }
  mmB<8, 1>(c, b);
  c.wb = opq(b.lw);
  asm volatile("s_waitcnt vmcnt(%0) lgkmcnt(0)\n\ts_barrier" ::"i"(4 + ((W1PREF & 8) ? 1 : 0)) : "memory");
}

__global__ __launch_bounds__(512, 2) void mixer_mlp_kernel(const void* __restrict__ x, const void* __restrict__ w1, const void* __restrict__ b1,
                                                           const void* __restrict__ w2, const void* __restrict__ b2, void* __restrict__ out,
                                                           void* __restrict__ ws) {
  Ctx c;
  asm volatile("s_getpc_b64 %0\nmixer_code_begin:" : "=s"(c.pc));
  const unsigned blk = blockIdx.x, xg = blk & 7u, idx = blk >> 3;
  c.r = 8u * (xg >> 2) + (idx >> 2); c.j = xg & 3u; c.q = idx & 3u;
  c.w = rfl(threadIdx.x >> 6); c.t = c.w & 3u; c.hf = c.w >> 2;
  STAMP(0);
  const srd_t sx = __builtin_amdgcn_make_buffer_rsrc((void*)x, 0, S_ * D_ * 2, 0x00020000);
  c.s1 = __builtin_amdgcn_make_buffer_rsrc((void*)w1, 0, D_ * F_ * 2, 0x00020000);
  c.s2 = __builtin_amdgcn_make_buffer_rsrc((void*)w2, 0, D_ * F_ * 2, 0x00020000);
  c.sw = __builtin_amdgcn_make_buffer_rsrc(ws, 0, WS_USED, 0x00020000);
  c.sb2 = __builtin_amdgcn_make_buffer_rsrc((void*)b2, 0, D_ * 2, 0x00020000);
  const srd_t sb1 = __builtin_amdgcn_make_buffer_rsrc((void*)b1, 0, F_ * 2, 0x00020000);
  c.freg2 = (1152u * c.j + fstart(c.q, 0)) * 2u;
  c.f82 = (1152u * c.j + fstart(c.q, 8)) * 2u;
  c.fown = WS_FLAG + (((c.r * 4u + c.j) * 4u + c.q) << 10);
  c.pollv = 0u;
  c.sw1 = (72u * c.t + 40u * c.hf) * W1ROW + c.freg2;
  c.ldst = c.t * 9216u + c.hf * 5120u;
  B0 b0;
  if (c.hf == 0) prologue<0>(c, b0, sx, sb1); else prologue<1>(c, b0, sx, sb1);
  asm volatile("" : "+s"(c.sw1), "+s"(c.ldst), "+s"(c.freg2), "+s"(c.f82), "+s"(c.r), "+s"(c.w), "+s"(c.t));
  block0(c, b0);
  {
    const unsigned hb = WS_H + ((c.r * 4u + c.j) * 144u + c.t) * 2048u;
#pragma unroll
    for (int d = 0; d < 4; ++d) c.hq[d] = hb + ((c.q + d) & 3u) * 73728u + c.hf * 8192u;
#pragma unroll
    for (int e = 0; e < 2; ++e) {
      c.h8[e] = hb + ((c.q + 2u * c.hf + e) & 3u) * 73728u + 65536u;
      c.sw2[e] = (1152u * c.j + fstart((c.q + 2u * e + (c.t & 1u)) & 3u, c.t >> 1)) * W2ROW + 288u * c.q;
    }
    c.sw28 = (1152u * c.j + fstart((c.q + c.t) & 3u, 8)) * W2ROW + 288u * c.q;
  }
  asm volatile("" : "+s"(c.sw1), "+s"(c.ldst), "+s"(c.hq[0]), "+s"(c.hq[1]), "+s"(c.hq[2]), "+s"(c.hq[3]), "+s"(c.h8[0]), "+s"(c.h8[1]),
               "+s"(c.sw2[0]), "+s"(c.sw2[1]), "+s"(c.sw28));
  STAMP(12);
  half_stage<0>(c); STAMP(13);
  half_stage<1>(c); STAMP(14);
  half_stage<2>(c); STAMP(15);
  half_stage<3>(c); STAMP(16);
  half_stage<4>(c); STAMP(17);
  half_stage<5>(c); STAMP(18);
  half_stage<6>(c); STAMP(19);
  half_stage<7>(c); STAMP(20);
  half_stage<8>(c); STAMP(21);
  half_stage<9>(c); STAMP(22);
  half_stage<10>(c); STAMP(23);
  half_stage<11>(c); STAMP(24);
  half_stage<12>(c); STAMP(25);
  half_stage<13>(c); STAMP(26);
  half_stage<14>(c); STAMP(27);

  const unsigned l = lane_id();
  comb<4, 0>(c); comb<4, 1>(c);
  {
    for (unsigned spin = 0; spin < (1u << 20); ++spin)
      if (rfl(*LDSP(volatile unsigned, c.vpg ^ 16u)) > 4u) break;
    asm volatile("" ::: "memory");
    f32x4 g8 = ldsf4(c.xr + 8192u);
    c.xg[0] = ldsf4(c.xr + 6144u); c.xg[1] = ldsf4(c.xr + 7168u);
    comb<3, 4>(c); comb<3, 5>(c); comb<3, 6>(c);
    c.xg[0] = g8;
    comb<4, 4>(c); comb<4, 5>(c); comb<4, 6>(c);
  }
  BARRIER_LGKM();
  STAMP(28);
  {
    const u32x2 bA = c.b2v[0], bB = c.b2v[1], bC = c.b2v[2];
    const unsigned sp = WS_P + ((c.r * 4u + c.q) * 4u + c.j) * 4u * 9216u + c.hf * 4608u;
    const unsigned s0 = sp + ((c.j + 1u) & 3u) * 9216u, s1 = sp + ((c.j + 2u) & 3u) * 9216u, s2 = sp + ((c.j + 3u) & 3u) * 9216u;
    const unsigned va = l * 16u + c.t * 1024u, vb = 4096u + l * 8u, oa = (c.hf ^ 1u) * 3u * SLOT + c.j * 9216u + l * 16u;
    u32x4 pa[3]; u32x2 pb[3];
    f32x4 o0 = ldsf4(oa + c.t * 2048u), o1 = ldsf4(oa + c.t * 2048u + 1024u), o2 = ldsf4(oa + 8192u);
    if (c.t == 3) __builtin_amdgcn_s_sleep(TDELAY);
    for (unsigned spin = 0; spin < (1u << 16); ++spin) {
      asm volatile("buffer_load_dwordx4 %0, %6, %8, %9 offen sc1\n\tbuffer_load_dwordx4 %1, %6, %8, %10 offen sc1\n\t"
                   "buffer_load_dwordx4 %2, %6, %8, %11 offen sc1\n\t"
                   "s_cmp_lg_u32 %12, 3\n\ts_cbranch_scc1 .Lskip%=\n\t"
                   "buffer_load_dwordx2 %3, %7, %8, %9 offen sc1\n\tbuffer_load_dwordx2 %4, %7, %8, %10 offen sc1\n\t"
                   "buffer_load_dwordx2 %5, %7, %8, %11 offen sc1\n.Lskip%=:\n\ts_waitcnt vmcnt(0)"
                   : "=&v"(pa[0]), "=&v"(pa[1]), "=&v"(pa[2]), "=&v"(pb[0]), "=&v"(pb[1]), "=&v"(pb[2])
                   : "v"(va), "v"(vb), "s"(c.sw), "s"(s0), "s"(s1), "s"(s2), "s"(c.t) : "memory", "scc");
      unsigned mx = 0;
#pragma unroll
      for (int k = 0; k < 3; ++k) {
#pragma unroll
        for (int i = 0; i < 4; ++i) mx = mx > pa[k][i] ? mx : pa[k][i];
        if (c.t == 3) { mx = mx > pb[k][0] ? mx : pb[k][0]; mx = mx > pb[k][1] ? mx : pb[k][1]; }
      }
      if (!__any(mx == ~0u)) break;
      __builtin_amdgcn_s_sleep(2);
    }
    STAMP(29);
#pragma unroll
    for (int k = 0; k < 3; ++k)
#pragma unroll
      for (int i = 0; i < 2; ++i) {
        o0[2 * i] += __uint_as_float(pa[k][i] << 16); o0[2 * i + 1] += __uint_as_float(pa[k][i] & 0xffff0000u);
        o1[2 * i] += __uint_as_float(pa[k][2 + i] << 16); o1[2 * i + 1] += __uint_as_float(pa[k][2 + i] & 0xffff0000u);
      }
    unsigned short* op = (unsigned short*)out + (size_t)(128u * c.r + 32u * c.j + 16u * c.hf + (l & 15u)) * D_ + 144u * c.q;
    const unsigned e0 = cvtpk(__builtin_fmaf(o0[0], RKAP, __uint_as_float(bA[0] << 16)), __builtin_fmaf(o0[1], RKAP, __uint_as_float(bA[0] & 0xffff0000u)));
    const unsigned e1 = cvtpk(__builtin_fmaf(o0[2], RKAP, __uint_as_float(bA[1] << 16)), __builtin_fmaf(o0[3], RKAP, __uint_as_float(bA[1] & 0xffff0000u)));
    const unsigned f0 = cvtpk(__builtin_fmaf(o1[0], RKAP, __uint_as_float(bB[0] << 16)), __builtin_fmaf(o1[1], RKAP, __uint_as_float(bB[0] & 0xffff0000u)));
    const unsigned f1 = cvtpk(__builtin_fmaf(o1[2], RKAP, __uint_as_float(bB[1] << 16)), __builtin_fmaf(o1[3], RKAP, __uint_as_float(bB[1] & 0xffff0000u)));
    auto r0 = __builtin_amdgcn_permlane16_swap(e0, f0, false, false);
    auto r1 = __builtin_amdgcn_permlane16_swap(e1, f1, false, false);
    *(u32x4*)(op + 32u * c.t + 16u * ((l >> 4) & 1u) + 8u * (l >> 5)) = (u32x4){r0[0], r1[0], r0[1], r1[1]};
    if (c.t == 3) {
#pragma unroll
      for (int k = 0; k < 3; ++k)
#pragma unroll
        for (int i = 0; i < 2; ++i) { o2[2 * i] += __uint_as_float(pb[k][i] << 16); o2[2 * i + 1] += __uint_as_float(pb[k][i] & 0xffff0000u); }
      *(u32x2*)(op + 128u + 4u * (l >> 4)) =
          (u32x2){cvtpk(__builtin_fmaf(o2[0], RKAP, __uint_as_float(bC[0] << 16)), __builtin_fmaf(o2[1], RKAP, __uint_as_float(bC[0] & 0xffff0000u))),
                  cvtpk(__builtin_fmaf(o2[2], RKAP, __uint_as_float(bC[1] << 16)), __builtin_fmaf(o2[3], RKAP, __uint_as_float(bC[1] & 0xffff0000u)))};
    }
    asm volatile("mixer_code_end:");
    if (c.w == 0 && c.j == 0 && c.q == 0 && l == 0)
      asm volatile("buffer_store_dword %0, off, %1, %2 sc1" ::"v"(c.tag), "s"(c.sw), "s"(WS_EPOCH + c.r * 128u));
  }
}

extern "C" void kernel_launch(void* const* d_in, const int* in_sizes, int n_in, void* d_out, int out_size, void* d_ws, size_t ws_size,
                              hipStream_t stream) {
  (void)in_sizes; (void)n_in; (void)out_size; (void)ws_size;
  (void)hipFuncSetAttribute((const void*)mixer_mlp_kernel, hipFuncAttributeMaxDynamicSharedMemorySize, LDS_BYTES);
  mixer_mlp_kernel<<<256, 512, LDS_BYTES, stream>>>(d_in[0], d_in[1], d_in[2], d_in[3], d_in[4], d_out, d_ws);
}
```

```cpp
#include <hip/hip_runtime.h>
#include <stdint.h>
#include <utility>

typedef __bf16 bf16x8 __attribute__((ext_vector_type(8)));
typedef __bf16 bf16x2 __attribute__((ext_vector_type(2)));
typedef short s16x4 __attribute__((ext_vector_type(4)));
typedef float f32x16 __attribute__((ext_vector_type(16)));
typedef float f32x4 __attribute__((ext_vector_type(4)));
typedef float f32x2 __attribute__((ext_vector_type(2)));
typedef unsigned u32x4 __attribute__((ext_vector_type(4)));
typedef unsigned u32x2 __attribute__((ext_vector_type(2)));
typedef __amdgpu_buffer_rsrc_t srd_t;
#define DI __device__ __forceinline__
#define LAS __attribute__((address_space(3)))
#define LDSP(T, a) ((LAS T*)(size_t)(a))

constexpr unsigned S_ = 2048, D_ = 576, F_ = 4608;
constexpr unsigned XROW = D_ * 2, W1ROW = F_ * 2, W2ROW = D_ * 2;
constexpr unsigned SLOT = 36864, RING = 4 * SLOT, SLICE = 28672;
constexpr unsigned L_B1 = RING, L_PAT = L_B1 + 1152, L_JUNK = L_PAT + 4608, L_PROG = L_JUNK + 256, LDS_BYTES = L_PROG + 64;
static_assert(LDS_BYTES <= 163840, "LDS");
constexpr unsigned WS_EPOCH = 0, WS_FLAG = 4096, WS_Y = WS_FLAG + 256 * 8 * 128, WS_H = WS_Y + S_ * D_ * 2, H_BYTES = S_ * F_ * 2;
constexpr unsigned WS_P = WS_H + H_BYTES, P_BYTES = 16 * 4 * 4 * 4 * 9216;
constexpr unsigned WS_USED = WS_P + P_BYTES;
constexpr float KAP = 0.4686686930532063f, GAM = 4.91222953926424f, RKAP = 2.1337034344781243f;
#define KSW 10
#define PSLEEP 1
#define TOUCH 1
#define PREFH 1
#define HK2 2
#define CPREF 1
#define W1PREF 24
#define PDELAY 10
#define TDELAY 8
#define EARLYMARK 0
#define BPRIO 0
#define HSCHED 1
#define HDELAY 0
#define PREF 1
#define STAMP(i) do {} while (0)

DI unsigned rfl(unsigned x) { return __builtin_amdgcn_readfirstlane(x); }
DI s16x4 trr(unsigned a) { return __builtin_amdgcn_ds_read_tr16_b64_v4i16(LDSP(s16x4, a)); }
DI bf16x8 cat(s16x4 lo, s16x4 hi) { return __builtin_bit_cast(bf16x8, __builtin_shufflevector(lo, hi, 0, 1, 2, 3, 4, 5, 6, 7)); }
DI f32x4 ldsf4(unsigned a) { return *LDSP(f32x4, a); }
DI unsigned cvtpk(float lo, float hi) { f32x2 v = {lo, hi}; return __builtin_bit_cast(unsigned, __builtin_convertvector(v, bf16x2)); }
template <int CTRL> DI float dpp_add(float v) {
  return v + __builtin_bit_cast(float, __builtin_amdgcn_update_dpp(0, __builtin_bit_cast(int, v), CTRL, 0xf, 0xf, false));
}
DI void mfma16(f32x4& acc, bf16x8 a, bf16x8 b) { asm volatile("v_mfma_f32_16x16x32_bf16 %0, %1, %2, %0" : "+v"(acc) : "v"(a), "v"(b)); }
DI void mfma16z(f32x4& acc, bf16x8 a, bf16x8 b) { asm volatile("v_mfma_f32_16x16x32_bf16 %0, %1, %2, 0" : "=&v"(acc) : "v"(a), "v"(b)); }
#define BARRIER_LGKM() asm volatile("s_waitcnt lgkmcnt(0)\n\ts_barrier" ::: "memory")
#define GAP() __builtin_amdgcn_sched_barrier(0)
#define PIN(x) asm volatile("" : "+v"(x))

struct Ctx {
  srd_t s1, s2, sw;
  unsigned w, t, hf, r, j, q;
  unsigned freg2, f82;
  unsigned tag, sdma, sw1, ldst, hq[4], h8[2], sw2[2], sw28, fown;
  u32x4 vpat;
  unsigned vpat4;
  unsigned wb, wb2;
  unsigned vhld[2], tb, pref, vtch, xw, xr, pg, sp, vpg;
  u32x4 pk;
  u32x2 b2v[3];
  f32x4 xg[2];
  srd_t sb2;
  float gz[2], gm, gz8, gm8;
  unsigned own[4][8], hv8[8];
  bf16x8 X[36];
  bf16x8 F[4];
  f32x4 acc[2][4];
  f32x4 acc8[2];
  f32x4 acc2[2][9];
  u32x4 Hb[8][2];
  unsigned pollv;
};
DI unsigned lane_id() {
  unsigned l;
  asm volatile("v_mbcnt_lo_u32_b32 %0, -1, 0\n\tv_mbcnt_hi_u32_b32 %0, -1, %0" : "=v"(l));
  return l;
}
DI unsigned opq(unsigned v) { PIN(v); return v; }

constexpr int pair_of(int h) { return h < 8 ? 0 : (h - 7) / 2; }
constexpr int ph_of(int h) { return h < 8 ? h - 6 : (h - 7) % 2; }
constexpr bool own_unit(int n) { return n == 0 || n == 6 || n == 10 || n == 14; }
constexpr int hload_at(int h, int k) {
  if (h == 5) return k == 2 ? 2 : k == 5 ? 3 : k == 10 ? 4 : k == 12 ? 5 : -1;
  if (h == 6) return k == 0 ? 6 : k == 2 ? 7 : k == 10 ? 8 : k == 12 ? 9 : -1;
  if (h == 7) return k == 2 ? 10 : k == 10 ? 11 : -1;
  if (h == 8) return k == 0 ? 14 : k == 2 ? 15 : k == 12 ? 16 : -1;
  if (h == 9) return k == 0 ? 17 : k == 2 ? 18 : k == 10 ? 19 : -1;
  if (h == 10) return k == 0 ? 22 : k == 2 ? 23 : k == 10 ? 24 : k == 12 ? 25 : -1;
  if (h == 11) return k == 0 ? 30 : k == 2 ? 31 : k == 10 ? 26 : k == 12 ? 27 : -1;
  if (h == 12) return k == -2 ? 32 : k == 0 ? 33 : k == 2 ? 34 : k == 4 ? 35 : -1;
  return -1;
}
constexpr bool b2_at(int h, int k) { return h == 12 && k == 6; }
constexpr int dma_at(int h, int k) {
  if (h == 0) return (k < 16 && (k & 1) == 0) ? (k < 8 ? 2 * 8 + (k + 2) / 2 : 3 * 8 + (k - 8) / 2) : (k == 15 ? 3 * 8 + 4 : -1);
  if (h + 3 > 14) return -1;
  const int i = h < 4 ? (k == -2 ? 0 : k == 2 ? 1 : k == 6 ? 2 : k == 10 ? 3 : k == 13 ? 4 : -1)
                      : (k == -2 ? 0 : k == 4 ? 1 : k == 6 ? 2 : k == 8 ? 3 : k == 14 ? 4 : -1);
  return i < 0 ? -1 : (h + 3) * 8 + i;
}
constexpr int poll_k(int h) { return h == 4 ? 5 : h == 10 ? 5 : (h == 7 || h == 9 || h == 11) ? -1 : -9; }
constexpr int poll_ev(int h) { return h == 4 ? 2 : h == 7 ? 3 : h == 10 ? 5 : 4; }
constexpr bool poll_at(int h, int k) { return k == poll_k(h); }
constexpr int flag_at(int h) { return (h == 3 || h == 5 || h == 7 || h == 9) ? (h + 1) / 2 : 0; }
constexpr bool hst_at(int h, int k) { return (h < 8 && (h & 1) && (k == 0 || k == 15)) || (h == 1 && k == 12); }
constexpr bool pref_at(int h, int k) { return PREF && h < 6 && k == 15; }
constexpr bool touch_at(int h, int k) { return TOUCH && (h == 2 || h == 4 || h == 6 || h == 8) && k == 13; }
constexpr bool mark_at(int h, int k) { return !EARLYMARK && ((h == 2 && (k == 4 || k == 12)) || (h == 4 && k == 2)); }
constexpr int nops(int h, int k, int n) {
  return (b2_at(h, k) ? 3 : 0) + (hload_at(h, k) >= 0 ? 1 : 0) + (hst_at(h, k) ? 1 : 0) + (mark_at(h, k) ? 1 : 0) +
         (dma_at(h, k) >= 0 && (dma_at(h, k) & 7) < n ? 1 : 0);
}
constexpr int between(int h0, int k0, int h1, int k1, int n) {
  int s = 0;
  for (int h = h0; h <= h1; ++h)
    for (int k = -2; k < 16; ++k)
      if ((h > h0 || k > k0) && (h < h1 || k < k1)) s += nops(h, k, n);
  for (int h = h0; h <= h1; ++h)
    if (pref_at(h, 15) && (h < h1 || k1 == 16)) s += 1;
  return s;
}
constexpr int cmin(int a, int b) { return a < b ? a : b; }

DI unsigned fstart(unsigned qq, unsigned ch) { return 32u * (9u * qq + ch + (qq & 1u) * (1u - 9u * (ch >> 3))); }

DI void dma16(unsigned m0v, unsigned voff, srd_t srd, unsigned soff) {
  asm volatile("s_mov_b32 m0, %0\n\ts_nop 0\n\tbuffer_load_dwordx4 %1, %2, %3 offen lds" ::"s"(m0v), "v"(voff), "s"(srd), "s"(soff));
}
template <int HS, int I> DI void issue_piece(Ctx& c) {
  const srd_t sd = HS < 6 ? c.s1 : c.s2;
  if constexpr (I == 0) {
    if constexpr (HS < 6) c.sdma = c.sw1 + ((HS & 1) * 288u * W1ROW + ((HS >> 1) + 1) * 128u);
    else c.sdma = HS == 8 ? c.sw28 : c.sw2[ph_of(HS)] + pair_of(HS) * 64u * W2ROW;
    dma16((HS & 3) * SLOT + c.ldst, c.vpat[0], sd, c.sdma);
  } else if constexpr (I < 4) {
    asm volatile("buffer_load_dwordx4 %0, %1, %2 offen offset:%3 lds" ::"v"(c.vpat[I]), "s"(sd), "s"(c.sdma), "i"(1024 * I));
  } else {
    asm volatile("s_cmp_lg_u32 %4, 0\n\ts_cbranch_scc1 .Lskip%=\n\ts_mov_b32 m0, %0\n\ts_nop 0\n\tbuffer_load_dwordx4 %1, %2, %3 offen lds\n.Lskip%=:"
                 ::"s"((HS & 3) * SLOT + 4096u + c.ldst), "v"(c.vpat4), "s"(sd), "s"(c.sdma), "s"(c.hf) : "scc");
  }
}
template <int HS> DI void issue_slot(Ctx& c) {
  issue_piece<HS, 0>(c); issue_piece<HS, 1>(c); issue_piece<HS, 2>(c); issue_piece<HS, 3>(c); issue_piece<HS, 4>(c);
}
template <int N, int U> DI void issue_hload(Ctx& c) {
  constexpr int h = 6 + N / 2;
  const unsigned so = h == 8 ? c.h8[N & 1] : c.hq[2 * ph_of(h) + (N & 1)] + pair_of(h) * 16384u;
  asm volatile("buffer_load_dwordx4 %0, %1, %2, %3 offen sc1" : "=v"(c.Hb[N & 7][U]) : "v"(c.vhld[U]), "s"(c.sw), "s"(so));
}
DI unsigned poll_off(const Ctx& c, bool dest) {
  const unsigned l = lane_id(), dp = (l & 3u) ? (l & 3u) : 1u;
  return WS_FLAG + ((dest ? (c.r * 4u + ((c.j + dp) & 3u)) * 4u + c.q : (c.r * 4u + c.j) * 4u + ((c.q + dp) & 3u)) << 10);
}
template <int EV> DI void store_flag(Ctx& c) {
  const u32x4 v = {c.tag, c.tag, c.tag, c.tag};
  asm volatile("s_cmp_lg_u32 %4, 0\n\ts_cbranch_scc1 .Lskip%=\n\ts_mov_b64 exec, 0xff\n\t"
               "buffer_store_dwordx4 %0, %1, %2, %3 offen offset:%5 sc1\n\ts_mov_b64 exec, -1\n.Lskip%=:\n\ts_nop 0"
               ::"v"(v), "v"(c.vhld[0]), "s"(c.sw), "s"(c.fown), "s"(c.w), "i"(EV * 128) : "scc");
}
template <int EV> DI void issue_poll(Ctx& c, unsigned& pv, unsigned voff) {
  asm volatile("s_cmp_lg_u32 %3, 7\n\ts_cbranch_scc1 .Lskip%=\n\tbuffer_load_dword %0, %1, %2, 0 offen offset:%4 sc1\n.Lskip%=:"
               : "+v"(pv) : "v"(voff), "s"(c.sw), "s"(c.w), "i"(EV * 128) : "scc");
}
template <int EV, int N> DI void check_poll(Ctx& c, unsigned& pv, bool dest) {
  if (c.w == 7) {
    asm volatile("s_waitcnt vmcnt(%1)" : "+v"(pv) : "i"(N) : "memory");
    if (__builtin_expect(__builtin_amdgcn_ballot_w64(pv != c.tag) != 0, 0)) {
      const unsigned voff = poll_off(c, dest);
      for (unsigned spin = 0; spin < (1u << 16); ++spin) {
        unsigned v;
        asm volatile("s_sleep 1\n\tbuffer_load_dword %0, %1, %2, 0 offen offset:%3 sc1\n\ts_waitcnt vmcnt(0)" : "=&v"(v) : "v"(voff), "s"(c.sw), "i"(EV * 128) : "memory");
        if (__builtin_amdgcn_ballot_w64(v != c.tag) == 0) break;
      }
    }
  }
}
template <int P> DI void store_h(Ctx& c, const unsigned (&hv)[8], unsigned vo, unsigned so) {
  const u32x4 v = {hv[4 * P], hv[4 * P + 1], hv[4 * P + 2], hv[4 * P + 3]};
  asm volatile("buffer_store_dwordx4 %0, %1, %2, %3 offen sc1\n\ts_nop 1" ::"v"(v), "v"(vo), "s"(c.sw), "s"(so));
}
template <int N0, int N1> DI void wait_vm(const Ctx&) {
  static_assert(N1 <= N0 && N1 >= 0 && N0 < 64, "vmcnt");
  asm volatile("s_waitcnt vmcnt(%0)" ::"i"(N1) : "memory");
}

template <int H> DI void set_bases(Ctx& c) {
  constexpr unsigned d = (H & 3) ? SLOT : 0u - 3u * SLOT;
  if constexpr (H == 6) {
    const unsigned l = lane_id();
    c.wb = opq(2u * SLOT + (l >> 2) * 288u + 8u * (l & 3u) + c.hf * 18432u);
  } else if constexpr (H > 0 && H < 14) c.wb = opq(c.wb + d);
  if constexpr (H < 6) c.wb2 = opq(c.wb ^ 32u);
}
template <int H, int S> DI void rd(Ctx& c) {
  constexpr int tau = 18 * (H - 13) + S;
  constexpr unsigned o = H < 6 ? 4096u * (S >> 1) : H < 13 ? 9216u * (S / 9) + 32u * (S % 9) : ((tau >> 1) & 1) * SLOT + (tau & 1) * 9216u + 32u * (tau >> 2);
  constexpr unsigned e = H < 6 ? 512u : 4608u;
  const unsigned b = (H < 6 && (S & 1)) ? c.wb2 : c.wb;
  c.F[(H * 18 + S) & 3] = cat(trr(b + o), trr(b + o + e));
}
template <int H, int S> DI void mm(Ctx& c) {
  const bf16x8 a = c.F[(H * 18 + S) & 3];
  if constexpr (H < 6) {
    constexpr int B = ((H >> 1) + 1) & 1;
    constexpr int P = 9 * (H & 1) + (S >> 1);
    mfma16(c.acc[B][S & 1], a, c.X[2 * P]);
    mfma16(c.acc[B][2 + (S & 1)], a, c.X[2 * P + 1]);
  } else {
    constexpr int tau = 18 * (H - 13) + S;
    constexpr int N = H < 13 ? 2 * (H - 6) + S / 9 : 14 + (tau & 3), dt = H < 13 ? S % 9 : tau >> 2;
    u32x4 b0 = c.Hb[N & 7][0], b1 = c.Hb[N & 7][1];
    if constexpr (own_unit(N)) {
      const unsigned(&o)[8] = c.own[pair_of(6 + N / 2)];
      b0 = (u32x4){o[0], o[1], o[2], o[3]}; b1 = (u32x4){o[4], o[5], o[6], o[7]};
    }
    if constexpr (N == 0) {
      mfma16z(c.acc2[0][dt], a, __builtin_bit_cast(bf16x8, b0));
      mfma16z(c.acc2[1][dt], a, __builtin_bit_cast(bf16x8, b1));
    } else {
      mfma16(c.acc2[0][dt], a, __builtin_bit_cast(bf16x8, b0));
      mfma16(c.acc2[1][dt], a, __builtin_bit_cast(bf16x8, b1));
    }
  }
}
template <int Q> DI void bias_quad(f32x4 (&acc)[4], unsigned a) { acc[Q] = *LDSP(volatile f32x4, a + (Q & 1) * 64u); }
template <int E, int ST> DI void gelu_stage(float s, float& z, float& m0, unsigned (&hv)[8]) {
  if constexpr (ST == 0) { z = s * __builtin_fmaf(s, s, GAM); PIN(z); }
  if constexpr (ST == 1) { z = __builtin_amdgcn_exp2f(-z); PIN(z); }
  if constexpr (ST == 2) { z = __builtin_amdgcn_rcpf(1.0f + z); PIN(z); }
  if constexpr (ST == 3) {
    if constexpr ((E & 1) == 0) { m0 = s * z; PIN(m0); }
    else { float m = s * z, mm0 = m0; PIN(m); PIN(mm0); hv[E >> 1] = cvtpk(mm0, m); PIN(hv[E >> 1]); }
  }
}
constexpr int gstart(int e) { return 1 + (e * 29) / 15; }
constexpr int gelem(int g, int st) {
  for (int e = 0; e < 16; ++e) if (gstart(e) + st == g) return e;
  return -1;
}
template <int B, int G, int ST> DI void gelu_part(Ctx& c) {
  constexpr int e = gelem(G, ST);
  if constexpr (e >= 0) gelu_stage<e, ST>(c.acc[B & 1][e >> 2][e & 3], c.gz[e & 1], c.gm, c.own[B]);
}
template <int B, int G> DI void gelu_gap(Ctx& c) {
  gelu_part<B, G, 3>(c); gelu_part<B, G, 2>(c); gelu_part<B, G, 1>(c); gelu_part<B, G, 0>(c);
  if constexpr (B < 2) {
    if constexpr (G == 27) { bias_quad<0>(c.acc[B & 1], c.tb + (B + 2) * 256u); bias_quad<1>(c.acc[B & 1], c.tb + (B + 2) * 256u); }
    if constexpr (G == 31) bias_quad<2>(c.acc[B & 1], c.tb + (B + 2) * 256u);
    if constexpr (G == 33) bias_quad<3>(c.acc[B & 1], c.tb + (B + 2) * 256u);
  }
}

template <int H0, int K0, int H1, int K1> DI void wait_after(const Ctx& c) {
  wait_vm<between(H0, K0, H1, K1, 5), between(H0, K0, H1, K1, 4)>(c);
}
DI unsigned nosent(unsigned v) { return v < 0xfffffffeu ? v : 0xfffffffeu; }
template <int M, int ST> DI void comb(Ctx& c) {
  constexpr int NT = M < 4 ? 2 : 1;
  if constexpr (ST == 0) *LDSP(f32x4, c.xw + 2 * M * 1024u) = c.acc2[1][2 * M];
  if constexpr (ST == 1) {
    if constexpr (NT == 2) *LDSP(f32x4, c.xw + (2 * M + 1) * 1024u) = c.acc2[1][2 * M + 1];
    asm volatile("s_mov_b64 exec, 1\n\tds_write_b32 %0, %1\n\ts_mov_b64 exec, -1" ::"v"(c.vpg), "v"(M + 1u) : "memory");
  }
  if constexpr (ST == 2) c.pg = *LDSP(volatile unsigned, c.vpg ^ 16u);
  if constexpr (ST == 3) {
    if (__builtin_expect(rfl(c.pg) <= (unsigned)M, 0)) {
      for (unsigned spin = 0; spin < (1u << 20); ++spin)
        if (rfl(*LDSP(volatile unsigned, c.vpg ^ 16u)) > (unsigned)M) break;
    }
    asm volatile("" ::: "memory");
#pragma unroll
    for (int i = 0; i < NT; ++i) c.xg[i] = ldsf4(c.xr + (2 * M + i) * 1024u);
  }
  if constexpr (ST == 4) {
#pragma unroll
    for (int i = 0; i < NT; ++i)
#pragma unroll
      for (int k = 0; k < 4; ++k) { c.xg[i][k] += c.acc2[0][2 * M + i][k]; PIN(c.xg[i][k]); }
  }
  if constexpr (ST == 5) {
#pragma unroll
    for (int i = 0; i < 2 * NT; ++i) { c.pk[i] = nosent(cvtpk(c.xg[i >> 1][2 * (i & 1)], c.xg[i >> 1][2 * (i & 1) + 1])); }
    PIN(c.pk);
  }
  if constexpr (ST == 6) {
    if (c.t != c.j) {
      if constexpr (NT == 2)
        asm volatile("buffer_store_dwordx4 %0, %1, %2, %3 offen offset:%4 sc1\n\ts_nop 1" ::"v"(c.pk), "v"(c.vtch), "s"(c.sw), "s"(c.sp), "i"(M * 1024));
      else
        asm volatile("buffer_store_dwordx2 %0, %1, %2, %3 offen sc1" ::"v"((u32x2){c.pk[0], c.pk[1]}), "v"(c.vtch >> 1), "s"(c.sw), "s"(c.sp + 4096u));
    } else {
#pragma unroll
      for (int i = 0; i < NT; ++i) *LDSP(f32x4, c.xr + (2 * M + i) * 1024u) = c.xg[i];
    }
  }
}
template <int TAU, int ST, int OFF> DI void comb_at(Ctx& c) {
  if constexpr (TAU >= OFF && (TAU - OFF) % 8 == 0 && (TAU - OFF) / 8 < 4) comb<(TAU - OFF) / 8, ST>(c);
}
template <int H, int K> DI void side(Ctx& c) {
  if constexpr (poll_at(H, K)) {
    if constexpr (H == 11) issue_poll<4>(c, c.pollv, poll_off(c, true));
    else issue_poll<poll_ev(H)>(c, c.pollv, c.pref);
  }
  if constexpr (K == -1 && flag_at(H)) store_flag<flag_at(H)>(c);
  if constexpr (hload_at(H, K) >= 0) issue_hload<(hload_at(H, K) >> 1), (hload_at(H, K) & 1)>(c);
  if constexpr (mark_at(H, K)) {
    constexpr unsigned k = H == 4 ? 2u : K == 4 ? 0u : 1u;
    const u32x4 ones = {~0u, ~0u, ~0u, ~0u};
    const unsigned so = WS_P + (((c.r * 4u + c.q) * 4u + c.j) * 4u + ((c.j + 1u + k) & 3u)) * 9216u, vo = lane_id() * 16u;
    asm volatile("buffer_store_dwordx4 %0, %1, %2, %3 offen sc1\n\ts_cmp_lg_u32 %4, %5\n\ts_cbranch_scc1 .Lskip%=\n\t"
                 "buffer_store_dwordx4 %0, %1, %2, %6 offen sc1\n.Lskip%=:\n\ts_nop 1"
                 ::"v"(ones), "v"(vo), "s"(c.sw), "s"(so + c.w * 1024u), "s"(c.w), "i"(k), "s"(so + 8192u) : "scc");
  }
  if constexpr (dma_at(H, K) >= 0) issue_piece<(dma_at(H, K) >> 3), (dma_at(H, K) & 7)>(c);
  if constexpr ((H == 2 && K == 14) || ((H == 4 || H == 5) && K == 15)) {
    const unsigned a = L_PAT + (H == 4 ? 2304u : 0u) + lane_id() * 4u;
#pragma unroll
    for (int i = 0; i < 4; ++i) c.vpat[i] = *LDSP(unsigned, a + (c.hf * 5u + i) * 256u);
    c.vpat4 = *LDSP(unsigned, a + 1024u);
  }
  if constexpr (b2_at(H, K)) {
    const unsigned l = lane_id();
#pragma unroll
    for (int i = 0; i < 3; ++i)
      asm volatile("buffer_load_dwordx2 %0, %1, %2, %3 offen" : "=v"(c.b2v[i]) : "v"((l >> 4) * 8u), "s"(c.sb2), "s"(288u * c.q + (i < 2 ? 64u * c.t + 32u * i : 256u)));
    c.xw = c.hf * 3u * SLOT + c.t * 9216u + l * 16u;
    c.xr = (c.hf ^ 1u) * 3u * SLOT + c.t * 9216u + l * 16u;
    c.sp = WS_P + (((c.r * 4u + c.q) * 4u + c.t) * 4u + c.j) * 9216u + c.hf * 4608u;
    c.vpg = L_PROG + c.w * 4u; c.vtch = l * 16u;
    asm volatile("" : "+v"(c.xw), "+v"(c.xr), "+v"(c.vpg), "+v"(c.vtch), "+s"(c.sp));
  }
  if constexpr (H >= 13) {
    constexpr int tau = 18 * (H - 13) + K;
    comb_at<tau, 6, 17>(c); comb_at<tau, 5, 16>(c); comb_at<tau, 4, 15>(c); comb_at<tau, 3, 13>(c);
    comb_at<tau, 2, 12>(c); comb_at<tau, 1, 9>(c); comb_at<tau, 0, 6>(c);
  }
  if constexpr (H < 8) gelu_gap<(H >> 1), K + 18 * (H & 1)>(c);
  if constexpr (H < 2) {
    constexpr int G = K + 18 * H - 1;
    if constexpr (G >= 0 && G < 28) gelu_stage<G / 4, (G & 3)>(c.acc8[G / 16][(G / 4) & 3], c.gz8, c.gm8, c.hv8);
    if constexpr (G == 28) { gelu_stage<7, 0>(c.acc8[1][3], c.gz8, c.gm8, c.hv8); gelu_stage<7, 1>(c.acc8[1][3], c.gz8, c.gm8, c.hv8); }
    if constexpr (G == 29) { gelu_stage<7, 2>(c.acc8[1][3], c.gz8, c.gm8, c.hv8); gelu_stage<7, 3>(c.acc8[1][3], c.gz8, c.gm8, c.hv8); }
  }
  if constexpr (hst_at(H, K)) {
    if constexpr (K == 12) {
      const unsigned l = lane_id();
#pragma unroll
      for (int i = 0; i < 2; ++i) {
        auto rr = __builtin_amdgcn_permlane16_swap(c.hv8[i], c.hv8[2 + i], false, false);
        c.hv8[i] = rr[0]; c.hv8[2 + i] = rr[1];
      }
      store_h<0>(c, c.hv8, (((l >> 4) & 1u) ^ c.hf) * 1024u + ((l & 15u) + 16u * (l >> 5)) * 16u, c.hq[0] + 65536u - c.hf * 7680u);
    } else store_h<(K == 0 ? 0 : 1)>(c, c.own[H >> 1], c.vhld[K == 0 ? 0 : 1], c.hq[0] + (H >> 1) * 16384u);
  }
  if constexpr (pref_at(H, K))
    asm volatile("s_mov_b32 m0, %0\n\ts_nop 0\n\tbuffer_load_dword %1, %2, %3 offen lds" ::"s"(L_JUNK), "v"(c.vtch), "s"(c.s2), "s"((c.j * 1152u + H * 48u) * W2ROW));
}
constexpr int hpos(int n) {
  for (int h = 4; h < 14; ++h) for (int k = -2; k < 16; ++k) if (hload_at(h, k) == 2 * n + 1) return h * 32 + k + 2;
  return 0;
}
constexpr int cmax(int a, int b) { return a > b ? a : b; }
template <int H> DI void end_hs(Ctx& c) {
  if constexpr (H < 13) {
    constexpr int p0 = H == 0 ? -1 : H == 1 ? 8 : H == 12 ? 12 * 32 + 8 : (H - 2) * 32 + 17;
    constexpr int p1 = (H >= 5 && !own_unit(2 * (H - 5))) ? hpos(2 * (H - 5)) : 0, p2 = 0, p3 = flag_at(H + 1) ? (H - 1) * 32 + 17 : 0;
    constexpr int p = H == 0 ? -1 : cmax(cmax(p0, p1), cmax(p2, p3));
    if constexpr (H == 0) wait_vm<between(0, -3, 0, 16, 4) + ((W1PREF & 8) ? 1 : 0), between(0, -3, 0, 16, 4) + ((W1PREF & 8) ? 1 : 0)>(c);
    else wait_after<p / 32, p % 32 - 2, H, 16>(c);
    if constexpr (poll_k(H) > -9) check_poll<poll_ev(H), between(H, poll_k(H), H, 16, 4)>(c, c.pollv, H == 11);
    BARRIER_LGKM();
  }
  if constexpr (H == 12) {
#pragma unroll
    for (int n = 15; n < 18; ++n) { PIN(c.Hb[n & 7][0]); PIN(c.Hb[n & 7][1]); }
    PIN(c.b2v[0]); PIN(c.b2v[1]); PIN(c.b2v[2]);
  }
  if constexpr (H >= 5 && H < 12 && !own_unit(2 * (H - 5))) { PIN(c.Hb[(2 * (H - 5)) & 7][0]); PIN(c.Hb[(2 * (H - 5)) & 7][1]); }
  GAP();
}
template <int H> DI void mid_wait(Ctx& c) {
  if constexpr (H >= 6 && H < 13) {
    constexpr int p = hpos(2 * (H - 6) + 1);
    wait_after<p / 32, p % 32 - 2, H, 9>(c);
    PIN(c.Hb[(2 * (H - 6) + 1) & 7][0]); PIN(c.Hb[(2 * (H - 6) + 1) & 7][1]);
  }
}
#define GST(H, i) do {} while (0)
template <int H, int K> DI void step(Ctx& c) {
  GST(H, K + 2);
  if constexpr (K == 9) mid_wait<H>(c);
  if constexpr (K == KSW) asm volatile("s_setprio 0");
  mm<H, K>(c);
  if constexpr (K + 4 < 18) rd<H, K + 4>(c);
  else if constexpr (H == 13) rd<14, K - 14>(c);
  side<H, K>(c);
  GAP();
}
template <int H, int... Ks> DI void steps(Ctx& c, std::integer_sequence<int, Ks...>) { (step<H, Ks>(c), ...); }
DI void prio_young(const Ctx& c) {
  asm volatile("s_cmp_eq_u32 %0, 0\n\ts_cbranch_scc1 .Lskip%=\n\ts_setprio 1\n.Lskip%=:" ::"s"(c.hf) : "scc");
}
template <int H> DI void half_stage(Ctx& c) {
  if constexpr (H < 13) prio_young(c);
  set_bases<H>(c);
  if constexpr (H < 14) { rd<H, 0>(c); rd<H, 1>(c); }
  GAP();
  GST(H, 0);
  if constexpr (H > 0) mm<H - 1, 16>(c); else { mfma16(c.acc[0][0], c.F[2], c.X[34]); mfma16(c.acc[0][2], c.F[2], c.X[35]); }
  rd<H, 2>(c);
  side<H, -2>(c);
  GAP();
  GST(H, 1);
  if constexpr (H > 0) mm<H - 1, 17>(c); else { mfma16(c.acc[0][1], c.F[3], c.X[34]); mfma16(c.acc[0][3], c.F[3], c.X[35]); }
  rd<H, 3>(c);
  side<H, -1>(c);
  GAP();
  steps<H>(c, std::make_integer_sequence<int, 16>{});
  if constexpr (H == 14) { mm<14, 16>(c); mm<14, 17>(c); GAP(); asm volatile("s_nop 15" ::: "memory"); GAP(); }
  GST(H, 18);
  end_hs<H>(c);
  GST(H, 20);
}

struct B0 {
  unsigned vy, vw1, vc8, xa[4], l8, l80, lx, lw;
  bf16x8 G[2];
};
template <int S> DI void issue_y(Ctx& c, B0& b) {
  const unsigned dst = (S % 5) * SLICE + c.w * 1024u, so = WS_Y + c.r * 128u * XROW + S * 128u;
  asm volatile("s_mov_b32 m0, %0\n\ts_nop 0\n\tbuffer_load_dwordx4 %1, %2, %3 offen sc1 lds" ::"s"(dst), "v"(b.vy), "s"(c.sw), "s"(so));
  asm volatile("s_mov_b32 m0, %0\n\ts_nop 0\n\tbuffer_load_dwordx4 %1, %2, %3 offen sc1 lds" ::"s"(dst + 8192u), "v"(b.vy), "s"(c.sw), "s"(so + 64u * XROW));
}
template <int S> DI void issue_w(Ctx& c, B0& b) {
  const unsigned dst = (S % 5) * SLICE, kk = S * 64u;
  dma16(dst + c.w * 1024u + 16384u, b.vw1, c.s1, (kk + 8u * c.w) * W1ROW + c.freg2);
  asm volatile("s_cmp_lg_u32 %4, 0\n\ts_cbranch_scc1 .Lskip%=\n\ts_mov_b32 m0, %0\n\ts_nop 0\n\tbuffer_load_dwordx4 %1, %2, %3 offen lds\n.Lskip%=:"
               ::"s"(dst + c.t * 1024u + 24576u), "v"(b.vc8), "s"(c.s1), "s"((kk + 16u * c.t) * W1ROW + c.f82), "s"(c.hf) : "scc");
}
template <int S, int P> DI void rdx(Ctx& c, B0& b) {
  c.X[4 * S + 2 * P] = *LDSP(bf16x8, b.xa[2 * P]);
  c.X[4 * S + 2 * P + 1] = *LDSP(bf16x8, b.xa[2 * P + 1]);
}
template <int K> DI void rdf(Ctx& c) {
  const unsigned a = ((K & 1) ? c.wb2 : c.wb) + 4096u * (K >> 1);
  c.F[K] = cat(trr(a), trr(a + 512u));
}
template <int P> DI void rdg(B0& b) { b.G[P] = cat(trr(b.l8 + 2048u * P), trr(b.l8 + 2048u * P + 256u)); }
template <int S, int K> DI void mmA(Ctx& c) {
  mfma16(c.acc[0][K & 1], c.F[K], c.X[4 * S + (K & 2)]);
  mfma16(c.acc[0][2 + (K & 1)], c.F[K], c.X[4 * S + (K & 2) + 1]);
}
template <int S, int P> DI void mmB(Ctx& c, B0& b) {
  mfma16(c.acc8[0], b.G[P], c.X[4 * S + 2 * P]);
  mfma16(c.acc8[1], b.G[P], c.X[4 * S + 2 * P + 1]);
}
template <int S> DI void bases0(Ctx& c, B0& b) {
  constexpr unsigned base = (S % 5) * SLICE;
#pragma unroll
  for (int k = 0; k < 4; ++k) b.xa[k] = opq(base + (b.lx ^ (unsigned)((k >> 1) * 64 + (k & 1) * 2048)));
  c.wb = opq(base + 16384u + b.lw);
  c.wb2 = opq(c.wb ^ 32u);
  b.l8 = opq(base + 24576u + b.l80);
}
template <int S> DI void slice(Ctx& c, B0& b) {
  constexpr int n0 = 3, n = 4;
  constexpr int N = S == 0 ? 2 : S == 1 ? 4 : S == 2 ? 4 + n0 : S == 3 ? 2 + 2 * n0 : S <= 5 ? 3 * n0 : S == 6 ? 2 * n0 : S == 7 ? n0 : n;
  asm volatile("s_waitcnt vmcnt(%0) lgkmcnt(0)\n\ts_barrier" ::"i"(N) : "memory");
  STAMP(3 + S);
  GAP();
  if constexpr (S > 0) mmA<S - 1, 2>(c);
  rdx<S, 0>(c, b); rdf<0>(c);
  GAP();
  if constexpr (S > 0) mmA<S - 1, 3>(c);
  rdf<1>(c); rdg<0>(b);
  GAP();
  if constexpr (S > 0) mmB<S - 1, 1>(c, b);
  rdx<S, 1>(c, b); rdf<2>(c);
  if constexpr (S <= 1) issue_y<2 * S + 2>(c, b);
  if constexpr (S >= 2 && S <= 4) issue_y<S + 4>(c, b);
  if constexpr (S == 7) issue_slot<0>(c);
  if constexpr (S == 8) issue_slot<1>(c);
  GAP();
  mmA<S, 0>(c);
  rdf<3>(c); rdg<1>(b);
  GAP();
  mmA<S, 1>(c);
  if constexpr (S <= 1) issue_y<2 * S + 3>(c, b);
  if constexpr (S >= 1 && S <= 4) issue_w<S + 4>(c, b);
  if constexpr (S == 5) {
    const unsigned m = c.w * 7u + (lane_id() & 7u), ln = ((c.r & 7u) * 4u + c.q) * 54u + (m < 54u ? m : 53u);
    const unsigned mem = ln / 432u, rem = ln - mem * 432u, row = rem / 9u;
    c.vtch = (288u * mem + row) * W2ROW + (rem - row * 9u) * 128u;
    c.pref = poll_off(c, false);
  }
  if constexpr (S == 6) {
    const unsigned v0 = b.vw1 ^ (c.hf << 5);
#pragma unroll
    for (int i = 0; i < 4; ++i) c.vpat[i] = (v0 ^ ((i & 1) << 5)) + i * (8u * W1ROW - 1024u);
    c.vpat4 = v0 + 32u * W1ROW;
    bias_quad<0>(c.acc[1], c.tb + 256u); bias_quad<1>(c.acc[1], c.tb + 256u); bias_quad<2>(c.acc[1], c.tb + 256u); bias_quad<3>(c.acc[1], c.tb + 256u);
  }
  if constexpr (S < 8) bases0<S + 1>(c, b);
  GAP();
  mmB<S, 0>(c, b);
  GAP();
}
DI float half_sum(float v) {
  v = dpp_add<0x121>(v); v = dpp_add<0x122>(v); v = dpp_add<0x124>(v); v = dpp_add<0x128>(v);
  auto a = __builtin_amdgcn_permlane16_swap(__float_as_uint(v), __float_as_uint(v), false, false);
  return __uint_as_float(a[0]) + __uint_as_float(a[1]);
}
DI void scalars(Ctx& c) {
  c.freg2 = (1152u * c.j + fstart(c.q, 0)) * 2u;
  c.f82 = (1152u * c.j + fstart(c.q, 8)) * 2u;
  c.fown = WS_FLAG + (((c.r * 4u + c.j) * 4u + c.q) << 10);
  c.sw1 = (72u * c.t + 40u * c.hf) * W1ROW + c.freg2;
  c.ldst = c.t * 9216u + c.hf * 5120u;
}
template <int HF> DI void prologue(Ctx& c, B0& b, const srd_t sx, const srd_t sb1) {
  const unsigned l = threadIdx.x & 63u, g = l >> 4, qq = (l >> 2) & 3u, pp = l & 3u, h = l >> 5;
  unsigned epoch;
  if constexpr (HF == 0) {
    const unsigned row = (c.r * 128u + (c.j * 4u + c.q) * 8u + 2u * c.t) * XROW, va = h * XROW + (l & 31u) * 16u, vb = h * XROW + 1024u + (l & 31u) * 4u;
    u32x4 xa, xc; unsigned xb;
    asm volatile("buffer_load_dwordx4 %0, %1, %2, %3 offen" : "=v"(xa) : "v"(va), "s"(sx), "s"(row));
    asm volatile("buffer_load_dwordx4 %0, %1, %2, %3 offen offset:512" : "=v"(xc) : "v"(va), "s"(sx), "s"(row));
    asm volatile("buffer_load_dword %0, %1, %2, %3 offen" : "=v"(xb) : "v"(vb), "s"(sx), "s"(row));
    asm volatile("buffer_load_dword %0, off, %1, %2 sc1" : "=v"(epoch) : "s"(c.sw), "s"(WS_EPOCH + c.r * 128u));
    GAP();
    scalars(c);
    b.vw1 = (l >> 3) * W1ROW + (((l & 7u) ^ ((g & 1u) << 2) ^ ((c.w & 1u) << 1)) << 4);
    b.vc8 = (l >> 2) * W1ROW + ((pp ^ (h << 1)) << 4);
    GAP();
    asm volatile("s_waitcnt vmcnt(1)" : "+v"(xa), "+v"(xb), "+v"(xc)::"memory");
    float v[18];
#pragma unroll
    for (int i = 0; i < 4; ++i) {
      v[2 * i] = __uint_as_float(xa[i] << 16); v[2 * i + 1] = __uint_as_float(xa[i] & 0xffff0000u);
      v[8 + 2 * i] = __uint_as_float(xc[i] << 16); v[9 + 2 * i] = __uint_as_float(xc[i] & 0xffff0000u);
    }
    v[16] = __uint_as_float(xb << 16); v[17] = __uint_as_float(xb & 0xffff0000u);
    float s0 = v[16], s1 = v[17];
#pragma unroll
    for (int i = 0; i < 8; ++i) { s0 += v[2 * i]; s1 += v[2 * i + 1]; }
    const float mean = half_sum(s0 + s1) * (1.0f / 576.0f);
#pragma unroll
    for (int i = 0; i < 18; ++i) v[i] -= mean;
    s0 = v[16] * v[16]; s1 = v[17] * v[17];
#pragma unroll
    for (int i = 0; i < 8; ++i) { s0 = __builtin_fmaf(v[2 * i], v[2 * i], s0); s1 = __builtin_fmaf(v[2 * i + 1], v[2 * i + 1], s1); }
    const float rs = KAP * __builtin_amdgcn_rsqf(half_sum(s0 + s1) * (1.0f / 576.0f) + 1e-5f);
    u32x4 ya, yc;
#pragma unroll
    for (int i = 0; i < 4; ++i) { ya[i] = cvtpk(v[2 * i] * rs, v[2 * i + 1] * rs); yc[i] = cvtpk(v[8 + 2 * i] * rs, v[9 + 2 * i] * rs); }
    const unsigned yb = cvtpk(v[16] * rs, v[17] * rs);
    asm volatile("buffer_store_dwordx4 %0, %1, %2, %3 offen sc1" ::"v"(ya), "v"(va), "s"(c.sw), "s"(WS_Y + row));
    asm volatile("buffer_store_dwordx4 %0, %1, %2, %3 offen offset:512 sc1" ::"v"(yc), "v"(va), "s"(c.sw), "s"(WS_Y + row));
    asm volatile("buffer_store_dword %0, %1, %2, %3 offen sc1\n\ts_nop 1" ::"v"(yb), "v"(vb), "s"(c.sw), "s"(WS_Y + row));
    GAP();
    STAMP(1);
  b.vy = ((l >> 3) + 8u * c.w) * XROW + (((l & 7u) ^ ((g + 4u * c.w) & 7u)) << 4);
  b.lw = (8u * g + qq) * 128u + (((4u * HF + 2u * (g & 1u) + (pp >> 1)) ^ ((qq >> 1) << 2)) << 4) + 8u * (pp & 1u);
  const unsigned xrow = 32u * c.t + 16u * HF + (l & 15u);
  b.lx = xrow * 128u + ((g ^ ((xrow >> 1) & 7u)) << 4);
  b.l80 = (8u * g + qq) * 64u + ((HF ^ (g & 1u)) << 5) + 8u * pp;
  c.tb = L_B1 + HF * 128u + g * 16u;
  c.vhld[0] = l * 16u + HF * 1024u; c.vhld[1] = c.vhld[0] ^ 1024u;
  bases0<0>(c, b);
    GAP();
    asm volatile("s_waitcnt vmcnt(0)\n\ts_barrier" : "+v"(epoch)::"memory");
    c.tag = rfl(epoch) + 1u;
    STAMP(31);
    store_flag<1>(c);
    issue_w<0>(c, b); issue_w<1>(c, b); issue_w<2>(c, b); issue_w<3>(c, b); issue_w<4>(c, b);
  } else {
    unsigned b1a, b1b;
    scalars(c);
    b.vw1 = (l >> 3) * W1ROW + (((l & 7u) ^ ((g & 1u) << 2) ^ ((c.w & 1u) << 1)) << 4);
    b.vc8 = (l >> 2) * W1ROW + ((pp ^ (h << 1)) << 4);
    if (HDELAY) __builtin_amdgcn_s_sleep(HDELAY);
    asm volatile("buffer_load_dword %0, off, %1, %2 sc1" : "=v"(epoch) : "s"(c.sw), "s"(WS_EPOCH + c.r * 128u));
    asm volatile("buffer_load_ushort %0, %1, %2, %3 offen" : "=v"(b1a) : "v"((c.t * 64u + l) * 2u), "s"(sb1), "s"(c.freg2));
    asm volatile("buffer_load_ushort %0, %1, %2, %3 offen" : "=v"(b1b) : "v"((l & 31u) * 2u), "s"(sb1), "s"(c.f82));
    issue_w<0>(c, b); issue_w<1>(c, b); issue_w<2>(c, b); issue_w<3>(c, b); issue_w<4>(c, b);
    const unsigned uln = (c.r * 4u + c.q) + 64u * (c.t * 54u + (l < 54u ? l : 53u));
    STAMP(1);
  b.vy = ((l >> 3) + 8u * c.w) * XROW + (((l & 7u) ^ ((g + 4u * c.w) & 7u)) << 4);
  b.lw = (8u * g + qq) * 128u + (((4u * HF + 2u * (g & 1u) + (pp >> 1)) ^ ((qq >> 1) << 2)) << 4) + 8u * (pp & 1u);
  const unsigned xrow = 32u * c.t + 16u * HF + (l & 15u);
  b.lx = xrow * 128u + ((g ^ ((xrow >> 1) & 7u)) << 4);
  b.l80 = (8u * g + qq) * 64u + ((HF ^ (g & 1u)) << 5) + 8u * pp;
  c.tb = L_B1 + HF * 128u + g * 16u;
  c.vhld[0] = l * 16u + HF * 1024u; c.vhld[1] = c.vhld[0] ^ 1024u;
  bases0<0>(c, b);
#pragma unroll
    for (int k = 0; k < 3; ++k) {
      const unsigned i = k < 2 ? 4u * k + c.t : 8u, nn = 64u * i + l, rho = nn / 18u, chn = nn - rho * 18u;
      const unsigned rel = i < 5u ? i : i - 5u, o = chn * 16u - (rel < 4u ? rel * 1024u : 0u);
      *LDSP(unsigned, L_PAT + i * 256u + l * 4u) = rho * W2ROW + o;
      *LDSP(unsigned, L_PAT + 2304u + i * 256u + l * 4u) = ((rho & 3u) | ((rho & 12u) << 1) | ((rho & 16u) >> 2)) * W2ROW + o;
    }
    asm volatile("s_waitcnt vmcnt(%3)" : "+v"(b1a), "+v"(b1b), "+v"(epoch) : "i"(5) : "memory");
    c.tag = rfl(epoch) + 1u;
    *LDSP(float, L_B1 + (c.t * 64u + l) * 4u) = __uint_as_float(b1a << 16) * KAP;
    *LDSP(float, L_B1 + 1024u + (l & 31u) * 4u) = __uint_as_float(b1b << 16) * KAP;
    *LDSP(unsigned, L_PROG + (l & 7u) * 4u) = 0u;
    asm volatile("s_barrier" ::: "memory");
    STAMP(31);
    if (c.t < 3) {
      const unsigned rw = uln / 18u;
      asm volatile("s_mov_b32 m0, %0\n\ts_nop 0\n\tbuffer_load_dword %1, %2, %3 offen lds" ::"s"(L_JUNK),
                   "v"(rw * W1ROW + (uln - rw * 18u) * 128u), "s"(c.s1), "s"(c.j * 2304u));
    }
    asm volatile("mixer_code_end:");
    if (c.w == 7) {
      const unsigned voff = WS_FLAG + 128u + ((c.r * 16u + (l & 15u)) << 10);
      __builtin_amdgcn_s_sleep(PDELAY);
      for (unsigned spin = 0; spin < (1u << 16); ++spin) {
        unsigned pv;
        asm volatile("s_sleep %3\n\tbuffer_load_dword %0, %1, %2, 0 offen sc1\n\ts_waitcnt vmcnt(0)" : "=&v"(pv) : "v"(voff), "s"(c.sw), "i"(PSLEEP) : "memory");
        if (__builtin_amdgcn_ballot_w64(pv != c.tag) == 0) break;
      }
    }
  }
}
DI void block0(Ctx& c, B0& b) {
  BARRIER_LGKM();
  STAMP(2);
  issue_y<0>(c, b); issue_y<1>(c, b);
  bias_quad<0>(c.acc[0], c.tb); bias_quad<1>(c.acc[0], c.tb); bias_quad<2>(c.acc[0], c.tb); bias_quad<3>(c.acc[0], c.tb);
  c.acc8[0] = *LDSP(volatile f32x4, c.tb + 1024u - c.hf * 64u); c.acc8[1] = *LDSP(volatile f32x4, c.tb + 1024u - c.hf * 64u);
  slice<0>(c, b); slice<1>(c, b); slice<2>(c, b); slice<3>(c, b); slice<4>(c, b);
  slice<5>(c, b); slice<6>(c, b); slice<7>(c, b); slice<8>(c, b);
  {
    const unsigned l = lane_id(), m = c.w * 21u + (l < 21u ? l : 20u);
    asm volatile("s_mov_b32 m0, %0\n\ts_nop 0\n\tbuffer_load_dword %1, %2, %3 offen lds" ::"s"(L_JUNK),
                 "v"(((c.r * 4u + c.q) + 64u * (m < 162u ? m : 161u)) * 128u), "s"(c.s2), "s"(c.j * 1152u * W2ROW));
  }
  mmB<8, 1>(c, b);
  c.wb = opq(b.lw);
  asm volatile("s_waitcnt vmcnt(%0) lgkmcnt(0)\n\ts_barrier" ::"i"(4 + ((W1PREF & 8) ? 1 : 0)) : "memory");
}

__global__ __launch_bounds__(512, 2) void mixer_mlp_kernel(const void* __restrict__ x, const void* __restrict__ w1, const void* __restrict__ b1,
                                                           const void* __restrict__ w2, const void* __restrict__ b2, void* __restrict__ out,
                                                           void* __restrict__ ws) {
  Ctx c;
  {
    unsigned long long pc; unsigned off;
    asm volatile("s_getpc_b64 %0\nmixer_code_begin:\n\tv_lshlrev_b32 %1, 7, %2\n\tv_cmp_lt_u32 vcc, 0x7fff, %1\n\ts_cbranch_vccz .Lskip%=\n\t"
                 "v_and_b32 %1, 0x7fff, %1\n\tv_min_u32 %1, mixer_code_end-mixer_code_begin-4, %1\n\ts_mov_b32 m0, %3\n\ts_nop 0\n\t"
                 "global_load_lds_dword %1, %0\n.Lskip%=:"
                 : "=&s"(pc), "=&v"(off) : "v"(threadIdx.x), "i"(L_JUNK) : "vcc");
  }
  const unsigned blk = blockIdx.x, xg = blk & 7u, idx = blk >> 3;
  c.r = 8u * (xg >> 2) + (idx >> 2); c.j = xg & 3u; c.q = idx & 3u;
  c.w = rfl(threadIdx.x >> 6); c.t = c.w & 3u; c.hf = c.w >> 2;
  STAMP(0);
  srd_t sx = __builtin_amdgcn_make_buffer_rsrc((void*)x, 0, S_ * D_ * 2, 0x00020000);
  asm volatile("" : "+s"(sx));
  c.s1 = __builtin_amdgcn_make_buffer_rsrc((void*)w1, 0, D_ * F_ * 2, 0x00020000);
  c.s2 = __builtin_amdgcn_make_buffer_rsrc((void*)w2, 0, D_ * F_ * 2, 0x00020000);
  c.sw = __builtin_amdgcn_make_buffer_rsrc(ws, 0, WS_USED, 0x00020000);
  c.sb2 = __builtin_amdgcn_make_buffer_rsrc((void*)b2, 0, D_ * 2, 0x00020000);
  const srd_t sb1 = __builtin_amdgcn_make_buffer_rsrc((void*)b1, 0, F_ * 2, 0x00020000);
  B0 b0;
  if (__builtin_expect(c.hf == 0, 1)) prologue<0>(c, b0, sx, sb1); else prologue<1>(c, b0, sx, sb1);
  scalars(c);
  c.pollv = 0u;
  asm volatile("" : "+s"(c.sw1), "+s"(c.ldst), "+s"(c.freg2), "+s"(c.f82), "+s"(c.fown), "+s"(c.r), "+s"(c.w), "+s"(c.t));
  block0(c, b0);
  {
    const unsigned hb = WS_H + ((c.r * 4u + c.j) * 144u + c.t) * 2048u;
#pragma unroll
    for (int d = 0; d < 4; ++d) c.hq[d] = hb + ((c.q + d) & 3u) * 73728u + c.hf * 8192u;
#pragma unroll
    for (int e = 0; e < 2; ++e) {
      c.h8[e] = hb + ((c.q + 2u * c.hf + e) & 3u) * 73728u + 65536u;
      c.sw2[e] = (1152u * c.j + fstart((c.q + 2u * e + (c.t & 1u)) & 3u, c.t >> 1)) * W2ROW + 288u * c.q;
    }
    c.sw28 = (1152u * c.j + fstart((c.q + c.t) & 3u, 8)) * W2ROW + 288u * c.q;
  }
  asm volatile("" : "+s"(c.sw1), "+s"(c.ldst), "+s"(c.hq[0]), "+s"(c.hq[1]), "+s"(c.hq[2]), "+s"(c.hq[3]), "+s"(c.h8[0]), "+s"(c.h8[1]),
               "+s"(c.sw2[0]), "+s"(c.sw2[1]), "+s"(c.sw28));
  STAMP(12);
  half_stage<0>(c); STAMP(13);
  half_stage<1>(c); STAMP(14);
  half_stage<2>(c); STAMP(15);
  half_stage<3>(c); STAMP(16);
  half_stage<4>(c); STAMP(17);
  half_stage<5>(c); STAMP(18);
  half_stage<6>(c); STAMP(19);
  half_stage<7>(c); STAMP(20);
  half_stage<8>(c); STAMP(21);
  half_stage<9>(c); STAMP(22);
  half_stage<10>(c); STAMP(23);
  half_stage<11>(c); STAMP(24);
  half_stage<12>(c); STAMP(25);
  half_stage<13>(c); STAMP(26);
  half_stage<14>(c); STAMP(27);

  const unsigned l = lane_id();
  comb<4, 0>(c); comb<4, 1>(c);
  {
    for (unsigned spin = 0; spin < (1u << 20); ++spin)
      if (rfl(*LDSP(volatile unsigned, c.vpg ^ 16u)) > 4u) break;
    asm volatile("" ::: "memory");
    f32x4 g8 = ldsf4(c.xr + 8192u);
    c.xg[0] = ldsf4(c.xr + 6144u); c.xg[1] = ldsf4(c.xr + 7168u);
    comb<3, 4>(c); comb<3, 5>(c); comb<3, 6>(c);
    c.xg[0] = g8;
    comb<4, 4>(c); comb<4, 5>(c); comb<4, 6>(c);
  }
  BARRIER_LGKM();
  STAMP(28);
  {
    const u32x2 bA = c.b2v[0], bB = c.b2v[1], bC = c.b2v[2];
    const unsigned sp = WS_P + ((c.r * 4u + c.q) * 4u + c.j) * 4u * 9216u + c.hf * 4608u;
    const unsigned s0 = sp + ((c.j + 1u) & 3u) * 9216u, s1 = sp + ((c.j + 2u) & 3u) * 9216u, s2 = sp + ((c.j + 3u) & 3u) * 9216u;
    const unsigned va = l * 16u + c.t * 1024u, vb = 4096u + l * 8u, oa = (c.hf ^ 1u) * 3u * SLOT + c.j * 9216u + l * 16u;
    u32x4 pa[3]; u32x2 pb[3];
    f32x4 o0 = ldsf4(oa + c.t * 2048u), o1 = ldsf4(oa + c.t * 2048u + 1024u), o2 = ldsf4(oa + 8192u);
    if (c.t == 3) __builtin_amdgcn_s_sleep(TDELAY);
    for (unsigned spin = 0; spin < (1u << 16); ++spin) {
      asm volatile("buffer_load_dwordx4 %0, %6, %8, %9 offen sc1\n\tbuffer_load_dwordx4 %1, %6, %8, %10 offen sc1\n\t"
                   "buffer_load_dwordx4 %2, %6, %8, %11 offen sc1\n\t"
                   "s_cmp_lg_u32 %12, 3\n\ts_cbranch_scc1 .Lskip%=\n\t"
                   "buffer_load_dwordx2 %3, %7, %8, %9 offen sc1\n\tbuffer_load_dwordx2 %4, %7, %8, %10 offen sc1\n\t"
                   "buffer_load_dwordx2 %5, %7, %8, %11 offen sc1\n.Lskip%=:\n\ts_waitcnt vmcnt(0)"
                   : "=&v"(pa[0]), "=&v"(pa[1]), "=&v"(pa[2]), "=&v"(pb[0]), "=&v"(pb[1]), "=&v"(pb[2])
                   : "v"(va), "v"(vb), "s"(c.sw), "s"(s0), "s"(s1), "s"(s2), "s"(c.t) : "memory", "scc");
      unsigned mx = 0;
#pragma unroll
      for (int k = 0; k < 3; ++k) {
#pragma unroll
        for (int i = 0; i < 4; ++i) mx = mx > pa[k][i] ? mx : pa[k][i];
        if (c.t == 3) { mx = mx > pb[k][0] ? mx : pb[k][0]; mx = mx > pb[k][1] ? mx : pb[k][1]; }
      }
      if (!__any(mx == ~0u)) break;
      __builtin_amdgcn_s_sleep(2);
    }
    STAMP(29);
#pragma unroll
    for (int k = 0; k < 3; ++k)
#pragma unroll
      for (int i = 0; i < 2; ++i) {
        o0[2 * i] += __uint_as_float(pa[k][i] << 16); o0[2 * i + 1] += __uint_as_float(pa[k][i] & 0xffff0000u);
        o1[2 * i] += __uint_as_float(pa[k][2 + i] << 16); o1[2 * i + 1] += __uint_as_float(pa[k][2 + i] & 0xffff0000u);
      }
    unsigned short* op = (unsigned short*)out + (size_t)(128u * c.r + 32u * c.j + 16u * c.hf + (l & 15u)) * D_ + 144u * c.q;
    const unsigned e0 = cvtpk(__builtin_fmaf(o0[0], RKAP, __uint_as_float(bA[0] << 16)), __builtin_fmaf(o0[1], RKAP, __uint_as_float(bA[0] & 0xffff0000u)));
    const unsigned e1 = cvtpk(__builtin_fmaf(o0[2], RKAP, __uint_as_float(bA[1] << 16)), __builtin_fmaf(o0[3], RKAP, __uint_as_float(bA[1] & 0xffff0000u)));
    const unsigned f0 = cvtpk(__builtin_fmaf(o1[0], RKAP, __uint_as_float(bB[0] << 16)), __builtin_fmaf(o1[1], RKAP, __uint_as_float(bB[0] & 0xffff0000u)));
    const unsigned f1 = cvtpk(__builtin_fmaf(o1[2], RKAP, __uint_as_float(bB[1] << 16)), __builtin_fmaf(o1[3], RKAP, __uint_as_float(bB[1] & 0xffff0000u)));
    auto r0 = __builtin_amdgcn_permlane16_swap(e0, f0, false, false);
    auto r1 = __builtin_amdgcn_permlane16_swap(e1, f1, false, false);
    *(u32x4*)(op + 32u * c.t + 16u * ((l >> 4) & 1u) + 8u * (l >> 5)) = (u32x4){r0[0], r1[0], r0[1], r1[1]};
    if (c.t == 3) {
#pragma unroll
      for (int k = 0; k < 3; ++k)
#pragma unroll
        for (int i = 0; i < 2; ++i) { o2[2 * i] += __uint_as_float(pb[k][i] << 16); o2[2 * i + 1] += __uint_as_float(pb[k][i] & 0xffff0000u); }
      *(u32x2*)(op + 128u + 4u * (l >> 4)) =
          (u32x2){cvtpk(__builtin_fmaf(o2[0], RKAP, __uint_as_float(bC[0] << 16)), __builtin_fmaf(o2[1], RKAP, __uint_as_float(bC[0] & 0xffff0000u))),
                  cvtpk(__builtin_fmaf(o2[2], RKAP, __uint_as_float(bC[1] << 16)), __builtin_fmaf(o2[3], RKAP, __uint_as_float(bC[1] & 0xffff0000u)))};
    }
    if (c.w == 0 && c.j == 0 && c.q == 0 && l == 0)
      asm volatile("buffer_store_dword %0, off, %1, %2 sc1" ::"v"(c.tag), "s"(c.sw), "s"(WS_EPOCH + c.r * 128u));
  }
}

extern "C" void kernel_launch(void* const* d_in, const int* in_sizes, int n_in, void* d_out, int out_size, void* d_ws, size_t ws_size,
                              hipStream_t stream) {
  (void)in_sizes; (void)n_in; (void)out_size; (void)ws_size;
  (void)hipFuncSetAttribute((const void*)mixer_mlp_kernel, hipFuncAttributeMaxDynamicSharedMemorySize, LDS_BYTES);
  mixer_mlp_kernel<<<256, 512, LDS_BYTES, stream>>>(d_in[0], d_in[1], d_in[2], d_in[3], d_in[4], d_out, d_ws);
}
```

```cpp
#include <hip/hip_runtime.h>
#define THR 512
#define LDSB 155000
__device__ __forceinline__ float bf(unsigned short u){ return __uint_as_float((unsigned)u<<16); }
__device__ __forceinline__ unsigned short fb(float f){ __bf16 b=(__bf16)f; return __builtin_bit_cast(unsigned short,b); }
__global__ __launch_bounds__(THR) void k(const unsigned short* x,const unsigned short* w1,const unsigned short* b1,const unsigned short* w2,const unsigned short* b2,unsigned short* out){
  const unsigned long long rtb=__builtin_amdgcn_s_memrealtime();
  extern __shared__ char sm[];
  unsigned short* h=(unsigned short*)sm; float* y=(float*)(sm+73728);
  const int tid=threadIdx.x, r0=blockIdx.x*8;
  if(tid<8){ float s=0; for(int i=0;i<576;i++) s+=bf(x[(r0+tid)*576+i]); float m=s/576,q=0; for(int i=0;i<576;i++){ float d=bf(x[(r0+tid)*576+i])-m; q+=d*d; }
    float r=rsqrtf(q/576+1e-5f); for(int i=0;i<576;i++) y[tid*576+i]=bf(fb((bf(x[(r0+tid)*576+i])-m)*r)); }
  __syncthreads();
  for(int f=tid;f<4608;f+=THR){ float a[8]={}; for(int kk=0;kk<576;kk++){ float w=bf(w1[kk*4608+f]); for(int i=0;i<8;i++) a[i]+=y[i*576+kk]*w; }
    for(int i=0;i<8;i++){ float v=a[i]+bf(b1[f]); float u=0.7978845608f*(v+0.044715f*v*v*v); h[i*4608+f]=fb(v/(1.f+__expf(-2.f*u))); } }
  __syncthreads();
  for(int d=tid;d<576;d+=THR){ float a[8]={}; for(int f=0;f<4608;f++){ float w=bf(w2[f*576+d]); for(int i=0;i<8;i++) a[i]+=bf(h[i*4608+f])*w; }
    for(int i=0;i<8;i++) out[(r0+i)*576+d]=fb(a[i]+bf(b2[d])); }
  while(__builtin_amdgcn_s_memrealtime()-rtb<400000ull) __builtin_amdgcn_s_sleep(1);
}
extern "C" void kernel_launch(void* const* d_in,const int*,int,void* d_out,int,void*,size_t,hipStream_t st){
  (void)hipFuncSetAttribute((const void*)k,hipFuncAttributeMaxDynamicSharedMemorySize,LDSB);
  k<<<256,THR,LDSB,st>>>((const unsigned short*)d_in[0],(const unsigned short*)d_in[1],(const unsigned short*)d_in[2],(const unsigned short*)d_in[3],(const unsigned short*)d_in[4],(unsigned short*)d_out);
}
```

```cpp
#include <hip/hip_runtime.h>
#include <stdint.h>
#include <utility>

typedef __bf16 bf16x8 __attribute__((ext_vector_type(8)));
typedef __bf16 bf16x2 __attribute__((ext_vector_type(2)));
typedef short s16x4 __attribute__((ext_vector_type(4)));
typedef float f32x4 __attribute__((ext_vector_type(4)));
typedef float f32x2 __attribute__((ext_vector_type(2)));
typedef unsigned u32x4 __attribute__((ext_vector_type(4)));
typedef unsigned u32x2 __attribute__((ext_vector_type(2)));
typedef __amdgpu_buffer_rsrc_t srd_t;
#define DI __device__ __forceinline__
#define LAS __attribute__((address_space(3)))
#define LDSP(T, a) ((LAS T*)(size_t)(a))

constexpr unsigned S_ = 2048, D_ = 576, F_ = 4608;
constexpr unsigned XROW = D_ * 2, W1ROW = F_ * 2, W2ROW = D_ * 2;
constexpr unsigned SLOT = 36864, RING = 4 * SLOT, SLICE = 28672;
constexpr unsigned L_B1 = RING, L_PAT = L_B1 + 1152, L_JUNK = L_PAT + 4608, L_PROG = L_JUNK + 256, L_B2 = L_PROG + 64,
                   LDS_BYTES = L_B2 + 1024;
static_assert(LDS_BYTES <= 163840, "LDS");
constexpr unsigned WS_EPOCH = 0, WS_FLAG = 4096, WS_Y = WS_FLAG + 256 * 8 * 128, WS_H = WS_Y + S_ * D_ * 2, H_BYTES = S_ * F_ * 2;
constexpr unsigned WS_P = WS_H + H_BYTES, P_BYTES = 16 * 4 * 4 * 4 * 9216;
constexpr unsigned WS_USED = WS_P + P_BYTES;
constexpr float KAP = 0.4686686930532063f, GAM = 4.91222953926424f, RKAP = 2.1337034344781243f;

DI unsigned rfl(unsigned x) { return __builtin_amdgcn_readfirstlane(x); }
DI s16x4 trr(unsigned a) { return __builtin_amdgcn_ds_read_tr16_b64_v4i16(LDSP(s16x4, a)); }
DI bf16x8 cat(s16x4 lo, s16x4 hi) { return __builtin_bit_cast(bf16x8, __builtin_shufflevector(lo, hi, 0, 1, 2, 3, 4, 5, 6, 7)); }
DI f32x4 ldsf4(unsigned a) { return *LDSP(f32x4, a); }
DI unsigned cvtpk(float lo, float hi) { f32x2 v = {lo, hi}; return __builtin_bit_cast(unsigned, __builtin_convertvector(v, bf16x2)); }
template <int CTRL> DI float dpp_add(float v) {
  return v + __builtin_bit_cast(float, __builtin_amdgcn_update_dpp(0, __builtin_bit_cast(int, v), CTRL, 0xf, 0xf, false));
}
DI void mfma16(f32x4& acc, bf16x8 a, bf16x8 b) { asm volatile("v_mfma_f32_16x16x32_bf16 %0, %1, %2, %0" : "+v"(acc) : "v"(a), "v"(b)); }
DI void mfma16z(f32x4& acc, bf16x8 a, bf16x8 b) { asm volatile("v_mfma_f32_16x16x32_bf16 %0, %1, %2, 0" : "=&v"(acc) : "v"(a), "v"(b)); }
#define BARRIER_LGKM() asm volatile("s_waitcnt lgkmcnt(0)\n\ts_barrier" ::: "memory")
#define GAP() __builtin_amdgcn_sched_barrier(0)
#define PIN(x) asm volatile("" : "+v"(x))

struct Ctx {
  srd_t s1, s2, sw;
  unsigned w, t, hf, r, j, q;
  unsigned freg2, f82;
  unsigned tag, sdma, sw1, ldst, hq[4], h8[2], sw2[2], sw28, fown;
  u32x4 vpat;
  unsigned vpat4;
  unsigned wb, wb2;
  unsigned vhld[2], tb, pref, vtch, xw, xr, pg, sp, vpg;
  u32x4 pk;
  f32x4 xg[2];
  float gz[2], gm, gz8, gm8;
  unsigned own[4][8], hv8[8];
  bf16x8 X[36];
  bf16x8 F[4];
  f32x4 acc[2][4];
  f32x4 acc8[2];
  f32x4 acc2[2][9];
  u32x4 Hb[8][2];
  unsigned pollv;
};
DI unsigned lane_id() {
  unsigned l;
  asm volatile("v_mbcnt_lo_u32_b32 %0, -1, 0\n\tv_mbcnt_hi_u32_b32 %0, -1, %0" : "=v"(l));
  return l;
}
DI unsigned opq(unsigned v) { PIN(v); return v; }

constexpr int pair_of(int h) { return h < 8 ? 0 : (h - 7) / 2; }
constexpr int ph_of(int h) { return h < 8 ? h - 6 : (h - 7) % 2; }
constexpr bool own_unit(int n) { return n == 0 || n == 6 || n == 10 || n == 14; }
constexpr int hload_at(int h, int k) {
  if (h == 5) return k == 2 ? 2 : k == 5 ? 3 : k == 10 ? 4 : k == 12 ? 5 : -1;
  if (h == 6) return k == 0 ? 6 : k == 2 ? 7 : k == 10 ? 8 : k == 12 ? 9 : -1;
  if (h == 7) return k == 2 ? 10 : k == 10 ? 11 : -1;
  if (h == 8) return k == 0 ? 14 : k == 2 ? 15 : k == 12 ? 16 : -1;
  if (h == 9) return k == 0 ? 17 : k == 2 ? 18 : k == 10 ? 19 : -1;
  if (h == 10) return k == 0 ? 22 : k == 2 ? 23 : k == 10 ? 24 : k == 12 ? 25 : -1;
  if (h == 11) return k == 0 ? 30 : k == 2 ? 31 : k == 10 ? 26 : k == 12 ? 27 : -1;
  if (h == 12) return k == -2 ? 32 : k == 0 ? 33 : k == 2 ? 34 : k == 4 ? 35 : -1;
  return -1;
}
constexpr int dma_at(int h, int k) {
  if (h == 0) return (k < 16 && (k & 1) == 0) ? (k < 8 ? 2 * 8 + (k + 2) / 2 : 3 * 8 + (k - 8) / 2) : (k == 15 ? 3 * 8 + 4 : -1);
  if (h + 3 > 14) return -1;
  const int i = h < 4 ? (k == -2 ? 0 : k == 2 ? 1 : k == 6 ? 2 : k == 10 ? 3 : k == 13 ? 4 : -1)
                      : (k == -2 ? 0 : k == 4 ? 1 : k == 6 ? 2 : k == 8 ? 3 : k == 14 ? 4 : -1);
  return i < 0 ? -1 : (h + 3) * 8 + i;
}
constexpr int poll_k(int h) { return h == 4 ? 5 : h == 10 ? 5 : (h == 7 || h == 9 || h == 11) ? -1 : -9; }
constexpr int poll_ev(int h) { return h == 4 ? 2 : h == 7 ? 3 : h == 10 ? 5 : 4; }
constexpr bool poll_at(int h, int k) { return k == poll_k(h); }
constexpr int flag_at(int h) { return (h == 3 || h == 5 || h == 7 || h == 9) ? (h + 1) / 2 : 0; }
constexpr bool hst_at(int h, int k) { return (h < 8 && (h & 1) && (k == 0 || k == 15)) || (h == 1 && k == 12); }
constexpr bool pref_at(int h, int k) { return h < 6 && k == 15; }
constexpr bool mark_at(int h, int k) { return (h == 2 && (k == 4 || k == 12)) || (h == 4 && k == 2); }
constexpr int nops(int h, int k, int n) {
  return (hload_at(h, k) >= 0 ? 1 : 0) + (hst_at(h, k) ? 1 : 0) + (mark_at(h, k) ? 1 : 0) +
         (dma_at(h, k) >= 0 && (dma_at(h, k) & 7) < n ? 1 : 0);
}
constexpr int between(int h0, int k0, int h1, int k1, int n) {
  int s = 0;
  for (int h = h0; h <= h1; ++h)
    for (int k = -2; k < 16; ++k)
      if ((h > h0 || k > k0) && (h < h1 || k < k1)) s += nops(h, k, n);
  for (int h = h0; h <= h1; ++h)
    if (pref_at(h, 15) && (h < h1 || k1 == 16)) s += 1;
  return s;
}

DI unsigned fstart(unsigned qq, unsigned ch) { return 32u * (9u * qq + ch + (qq & 1u) * (1u - 9u * (ch >> 3))); }

DI void dma16(unsigned m0v, unsigned voff, srd_t srd, unsigned soff) {
  asm volatile("s_mov_b32 m0, %0\n\ts_nop 0\n\tbuffer_load_dwordx4 %1, %2, %3 offen lds" ::"s"(m0v), "v"(voff), "s"(srd), "s"(soff));
}
template <int HS, int I> DI void issue_piece(Ctx& c) {
  const srd_t sd = HS < 6 ? c.s1 : c.s2;
  if constexpr (I == 0) {
    if constexpr (HS < 6) c.sdma = c.sw1 + ((HS & 1) * 288u * W1ROW + ((HS >> 1) + 1) * 128u);
    else c.sdma = HS == 8 ? c.sw28 : c.sw2[ph_of(HS)] + pair_of(HS) * 64u * W2ROW;
    dma16((HS & 3) * SLOT + c.ldst, c.vpat[0], sd, c.sdma);
  } else if constexpr (I < 4) {
    asm volatile("buffer_load_dwordx4 %0, %1, %2 offen offset:%3 lds" ::"v"(c.vpat[I]), "s"(sd), "s"(c.sdma), "i"(1024 * I));
  } else {
    asm volatile("s_cmp_lg_u32 %4, 0\n\ts_cbranch_scc1 .Lskip%=\n\ts_mov_b32 m0, %0\n\ts_nop 0\n\tbuffer_load_dwordx4 %1, %2, %3 offen lds\n.Lskip%=:"
                 ::"s"((HS & 3) * SLOT + 4096u + c.ldst), "v"(c.vpat4), "s"(sd), "s"(c.sdma), "s"(c.hf) : "scc");
  }
}
template <int HS> DI void issue_slot(Ctx& c) {
  issue_piece<HS, 0>(c); issue_piece<HS, 1>(c); issue_piece<HS, 2>(c); issue_piece<HS, 3>(c); issue_piece<HS, 4>(c);
}
template <int N, int U> DI void issue_hload(Ctx& c) {
  constexpr int h = 6 + N / 2;
  const unsigned so = h == 8 ? c.h8[N & 1] : c.hq[2 * ph_of(h) + (N & 1)] + pair_of(h) * 16384u;
  asm volatile("buffer_load_dwordx4 %0, %1, %2, %3 offen sc1" : "=v"(c.Hb[N & 7][U]) : "v"(c.vhld[U]), "s"(c.sw), "s"(so));
}
DI unsigned poll_off(const Ctx& c, bool dest) {
  const unsigned l = lane_id(), dp = (l & 3u) ? (l & 3u) : 1u;
  return WS_FLAG + ((dest ? (c.r * 4u + ((c.j + dp) & 3u)) * 4u + c.q : (c.r * 4u + c.j) * 4u + ((c.q + dp) & 3u)) << 10);
}
template <int EV> DI void store_flag(Ctx& c) {
  const u32x4 v = {c.tag, c.tag, c.tag, c.tag};
  asm volatile("s_cmp_lg_u32 %4, 0\n\ts_cbranch_scc1 .Lskip%=\n\ts_mov_b64 exec, 0xff\n\t"
               "buffer_store_dwordx4 %0, %1, %2, %3 offen offset:%5 sc1\n\ts_mov_b64 exec, -1\n.Lskip%=:\n\ts_nop 0"
               ::"v"(v), "v"(c.vhld[0]), "s"(c.sw), "s"(c.fown), "s"(c.w), "i"(EV * 128) : "scc");
}
template <int EV> DI void issue_poll(Ctx& c, unsigned& pv, unsigned voff) {
  asm volatile("s_cmp_lg_u32 %3, 7\n\ts_cbranch_scc1 .Lskip%=\n\tbuffer_load_dword %0, %1, %2, 0 offen offset:%4 sc1\n.Lskip%=:"
               : "+v"(pv) : "v"(voff), "s"(c.sw), "s"(c.w), "i"(EV * 128) : "scc");
}
template <int EV, int N> DI void check_poll(Ctx& c, unsigned& pv, bool dest) {
  if (c.w == 7) {
    asm volatile("s_waitcnt vmcnt(%1)" : "+v"(pv) : "i"(N) : "memory");
    if (__builtin_expect(__builtin_amdgcn_ballot_w64(pv != c.tag) != 0, 0)) {
      const unsigned voff = poll_off(c, dest);
      for (unsigned spin = 0; spin < (1u << 16); ++spin) {
        unsigned v;
        asm volatile("s_sleep 1\n\tbuffer_load_dword %0, %1, %2, 0 offen offset:%3 sc1\n\ts_waitcnt vmcnt(0)" : "=&v"(v) : "v"(voff), "s"(c.sw), "i"(EV * 128) : "memory");
        if (__builtin_amdgcn_ballot_w64(v != c.tag) == 0) break;
      }
    }
  }
}
template <int P> DI void store_h(Ctx& c, const unsigned (&hv)[8], unsigned vo, unsigned so) {
  const u32x4 v = {hv[4 * P], hv[4 * P + 1], hv[4 * P + 2], hv[4 * P + 3]};
  asm volatile("buffer_store_dwordx4 %0, %1, %2, %3 offen sc1\n\ts_nop 1" ::"v"(v), "v"(vo), "s"(c.sw), "s"(so));
}
template <int N0, int N1> DI void wait_vm(const Ctx&) {
  static_assert(N1 <= N0 && N1 >= 0 && N0 < 64, "vmcnt");
  asm volatile("s_waitcnt vmcnt(%0)" ::"i"(N1) : "memory");
}

template <int H> DI void set_bases(Ctx& c) {
  constexpr unsigned d = (H & 3) ? SLOT : 0u - 3u * SLOT;
  if constexpr (H == 6) {
    const unsigned l = lane_id();
    c.wb = opq(2u * SLOT + (l >> 2) * 288u + 8u * (l & 3u) + c.hf * 18432u);
  } else if constexpr (H > 0 && H < 14) c.wb = opq(c.wb + d);
  if constexpr (H < 6) c.wb2 = opq(c.wb ^ 32u);
}
template <int H, int S> DI void rd(Ctx& c) {
  constexpr int tau = 18 * (H - 13) + S;
  constexpr unsigned o = H < 6 ? 4096u * (S >> 1) : H < 13 ? 9216u * (S / 9) + 32u * (S % 9) : ((tau >> 1) & 1) * SLOT + (tau & 1) * 9216u + 32u * (tau >> 2);
  constexpr unsigned e = H < 6 ? 512u : 4608u;
  const unsigned b = (H < 6 && (S & 1)) ? c.wb2 : c.wb;
  c.F[(H * 18 + S) & 3] = cat(trr(b + o), trr(b + o + e));
}
template <int H, int S> DI void mm(Ctx& c) {
  const bf16x8 a = c.F[(H * 18 + S) & 3];
  if constexpr (H < 6) {
    constexpr int B = ((H >> 1) + 1) & 1;
    constexpr int P = 9 * (H & 1) + (S >> 1);
    mfma16(c.acc[B][S & 1], a, c.X[2 * P]);
    mfma16(c.acc[B][2 + (S & 1)], a, c.X[2 * P + 1]);
  } else {
    constexpr int tau = 18 * (H - 13) + S;
    constexpr int N = H < 13 ? 2 * (H - 6) + S / 9 : 14 + (tau & 3), dt = H < 13 ? S % 9 : tau >> 2;
    u32x4 b0 = c.Hb[N & 7][0], b1 = c.Hb[N & 7][1];
    if constexpr (own_unit(N)) {
      const unsigned(&o)[8] = c.own[pair_of(6 + N / 2)];
      b0 = (u32x4){o[0], o[1], o[2], o[3]}; b1 = (u32x4){o[4], o[5], o[6], o[7]};
    }
    if constexpr (N == 0) {
      mfma16z(c.acc2[0][dt], a, __builtin_bit_cast(bf16x8, b0));
      mfma16z(c.acc2[1][dt], a, __builtin_bit_cast(bf16x8, b1));
    } else {
      mfma16(c.acc2[0][dt], a, __builtin_bit_cast(bf16x8, b0));
      mfma16(c.acc2[1][dt], a, __builtin_bit_cast(bf16x8, b1));
    }
  }
}
template <int Q> DI void bias_quad(f32x4 (&acc)[4], unsigned a) { acc[Q] = *LDSP(volatile f32x4, a + (Q & 1) * 64u); }
template <int E, int ST> DI void gelu_stage(float s, float& z, float& m0, unsigned (&hv)[8]) {
  if constexpr (ST == 0) { z = s * __builtin_fmaf(s, s, GAM); PIN(z); }
  if constexpr (ST == 1) { z = __builtin_amdgcn_exp2f(-z); PIN(z); }
  if constexpr (ST == 2) { z = __builtin_amdgcn_rcpf(1.0f + z); PIN(z); }
  if constexpr (ST == 3) {
    if constexpr ((E & 1) == 0) { m0 = s * z; PIN(m0); }
    else { float m = s * z, mm0 = m0; PIN(m); PIN(mm0); hv[E >> 1] = cvtpk(mm0, m); PIN(hv[E >> 1]); }
  }
}
constexpr int gstart(int e) { return 1 + (e * 29) / 15; }
constexpr int gelem(int g, int st) {
  for (int e = 0; e < 16; ++e) if (gstart(e) + st == g) return e;
  return -1;
}
template <int B, int G, int ST> DI void gelu_part(Ctx& c) {
  constexpr int e = gelem(G, ST);
  if constexpr (e >= 0) gelu_stage<e, ST>(c.acc[B & 1][e >> 2][e & 3], c.gz[e & 1], c.gm, c.own[B]);
}
template <int B, int G> DI void gelu_gap(Ctx& c) {
  gelu_part<B, G, 3>(c); gelu_part<B, G, 2>(c); gelu_part<B, G, 1>(c); gelu_part<B, G, 0>(c);
  if constexpr (B < 2) {
    if constexpr (G == 27) { bias_quad<0>(c.acc[B & 1], c.tb + (B + 2) * 256u); bias_quad<1>(c.acc[B & 1], c.tb + (B + 2) * 256u); }
    if constexpr (G == 31) bias_quad<2>(c.acc[B & 1], c.tb + (B + 2) * 256u);
    if constexpr (G == 33) bias_quad<3>(c.acc[B & 1], c.tb + (B + 2) * 256u);
  }
}

template <int H0, int K0, int H1, int K1> DI void wait_after(const Ctx& c) {
  wait_vm<between(H0, K0, H1, K1, 5), between(H0, K0, H1, K1, 4)>(c);
}
DI unsigned nosent(unsigned v) { return v < 0xfffffffeu ? v : 0xfffffffeu; }
template <int M, int ST> DI void comb(Ctx& c) {
  constexpr int NT = M < 4 ? 2 : 1;
  if constexpr (ST == 0) *LDSP(f32x4, c.xw + 2 * M * 1024u) = c.acc2[1][2 * M];
  if constexpr (ST == 1) {
    if constexpr (NT == 2) *LDSP(f32x4, c.xw + (2 * M + 1) * 1024u) = c.acc2[1][2 * M + 1];
    asm volatile("s_mov_b64 exec, 1\n\tds_write_b32 %0, %1\n\ts_mov_b64 exec, -1" ::"v"(c.vpg), "v"(M + 1u) : "memory");
  }
  if constexpr (ST == 2) c.pg = *LDSP(volatile unsigned, c.vpg ^ 16u);
  if constexpr (ST == 3) {
    if (__builtin_expect(rfl(c.pg) <= (unsigned)M, 0)) {
      for (unsigned spin = 0; spin < (1u << 20); ++spin)
        if (rfl(*LDSP(volatile unsigned, c.vpg ^ 16u)) > (unsigned)M) break;
    }
    asm volatile("" ::: "memory");
#pragma unroll
    for (int i = 0; i < NT; ++i) c.xg[i] = ldsf4(c.xr + (2 * M + i) * 1024u);
  }
  if constexpr (ST == 4) {
#pragma unroll
    for (int i = 0; i < NT; ++i)
#pragma unroll
      for (int k = 0; k < 4; ++k) { c.xg[i][k] += c.acc2[0][2 * M + i][k]; PIN(c.xg[i][k]); }
  }
  if constexpr (ST == 5) {
#pragma unroll
    for (int i = 0; i < 2 * NT; ++i) { c.pk[i] = nosent(cvtpk(c.xg[i >> 1][2 * (i & 1)], c.xg[i >> 1][2 * (i & 1) + 1])); }
    PIN(c.pk);
  }
  if constexpr (ST == 6) {
    if (c.t != c.j) {
      if constexpr (NT == 2)
        asm volatile("buffer_store_dwordx4 %0, %1, %2, %3 offen offset:%4 sc1\n\ts_nop 1" ::"v"(c.pk), "v"(c.vtch), "s"(c.sw), "s"(c.sp), "i"(M * 1024));
      else
        asm volatile("buffer_store_dwordx2 %0, %1, %2, %3 offen sc1" ::"v"((u32x2){c.pk[0], c.pk[1]}), "v"(c.vtch >> 1), "s"(c.sw), "s"(c.sp + 4096u));
    } else {
#pragma unroll
      for (int i = 0; i < NT; ++i) *LDSP(f32x4, c.xr + (2 * M + i) * 1024u) = c.xg[i];
    }
  }
}
template <int TAU, int ST, int OFF> DI void comb_at(Ctx& c) {
  if constexpr (TAU >= OFF && (TAU - OFF) % 8 == 0 && (TAU - OFF) / 8 < 4) comb<(TAU - OFF) / 8, ST>(c);
}
template <int H, int K> DI void side(Ctx& c) {
  if constexpr (poll_at(H, K)) {
    if constexpr (H == 11) issue_poll<4>(c, c.pollv, poll_off(c, true));
    else issue_poll<poll_ev(H)>(c, c.pollv, c.pref);
  }
  if constexpr (K == -1 && flag_at(H)) store_flag<flag_at(H)>(c);
  if constexpr (hload_at(H, K) >= 0) issue_hload<(hload_at(H, K) >> 1), (hload_at(H, K) & 1)>(c);
  if constexpr (mark_at(H, K)) {
    constexpr unsigned k = H == 4 ? 2u : K == 4 ? 0u : 1u;
    const u32x4 ones = {~0u, ~0u, ~0u, ~0u};
    const unsigned so = WS_P + (((c.r * 4u + c.q) * 4u + c.j) * 4u + ((c.j + 1u + k) & 3u)) * 9216u, vo = lane_id() * 16u;
    asm volatile("buffer_store_dwordx4 %0, %1, %2, %3 offen sc1\n\ts_cmp_lg_u32 %4, %5\n\ts_cbranch_scc1 .Lskip%=\n\t"
                 "buffer_store_dwordx4 %0, %1, %2, %6 offen sc1\n.Lskip%=:\n\ts_nop 1"
                 ::"v"(ones), "v"(vo), "s"(c.sw), "s"(so + c.w * 1024u), "s"(c.w), "i"(k), "s"(so + 8192u) : "scc");
  }
  if constexpr (dma_at(H, K) >= 0) issue_piece<(dma_at(H, K) >> 3), (dma_at(H, K) & 7)>(c);
  if constexpr ((H == 2 && K == 14) || ((H == 4 || H == 5) && K == 15)) {
    const unsigned a = L_PAT + (H == 4 ? 2304u : 0u) + lane_id() * 4u;
#pragma unroll
    for (int i = 0; i < 4; ++i) c.vpat[i] = *LDSP(unsigned, a + (c.hf * 5u + i) * 256u);
    c.vpat4 = *LDSP(unsigned, a + 1024u);
  }
  if constexpr (H == 12 && K == 7) {
    const unsigned l = lane_id();
    c.xw = c.hf * 3u * SLOT + c.t * 9216u + l * 16u;
    c.xr = (c.hf ^ 1u) * 3u * SLOT + c.t * 9216u + l * 16u;
    c.sp = WS_P + (((c.r * 4u + c.q) * 4u + c.t) * 4u + c.j) * 9216u + c.hf * 4608u;
    c.vpg = L_PROG + c.w * 4u; c.vtch = l * 16u;
    asm volatile("" : "+v"(c.xw), "+v"(c.xr), "+v"(c.vpg), "+v"(c.vtch), "+s"(c.sp));
  }
  if constexpr (H >= 13) {
    constexpr int tau = 18 * (H - 13) + K;
    comb_at<tau, 6, 17>(c); comb_at<tau, 5, 16>(c); comb_at<tau, 4, 15>(c); comb_at<tau, 3, 13>(c);
    comb_at<tau, 2, 12>(c); comb_at<tau, 1, 9>(c); comb_at<tau, 0, 6>(c);
  }
  if constexpr (H < 8) gelu_gap<(H >> 1), K + 18 * (H & 1)>(c);
  if constexpr (H < 2) {
    constexpr int G = K + 18 * H - 1;
    if constexpr (G >= 0 && G < 28) gelu_stage<G / 4, (G & 3)>(c.acc8[G / 16][(G / 4) & 3], c.gz8, c.gm8, c.hv8);
    if constexpr (G == 28) { gelu_stage<7, 0>(c.acc8[1][3], c.gz8, c.gm8, c.hv8); gelu_stage<7, 1>(c.acc8[1][3], c.gz8, c.gm8, c.hv8); }
    if constexpr (G == 29) { gelu_stage<7, 2>(c.acc8[1][3], c.gz8, c.gm8, c.hv8); gelu_stage<7, 3>(c.acc8[1][3], c.gz8, c.gm8, c.hv8); }
  }
  if constexpr (hst_at(H, K)) {
    if constexpr (K == 12) {
      const unsigned l = lane_id();
#pragma unroll
      for (int i = 0; i < 2; ++i) {
        auto rr = __builtin_amdgcn_permlane16_swap(c.hv8[i], c.hv8[2 + i], false, false);
        c.hv8[i] = rr[0]; c.hv8[2 + i] = rr[1];
      }
      store_h<0>(c, c.hv8, (((l >> 4) & 1u) ^ c.hf) * 1024u + ((l & 15u) + 16u * (l >> 5)) * 16u, c.hq[0] + 65536u - c.hf * 7680u);
    } else store_h<(K == 0 ? 0 : 1)>(c, c.own[H >> 1], c.vhld[K == 0 ? 0 : 1], c.hq[0] + (H >> 1) * 16384u);
  }
  if constexpr (pref_at(H, K))
    asm volatile("s_mov_b32 m0, %0\n\ts_nop 0\n\tbuffer_load_dword %1, %2, %3 offen lds" ::"s"(L_JUNK), "v"(c.vtch), "s"(c.s2), "s"((c.j * 1152u + H * 48u) * W2ROW));
}
constexpr int hpos(int n) {
  for (int h = 4; h < 14; ++h) for (int k = -2; k < 16; ++k) if (hload_at(h, k) == 2 * n + 1) return h * 32 + k + 2;
  return 0;
}
constexpr int cmax(int a, int b) { return a > b ? a : b; }
template <int H> DI void end_hs(Ctx& c) {
  if constexpr (H < 13) {
    constexpr int p0 = H == 0 ? -1 : H == 1 ? 8 : H == 12 ? 12 * 32 + 6 : (H - 2) * 32 + 17;
    constexpr int p1 = (H >= 5 && !own_unit(2 * (H - 5))) ? hpos(2 * (H - 5)) : 0, p3 = flag_at(H + 1) ? (H - 1) * 32 + 17 : 0;
    constexpr int p = H == 0 ? -1 : cmax(cmax(p0, p1), p3);
    if constexpr (H == 0) wait_after<0, -3, 0, 16>(c);
    else wait_after<p / 32, p % 32 - 2, H, 16>(c);
    if constexpr (poll_k(H) > -9) check_poll<poll_ev(H), between(H, poll_k(H), H, 16, 4)>(c, c.pollv, H == 11);
    BARRIER_LGKM();
  }
  if constexpr (H == 12) {
#pragma unroll
    for (int n = 15; n < 18; ++n) { PIN(c.Hb[n & 7][0]); PIN(c.Hb[n & 7][1]); }
  }
  if constexpr (H >= 5 && H < 12 && !own_unit(2 * (H - 5))) { PIN(c.Hb[(2 * (H - 5)) & 7][0]); PIN(c.Hb[(2 * (H - 5)) & 7][1]); }
  GAP();
}
template <int H> DI void mid_wait(Ctx& c) {
  if constexpr (H >= 6 && H < 13) {
    constexpr int p = hpos(2 * (H - 6) + 1);
    wait_after<p / 32, p % 32 - 2, H, 9>(c);
    PIN(c.Hb[(2 * (H - 6) + 1) & 7][0]); PIN(c.Hb[(2 * (H - 6) + 1) & 7][1]);
  }
}
template <int H, int K> DI void step(Ctx& c) {
  if constexpr (K == 9) mid_wait<H>(c);
  if constexpr (K == 10) asm volatile("s_setprio 0");
  mm<H, K>(c);
  if constexpr (K + 4 < 18) rd<H, K + 4>(c);
  else if constexpr (H == 13) rd<14, K - 14>(c);
  side<H, K>(c);
  GAP();
}
template <int H, int... Ks> DI void steps(Ctx& c, std::integer_sequence<int, Ks...>) { (step<H, Ks>(c), ...); }
DI void prio_young(const Ctx& c) {
  asm volatile("s_cmp_eq_u32 %0, 0\n\ts_cbranch_scc1 .Lskip%=\n\ts_setprio 1\n.Lskip%=:" ::"s"(c.hf) : "scc");
}
template <int H> DI void half_stage(Ctx& c) {
  if constexpr (H < 13) prio_young(c);
  set_bases<H>(c);
  if constexpr (H < 14) { rd<H, 0>(c); rd<H, 1>(c); }
  GAP();
  if constexpr (H > 0) mm<H - 1, 16>(c); else { mfma16(c.acc[0][0], c.F[2], c.X[34]); mfma16(c.acc[0][2], c.F[2], c.X[35]); }
  rd<H, 2>(c);
  side<H, -2>(c);
  GAP();
  if constexpr (H > 0) mm<H - 1, 17>(c); else { mfma16(c.acc[0][1], c.F[3], c.X[34]); mfma16(c.acc[0][3], c.F[3], c.X[35]); }
  rd<H, 3>(c);
  side<H, -1>(c);
  GAP();
  steps<H>(c, std::make_integer_sequence<int, 16>{});
  if constexpr (H == 14) { mm<14, 16>(c); mm<14, 17>(c); GAP(); asm volatile("s_nop 15" ::: "memory"); GAP(); }
  end_hs<H>(c);
}

struct B0 {
  unsigned vy, vw1, vc8, xa[4], l8, l80, lx, lw;
  bf16x8 G[2];
};
template <int S> DI void issue_y(Ctx& c, B0& b) {
  const unsigned dst = (S % 5) * SLICE + c.w * 1024u, so = WS_Y + c.r * 128u * XROW + S * 128u;
  asm volatile("s_mov_b32 m0, %0\n\ts_nop 0\n\tbuffer_load_dwordx4 %1, %2, %3 offen sc1 lds" ::"s"(dst), "v"(b.vy), "s"(c.sw), "s"(so));
  asm volatile("s_mov_b32 m0, %0\n\ts_nop 0\n\tbuffer_load_dwordx4 %1, %2, %3 offen sc1 lds" ::"s"(dst + 8192u), "v"(b.vy), "s"(c.sw), "s"(so + 64u * XROW));
}
template <int S> DI void issue_w(Ctx& c, B0& b) {
  const unsigned dst = (S % 5) * SLICE, kk = S * 64u;
  dma16(dst + c.w * 1024u + 16384u, b.vw1, c.s1, (kk + 8u * c.w) * W1ROW + c.freg2);
  asm volatile("s_cmp_lg_u32 %4, 0\n\ts_cbranch_scc1 .Lskip%=\n\ts_mov_b32 m0, %0\n\ts_nop 0\n\tbuffer_load_dwordx4 %1, %2, %3 offen lds\n.Lskip%=:"
               ::"s"(dst + c.t * 1024u + 24576u), "v"(b.vc8), "s"(c.s1), "s"((kk + 16u * c.t) * W1ROW + c.f82), "s"(c.hf) : "scc");
}
template <int S, int P> DI void rdx(Ctx& c, B0& b) {
  c.X[4 * S + 2 * P] = *LDSP(bf16x8, b.xa[2 * P]);
  c.X[4 * S + 2 * P + 1] = *LDSP(bf16x8, b.xa[2 * P + 1]);
}
template <int K> DI void rdf(Ctx& c) {
  const unsigned a = ((K & 1) ? c.wb2 : c.wb) + 4096u * (K >> 1);
  c.F[K] = cat(trr(a), trr(a + 512u));
}
template <int P> DI void rdg(B0& b) { b.G[P] = cat(trr(b.l8 + 2048u * P), trr(b.l8 + 2048u * P + 256u)); }
template <int S, int K> DI void mmA(Ctx& c) {
  mfma16(c.acc[0][K & 1], c.F[K], c.X[4 * S + (K & 2)]);
  mfma16(c.acc[0][2 + (K & 1)], c.F[K], c.X[4 * S + (K & 2) + 1]);
}
template <int S, int P> DI void mmB(Ctx& c, B0& b) {
  mfma16(c.acc8[0], b.G[P], c.X[4 * S + 2 * P]);
  mfma16(c.acc8[1], b.G[P], c.X[4 * S + 2 * P + 1]);
}
template <int S> DI void bases0(Ctx& c, B0& b) {
  constexpr unsigned base = (S % 5) * SLICE;
#pragma unroll
  for (int k = 0; k < 4; ++k) b.xa[k] = opq(base + (b.lx ^ (unsigned)((k >> 1) * 64 + (k & 1) * 2048)));
  c.wb = opq(base + 16384u + b.lw);
  c.wb2 = opq(c.wb ^ 32u);
  b.l8 = opq(base + 24576u + b.l80);
}
template <int S> DI void slice(Ctx& c, B0& b) {
  constexpr int n0 = 3, n = 4;
  constexpr int N = S == 0 ? 2 : S == 1 ? 4 : S == 2 ? 4 + n0 : S == 3 ? 2 + 2 * n0 : S <= 5 ? 3 * n0 : S == 6 ? 2 * n0 + 1 : S == 7 ? n0 + 1 : n + 1;
  asm volatile("s_waitcnt vmcnt(%0) lgkmcnt(0)\n\ts_barrier" ::"i"(N) : "memory");
  GAP();
  if constexpr (S > 0) mmA<S - 1, 2>(c);
  rdx<S, 0>(c, b); rdf<0>(c);
  GAP();
  if constexpr (S > 0) mmA<S - 1, 3>(c);
  rdf<1>(c); rdg<0>(b);
  GAP();
  if constexpr (S > 0) mmB<S - 1, 1>(c, b);
  rdx<S, 1>(c, b); rdf<2>(c);
  if constexpr (S <= 1) issue_y<2 * S + 2>(c, b);
  if constexpr (S >= 2 && S <= 4) issue_y<S + 4>(c, b);
  if constexpr (S == 5) {
    const unsigned l = lane_id(), m = c.w * 21u + (l < 21u ? l : 20u);
    asm volatile("s_mov_b32 m0, %0\n\ts_nop 0\n\tbuffer_load_dword %1, %2, %3 offen lds" ::"s"(L_JUNK),
                 "v"(((c.r * 4u + c.q) + 64u * (m < 162u ? m : 161u)) * 128u), "s"(c.s2), "s"(c.j * 1152u * W2ROW));
  }
  if constexpr (S == 7) issue_slot<0>(c);
  if constexpr (S == 8) issue_slot<1>(c);
  GAP();
  mmA<S, 0>(c);
  rdf<3>(c); rdg<1>(b);
  GAP();
  mmA<S, 1>(c);
  if constexpr (S <= 1) issue_y<2 * S + 3>(c, b);
  if constexpr (S >= 1 && S <= 4) issue_w<S + 4>(c, b);
  if constexpr (S == 5) {
    const unsigned m = c.w * 7u + (lane_id() & 7u), ln = ((c.r & 7u) * 4u + c.q) * 54u + (m < 54u ? m : 53u);
    const unsigned mem = ln / 432u, rem = ln - mem * 432u, row = rem / 9u;
    c.vtch = (288u * mem + row) * W2ROW + (rem - row * 9u) * 128u;
    c.pref = poll_off(c, false);
  }
  if constexpr (S == 6) {
    const unsigned v0 = b.vw1 ^ (c.hf << 5);
#pragma unroll
    for (int i = 0; i < 4; ++i) c.vpat[i] = (v0 ^ ((i & 1) << 5)) + i * (8u * W1ROW - 1024u);
    c.vpat4 = v0 + 32u * W1ROW;
    bias_quad<0>(c.acc[1], c.tb + 256u); bias_quad<1>(c.acc[1], c.tb + 256u); bias_quad<2>(c.acc[1], c.tb + 256u); bias_quad<3>(c.acc[1], c.tb + 256u);
  }
  if constexpr (S < 8) bases0<S + 1>(c, b);
  GAP();
  mmB<S, 0>(c, b);
  GAP();
}
DI float half_sum(float v) {
  v = dpp_add<0x121>(v); v = dpp_add<0x122>(v); v = dpp_add<0x124>(v); v = dpp_add<0x128>(v);
  auto a = __builtin_amdgcn_permlane16_swap(__float_as_uint(v), __float_as_uint(v), false, false);
  return __uint_as_float(a[0]) + __uint_as_float(a[1]);
}
DI void scalars(Ctx& c) {
  c.freg2 = (1152u * c.j + fstart(c.q, 0)) * 2u;
  c.f82 = (1152u * c.j + fstart(c.q, 8)) * 2u;
  c.fown = WS_FLAG + (((c.r * 4u + c.j) * 4u + c.q) << 10);
  c.sw1 = (72u * c.t + 40u * c.hf) * W1ROW + c.freg2;
  c.ldst = c.t * 9216u + c.hf * 5120u;
}
template <int HF> DI void lane_consts(Ctx& c, B0& b) {
  const unsigned l = threadIdx.x & 63u, g = l >> 4, qq = (l >> 2) & 3u, pp = l & 3u;
  b.vy = ((l >> 3) + 8u * c.w) * XROW + (((l & 7u) ^ ((g + 4u * c.w) & 7u)) << 4);
  b.lw = (8u * g + qq) * 128u + (((4u * HF + 2u * (g & 1u) + (pp >> 1)) ^ ((qq >> 1) << 2)) << 4) + 8u * (pp & 1u);
  const unsigned xrow = 32u * c.t + 16u * HF + (l & 15u);
  b.lx = xrow * 128u + ((g ^ ((xrow >> 1) & 7u)) << 4);
  b.l80 = (8u * g + qq) * 64u + ((HF ^ (g & 1u)) << 5) + 8u * pp;
  c.tb = L_B1 + HF * 128u + g * 16u;
  c.vhld[0] = l * 16u + HF * 1024u; c.vhld[1] = c.vhld[0] ^ 1024u;
  bases0<0>(c, b);
}
template <int HF> DI void prologue(Ctx& c, B0& b, const srd_t sx, const srd_t sb1, const srd_t sb2) {
  const unsigned l = threadIdx.x & 63u, g = l >> 4, pp = l & 3u, h = l >> 5;
  unsigned epoch;
  if constexpr (HF == 0) {
    const unsigned row = (c.r * 128u + (c.j * 4u + c.q) * 8u + 2u * c.t) * XROW, va = h * XROW + (l & 31u) * 16u, vb = h * XROW + 1024u + (l & 31u) * 4u;
    u32x4 xa, xc; unsigned xb;
    asm volatile("buffer_load_dwordx4 %0, %1, %2, %3 offen" : "=v"(xa) : "v"(va), "s"(sx), "s"(row));
    asm volatile("buffer_load_dwordx4 %0, %1, %2, %3 offen offset:512" : "=v"(xc) : "v"(va), "s"(sx), "s"(row));
    asm volatile("buffer_load_dword %0, %1, %2, %3 offen" : "=v"(xb) : "v"(vb), "s"(sx), "s"(row));
    asm volatile("buffer_load_dword %0, off, %1, %2 sc1" : "=v"(epoch) : "s"(c.sw), "s"(WS_EPOCH + c.r * 128u));
    GAP();
    scalars(c);
    b.vw1 = (l >> 3) * W1ROW + (((l & 7u) ^ ((g & 1u) << 2) ^ ((c.w & 1u) << 1)) << 4);
    b.vc8 = (l >> 2) * W1ROW + ((pp ^ (h << 1)) << 4);
    GAP();
    asm volatile("s_waitcnt vmcnt(1)" : "+v"(xa), "+v"(xb), "+v"(xc)::"memory");
    float v[18];
#pragma unroll
    for (int i = 0; i < 4; ++i) {
      v[2 * i] = __uint_as_float(xa[i] << 16); v[2 * i + 1] = __uint_as_float(xa[i] & 0xffff0000u);
      v[8 + 2 * i] = __uint_as_float(xc[i] << 16); v[9 + 2 * i] = __uint_as_float(xc[i] & 0xffff0000u);
    }
    v[16] = __uint_as_float(xb << 16); v[17] = __uint_as_float(xb & 0xffff0000u);
    float s0 = v[16], s1 = v[17];
#pragma unroll
    for (int i = 0; i < 8; ++i) { s0 += v[2 * i]; s1 += v[2 * i + 1]; }
    const float mean = half_sum(s0 + s1) * (1.0f / 576.0f);
#pragma unroll
    for (int i = 0; i < 18; ++i) v[i] -= mean;
    s0 = v[16] * v[16]; s1 = v[17] * v[17];
#pragma unroll
    for (int i = 0; i < 8; ++i) { s0 = __builtin_fmaf(v[2 * i], v[2 * i], s0); s1 = __builtin_fmaf(v[2 * i + 1], v[2 * i + 1], s1); }
    const float rs = KAP * __builtin_amdgcn_rsqf(half_sum(s0 + s1) * (1.0f / 576.0f) + 1e-5f);
    u32x4 ya, yc;
#pragma unroll
    for (int i = 0; i < 4; ++i) { ya[i] = cvtpk(v[2 * i] * rs, v[2 * i + 1] * rs); yc[i] = cvtpk(v[8 + 2 * i] * rs, v[9 + 2 * i] * rs); }
    const unsigned yb = cvtpk(v[16] * rs, v[17] * rs);
    asm volatile("buffer_store_dwordx4 %0, %1, %2, %3 offen sc1" ::"v"(ya), "v"(va), "s"(c.sw), "s"(WS_Y + row));
    asm volatile("buffer_store_dwordx4 %0, %1, %2, %3 offen offset:512 sc1" ::"v"(yc), "v"(va), "s"(c.sw), "s"(WS_Y + row));
    asm volatile("buffer_store_dword %0, %1, %2, %3 offen sc1\n\ts_nop 1" ::"v"(yb), "v"(vb), "s"(c.sw), "s"(WS_Y + row));
    GAP();

    lane_consts<HF>(c, b);
    GAP();
    asm volatile("s_waitcnt vmcnt(0)\n\ts_barrier" : "+v"(epoch)::"memory");
    c.tag = rfl(epoch) + 1u;
    store_flag<1>(c);
    issue_w<0>(c, b); issue_w<1>(c, b); issue_w<2>(c, b); issue_w<3>(c, b); issue_w<4>(c, b);
  } else {
    unsigned b1a, b1b, b2a;
    scalars(c);
    b.vw1 = (l >> 3) * W1ROW + (((l & 7u) ^ ((g & 1u) << 2) ^ ((c.w & 1u) << 1)) << 4);
    b.vc8 = (l >> 2) * W1ROW + ((pp ^ (h << 1)) << 4);
    asm volatile("buffer_load_dword %0, off, %1, %2 sc1" : "=v"(epoch) : "s"(c.sw), "s"(WS_EPOCH + c.r * 128u));
    asm volatile("buffer_load_ushort %0, %1, %2, %3 offen" : "=v"(b1a) : "v"((c.t * 64u + l) * 2u), "s"(sb1), "s"(c.freg2));
    asm volatile("buffer_load_ushort %0, %1, %2, %3 offen" : "=v"(b1b) : "v"((l & 31u) * 2u), "s"(sb1), "s"(c.f82));
    asm volatile("buffer_load_ushort %0, %1, %2, %3 offen" : "=v"(b2a) : "v"((c.t * 64u + l) * 2u), "s"(sb2), "s"(288u * c.q));
    issue_w<0>(c, b); issue_w<1>(c, b); issue_w<2>(c, b); issue_w<3>(c, b); issue_w<4>(c, b);

    lane_consts<HF>(c, b);
#pragma unroll
    for (int k = 0; k < 3; ++k) {
      const unsigned i = k < 2 ? 4u * k + c.t : 8u, nn = 64u * i + l, rho = nn / 18u, chn = nn - rho * 18u;
      const unsigned rel = i < 5u ? i : i - 5u, o = chn * 16u - (rel < 4u ? rel * 1024u : 0u);
      *LDSP(unsigned, L_PAT + i * 256u + l * 4u) = rho * W2ROW + o;
      *LDSP(unsigned, L_PAT + 2304u + i * 256u + l * 4u) = ((rho & 3u) | ((rho & 12u) << 1) | ((rho & 16u) >> 2)) * W2ROW + o;
    }
    asm volatile("s_waitcnt vmcnt(5)" : "+v"(b1a), "+v"(b1b), "+v"(b2a), "+v"(epoch)::"memory");
    c.tag = rfl(epoch) + 1u;
    *LDSP(float, L_B1 + (c.t * 64u + l) * 4u) = __uint_as_float(b1a << 16) * KAP;
    *LDSP(float, L_B1 + 1024u + (l & 31u) * 4u) = __uint_as_float(b1b << 16) * KAP;
    *LDSP(float, L_B2 + (c.t * 64u + l) * 4u) = __uint_as_float(b2a << 16);
    *LDSP(unsigned, L_PROG + (l & 7u) * 4u) = 0u;
    asm volatile("s_barrier" ::: "memory");
    if (c.t < 3) {
      const unsigned uln = (c.r * 4u + c.q) + 64u * (c.t * 54u + (l < 54u ? l : 53u)), rw = uln / 18u;
      asm volatile("s_mov_b32 m0, %0\n\ts_nop 0\n\tbuffer_load_dword %1, %2, %3 offen lds" ::"s"(L_JUNK),
                   "v"(rw * W1ROW + (uln - rw * 18u) * 128u), "s"(c.s1), "s"(c.j * 2304u));
    }
    asm volatile("mixer_code_end:");
    if (c.w == 7) {
      const unsigned voff = WS_FLAG + 128u + ((c.r * 16u + (l & 15u)) << 10);
      __builtin_amdgcn_s_sleep(10);
      for (unsigned spin = 0; spin < (1u << 16); ++spin) {
        unsigned pv;
        asm volatile("s_sleep 1\n\tbuffer_load_dword %0, %1, %2, 0 offen sc1\n\ts_waitcnt vmcnt(0)" : "=&v"(pv) : "v"(voff), "s"(c.sw) : "memory");
        if (__builtin_amdgcn_ballot_w64(pv != c.tag) == 0) break;
      }
    }
  }
}
DI void block0(Ctx& c, B0& b) {
  BARRIER_LGKM();
  issue_y<0>(c, b); issue_y<1>(c, b);
  bias_quad<0>(c.acc[0], c.tb); bias_quad<1>(c.acc[0], c.tb); bias_quad<2>(c.acc[0], c.tb); bias_quad<3>(c.acc[0], c.tb);
  c.acc8[0] = *LDSP(volatile f32x4, c.tb + 1024u - c.hf * 64u); c.acc8[1] = *LDSP(volatile f32x4, c.tb + 1024u - c.hf * 64u);
  slice<0>(c, b); slice<1>(c, b); slice<2>(c, b); slice<3>(c, b); slice<4>(c, b);
  slice<5>(c, b); slice<6>(c, b); slice<7>(c, b); slice<8>(c, b);
  mmB<8, 1>(c, b);
  c.wb = opq(b.lw);
  asm volatile("s_waitcnt vmcnt(%0) lgkmcnt(0)\n\ts_barrier" ::"i"(4) : "memory");
}

__global__ __launch_bounds__(512, 2) void mixer_mlp_kernel(const void* __restrict__ x, const void* __restrict__ w1, const void* __restrict__ b1,
                                                           const void* __restrict__ w2, const void* __restrict__ b2, void* __restrict__ out,
                                                           void* __restrict__ ws) {
  Ctx c;
  {
    unsigned long long pc; unsigned off;
    asm volatile("s_getpc_b64 %0\nmixer_code_begin:\n\tv_lshlrev_b32 %1, 7, %2\n\tv_cmp_lt_u32 vcc, 0x7fff, %1\n\ts_cbranch_vccz .Lskip%=\n\t"
                 "v_and_b32 %1, 0x7fff, %1\n\tv_min_u32 %1, mixer_code_end-mixer_code_begin-4, %1\n\ts_mov_b32 m0, %3\n\ts_nop 0\n\t"
                 "global_load_lds_dword %1, %0\n.Lskip%=:"
                 : "=&s"(pc), "=&v"(off) : "v"(threadIdx.x), "i"(L_JUNK) : "vcc");
  }
  const unsigned blk = blockIdx.x, xg = blk & 7u, idx = blk >> 3;
  c.r = 8u * (xg >> 2) + (idx >> 2); c.j = xg & 3u; c.q = idx & 3u;
  c.w = rfl(threadIdx.x >> 6); c.t = c.w & 3u; c.hf = c.w >> 2;

  srd_t sx = __builtin_amdgcn_make_buffer_rsrc((void*)x, 0, S_ * D_ * 2, 0x00020000);
  asm volatile("" : "+s"(sx));
  c.s1 = __builtin_amdgcn_make_buffer_rsrc((void*)w1, 0, D_ * F_ * 2, 0x00020000);
  c.s2 = __builtin_amdgcn_make_buffer_rsrc((void*)w2, 0, D_ * F_ * 2, 0x00020000);
  c.sw = __builtin_amdgcn_make_buffer_rsrc(ws, 0, WS_USED, 0x00020000);
  const srd_t sb2 = __builtin_amdgcn_make_buffer_rsrc((void*)b2, 0, D_ * 2, 0x00020000);
  const srd_t sb1 = __builtin_amdgcn_make_buffer_rsrc((void*)b1, 0, F_ * 2, 0x00020000);
  B0 b0;
  if (__builtin_expect(c.hf == 0, 1)) prologue<0>(c, b0, sx, sb1, sb2); else prologue<1>(c, b0, sx, sb1, sb2);
  scalars(c);
  c.pollv = 0u;
  asm volatile("" : "+s"(c.sw1), "+s"(c.ldst), "+s"(c.freg2), "+s"(c.f82), "+s"(c.fown), "+s"(c.r), "+s"(c.w), "+s"(c.t));
  block0(c, b0);
  {
    const unsigned hb = WS_H + ((c.r * 4u + c.j) * 144u + c.t) * 2048u;
#pragma unroll
    for (int d = 0; d < 4; ++d) c.hq[d] = hb + ((c.q + d) & 3u) * 73728u + c.hf * 8192u;
#pragma unroll
    for (int e = 0; e < 2; ++e) {
      c.h8[e] = hb + ((c.q + 2u * c.hf + e) & 3u) * 73728u + 65536u;
      c.sw2[e] = (1152u * c.j + fstart((c.q + 2u * e + (c.t & 1u)) & 3u, c.t >> 1)) * W2ROW + 288u * c.q;
    }
    c.sw28 = (1152u * c.j + fstart((c.q + c.t) & 3u, 8)) * W2ROW + 288u * c.q;
  }
  asm volatile("" : "+s"(c.sw1), "+s"(c.ldst), "+s"(c.hq[0]), "+s"(c.hq[1]), "+s"(c.hq[2]), "+s"(c.hq[3]), "+s"(c.h8[0]), "+s"(c.h8[1]),
               "+s"(c.sw2[0]), "+s"(c.sw2[1]), "+s"(c.sw28));

  half_stage<0>(c); half_stage<1>(c); half_stage<2>(c); half_stage<3>(c); half_stage<4>(c);
  half_stage<5>(c); half_stage<6>(c); half_stage<7>(c); half_stage<8>(c); half_stage<9>(c);
  half_stage<10>(c); half_stage<11>(c); half_stage<12>(c); half_stage<13>(c); half_stage<14>(c);

  const unsigned l = lane_id();
  comb<4, 0>(c); comb<4, 1>(c);
  {
    for (unsigned spin = 0; spin < (1u << 20); ++spin)
      if (rfl(*LDSP(volatile unsigned, c.vpg ^ 16u)) > 4u) break;
    asm volatile("" ::: "memory");
    f32x4 g8 = ldsf4(c.xr + 8192u);
    c.xg[0] = ldsf4(c.xr + 6144u); c.xg[1] = ldsf4(c.xr + 7168u);
    comb<3, 4>(c); comb<3, 5>(c); comb<3, 6>(c);
    c.xg[0] = g8;
    comb<4, 4>(c); comb<4, 5>(c); comb<4, 6>(c);
  }
  BARRIER_LGKM();

  {
    const unsigned ba = L_B2 + (l >> 4) * 16u;
    const f32x4 bA = ldsf4(ba + c.t * 128u), bB = ldsf4(ba + c.t * 128u + 64u), bC = ldsf4(ba + 512u);
    const unsigned sp = WS_P + ((c.r * 4u + c.q) * 4u + c.j) * 4u * 9216u + c.hf * 4608u;
    const unsigned s0 = sp + ((c.j + 1u) & 3u) * 9216u, s1 = sp + ((c.j + 2u) & 3u) * 9216u, s2 = sp + ((c.j + 3u) & 3u) * 9216u;
    const unsigned va = l * 16u + c.t * 1024u, vb = 4096u + l * 8u, oa = (c.hf ^ 1u) * 3u * SLOT + c.j * 9216u + l * 16u;
    u32x4 pa[3]; u32x2 pb[3];
    f32x4 o0 = ldsf4(oa + c.t * 2048u), o1 = ldsf4(oa + c.t * 2048u + 1024u), o2 = ldsf4(oa + 8192u);
    if (c.t == 3) __builtin_amdgcn_s_sleep(8);
    for (unsigned spin = 0; spin < (1u << 16); ++spin) {
      asm volatile("buffer_load_dwordx4 %0, %6, %8, %9 offen sc1\n\tbuffer_load_dwordx4 %1, %6, %8, %10 offen sc1\n\t"
                   "buffer_load_dwordx4 %2, %6, %8, %11 offen sc1\n\t"
                   "s_cmp_lg_u32 %12, 3\n\ts_cbranch_scc1 .Lskip%=\n\t"
                   "buffer_load_dwordx2 %3, %7, %8, %9 offen sc1\n\tbuffer_load_dwordx2 %4, %7, %8, %10 offen sc1\n\t"
                   "buffer_load_dwordx2 %5, %7, %8, %11 offen sc1\n.Lskip%=:\n\ts_waitcnt vmcnt(0)"
                   : "=&v"(pa[0]), "=&v"(pa[1]), "=&v"(pa[2]), "=&v"(pb[0]), "=&v"(pb[1]), "=&v"(pb[2])
                   : "v"(va), "v"(vb), "s"(c.sw), "s"(s0), "s"(s1), "s"(s2), "s"(c.t) : "memory", "scc");
      unsigned mx = 0;
#pragma unroll
      for (int k = 0; k < 3; ++k) {
#pragma unroll
        for (int i = 0; i < 4; ++i) mx = mx > pa[k][i] ? mx : pa[k][i];
        if (c.t == 3) { mx = mx > pb[k][0] ? mx : pb[k][0]; mx = mx > pb[k][1] ? mx : pb[k][1]; }
      }
      if (!__any(mx == ~0u)) break;
      __builtin_amdgcn_s_sleep(2);
    }

#pragma unroll
    for (int k = 0; k < 3; ++k)
#pragma unroll
      for (int i = 0; i < 2; ++i) {
        o0[2 * i] += __uint_as_float(pa[k][i] << 16); o0[2 * i + 1] += __uint_as_float(pa[k][i] & 0xffff0000u);
        o1[2 * i] += __uint_as_float(pa[k][2 + i] << 16); o1[2 * i + 1] += __uint_as_float(pa[k][2 + i] & 0xffff0000u);
      }
    unsigned short* op = (unsigned short*)out + (size_t)(128u * c.r + 32u * c.j + 16u * c.hf + (l & 15u)) * D_ + 144u * c.q;
    const unsigned e0 = cvtpk(__builtin_fmaf(o0[0], RKAP, bA[0]), __builtin_fmaf(o0[1], RKAP, bA[1]));
    const unsigned e1 = cvtpk(__builtin_fmaf(o0[2], RKAP, bA[2]), __builtin_fmaf(o0[3], RKAP, bA[3]));
    const unsigned f0 = cvtpk(__builtin_fmaf(o1[0], RKAP, bB[0]), __builtin_fmaf(o1[1], RKAP, bB[1]));
    const unsigned f1 = cvtpk(__builtin_fmaf(o1[2], RKAP, bB[2]), __builtin_fmaf(o1[3], RKAP, bB[3]));
    auto r0 = __builtin_amdgcn_permlane16_swap(e0, f0, false, false);
    auto r1 = __builtin_amdgcn_permlane16_swap(e1, f1, false, false);
    *(u32x4*)(op + 32u * c.t + 16u * ((l >> 4) & 1u) + 8u * (l >> 5)) = (u32x4){r0[0], r1[0], r0[1], r1[1]};
    if (c.t == 3) {
#pragma unroll
      for (int k = 0; k < 3; ++k)
#pragma unroll
        for (int i = 0; i < 2; ++i) { o2[2 * i] += __uint_as_float(pb[k][i] << 16); o2[2 * i + 1] += __uint_as_float(pb[k][i] & 0xffff0000u); }
      *(u32x2*)(op + 128u + 4u * (l >> 4)) =
          (u32x2){cvtpk(__builtin_fmaf(o2[0], RKAP, bC[0]), __builtin_fmaf(o2[1], RKAP, bC[1])),
                  cvtpk(__builtin_fmaf(o2[2], RKAP, bC[2]), __builtin_fmaf(o2[3], RKAP, bC[3]))};
    }
    if (c.w == 0 && c.j == 0 && c.q == 0 && l == 0)
      asm volatile("buffer_store_dword %0, off, %1, %2 sc1" ::"v"(c.tag), "s"(c.sw), "s"(WS_EPOCH + c.r * 128u));
  }
}

extern "C" void kernel_launch(void* const* d_in, const int* in_sizes, int n_in, void* d_out, int out_size, void* d_ws, size_t ws_size,
                              hipStream_t stream) {
  (void)in_sizes; (void)n_in; (void)out_size; (void)ws_size;
  (void)hipFuncSetAttribute((const void*)mixer_mlp_kernel, hipFuncAttributeMaxDynamicSharedMemorySize, LDS_BYTES);
  mixer_mlp_kernel<<<256, 512, LDS_BYTES, stream>>>(d_in[0], d_in[1], d_in[2], d_in[3], d_in[4], d_out, d_ws);
}
```
